```python
import torch

def _softmax(x, axis=-1):
    return torch.softmax(x, dim=axis)

def _gelu(x):
    return torch.nn.functional.gelu(x, approximate='tanh')

def _silu(x):
    return torch.nn.functional.silu(x)

def _sigmoid(x):
    return torch.sigmoid(x)

def _layernorm(x, axis=-1, eps=1e-05):
    m = x.mean(dim=axis, keepdim=True)
    v = x.var(dim=axis, keepdim=True, unbiased=False)
    return (x - m) / torch.sqrt(v + eps)

def _scatter_add(tgt, idx, src, axis=0):
    return tgt.clone().index_add_(axis, idx, src)

def _segment_sum(src, seg, n):
    out = torch.zeros((n,) + src.shape[1:], dtype=src.dtype)
    return out.index_add_(0, seg, src)

def _pad(x, pw):
    flat = tuple((v for pair in reversed(pw) for v in pair))
    return torch.nn.functional.pad(x, flat)
kernel_name = 'recurrent_scan__sliding_local_dilated__mixer_gmlp__prompt'

def setup_inputs():

    _w = lambda a: (lambda t: t.to(torch.bfloat16) if t.is_floating_point() else t)(torch.as_tensor(a))

    def _A_setup():
        B, S, D = (1, 2048, 576)
        return {'x': _w((lambda *s: torch.randn(*s, dtype=torch.bfloat16) * 0.05)(B, S, D)), 'wqkv': _w((lambda *s: torch.randn(*s, dtype=torch.bfloat16) * 0.05)(D, 3 * D)), 'wo': _w((lambda *s: torch.randn(*s, dtype=torch.bfloat16) * 0.05)(D, D))}
    _A_in = _A_setup()

    def _B_setup():
        B, S, D, F = (1, 2048, 576, 4608)
        Fh = F // 2
        return {'x': _w((lambda *s: torch.randn(*s, dtype=torch.bfloat16) * 0.05)(B, S, D)), 'w_in': _w((lambda *s: torch.randn(*s, dtype=torch.bfloat16) * 0.8)(D, 2 * Fh)), 'w_sp': _w(0.01 * (lambda *s: torch.randn(*s, dtype=torch.bfloat16) * 0.8)(S, S)), 'b_sp': _w(1.0 + 0.01 * (lambda *s: torch.randn(*s, dtype=torch.bfloat16) * 0.05)(S)), 'w_out': _w((lambda *s: torch.randn(*s, dtype=torch.bfloat16) * 0.05)(Fh, D))}
    _B_in = _B_setup()
    return {'A_x': _A_in['x'], 'A_wqkv': _A_in['wqkv'], 'A_wo': _A_in['wo'], 'B_x': _B_in['x'], 'B_w_in': _B_in['w_in'], 'B_w_sp': _B_in['w_sp'], 'B_b_sp': _B_in['b_sp'], 'B_w_out': _B_in['w_out']}

def reference(A_x, A_wqkv, A_wo, B_x, B_w_in, B_w_sp, B_b_sp, B_w_out):

    def _A_ref(x, wqkv, wo):
        B, S, D = x.shape
        H = 32
        Dh = D // H
        W, dil = (8, 2)
        qkv = (lambda x, *d: x.permute(*d))((x @ wqkv).reshape(B, S, 3, H, Dh), 2, 0, 3, 1, 4)
        q, k, v = (qkv[0], qkv[1], qkv[2])
        scale = Dh ** (-0.5)
        pos = torch.arange(S)
        off = torch.arange(W) * dil
        idx = pos[:, None] - off[None, :]
        mask = idx >= 0
        idx_c = torch.where(mask, idx, 0).reshape(-1)
        kg = k[:, :, idx_c, :].reshape(B, H, S, W, Dh)
        vg = v[:, :, idx_c, :].reshape(B, H, S, W, Dh)
        logits = torch.einsum('bhsd,bhswd->bhsw', q, kg) * scale
        logits = torch.where(mask[None, None, :, :], logits, -1000000000.0)
        attn = _softmax(logits, axis=-1)
        o = torch.einsum('bhsw,bhswd->bhsd', attn, vg)
        return (lambda x, *d: x.permute(*d))(o, 0, 2, 1, 3).reshape(B, S, D) @ wo

    def _B_ref(x, w_in, w_sp, b_sp, w_out):
        B, S, D = x.shape
        F = w_in.shape[-1]
        Fh = F // 2
        z = _gelu(torch.einsum('bsd,df->bsf', x, w_in))
        z1 = z[..., :Fh].clone()
        z2 = _layernorm(z[..., Fh:], axis=-1)
        g = torch.einsum('ts,bsf->btf', w_sp, z2) + b_sp[None, :, None]
        return torch.einsum('bsf,fd->bsd', z1 * g, w_out)
    x = A_x
    xn = _layernorm(x, axis=-1)
    s = _A_ref(xn, A_wqkv, A_wo)
    g = _silu(_B_ref(xn, B_w_in, B_w_sp, B_b_sp, B_w_out))
    return x + s * g
```

```cpp
#include <hip/hip_runtime.h>
#include <hip/hip_bf16.h>
#include <stdint.h>

typedef unsigned short u16;
__device__ __forceinline__ float bf2f(u16 v){ return __uint_as_float(((unsigned)v)<<16); }
__device__ __forceinline__ u16 f2bf(float f){ __hip_bfloat16 b = __float2bfloat16(f); return *(u16*)&b; }

constexpr int S=2048, D=576, F=4608, FH=2304, H=32, DH=18;

__global__ void k_ln(const u16* x, u16* xn){
  int row = blockIdx.x; int lane = threadIdx.x;
  float v[9]; float s=0;
  for(int i=0;i<9;i++){ v[i]=bf2f(x[row*D+lane+64*i]); s+=v[i]; }
  for(int o=32;o;o>>=1) s+=__shfl_xor(s,o);
  float m=s/D; float q=0;
  for(int i=0;i<9;i++){ float d=v[i]-m; q+=d*d; }
  for(int o=32;o;o>>=1) q+=__shfl_xor(q,o);
  float r=1.0f/sqrtf(q/D+1e-5f);
  for(int i=0;i<9;i++) xn[row*D+lane+64*i]=f2bf((v[i]-m)*r);
}

template<int EPI>
__global__ void k_gemm(const u16* A,int lda,const u16* B,int ldb,void* C,int ldc,int M,int N,int K){
  __shared__ float sa[32][33], sb[32][33];
  int tx=threadIdx.x&31, ty=threadIdx.x>>5;
  int r0=blockIdx.y*32, c0=blockIdx.x*32;
  float acc[4]={0,0,0,0};
  for(int k0=0;k0<K;k0+=32){
    for(int i=0;i<4;i++){
      int r=ty*4+i;
      sa[r][tx]=bf2f(A[(size_t)(r0+r)*lda+k0+tx]);
      int c=c0+tx;
      sb[r][tx]= c<N ? bf2f(B[(size_t)(k0+r)*ldb+c]) : 0.f;
    }
    __syncthreads();
    for(int k=0;k<32;k++){
      float b=sb[k][tx];
      for(int i=0;i<4;i++) acc[i]+=sa[ty*4+i][k]*b;
    }
    __syncthreads();
  }
  int c=c0+tx; if(c>=N) return;
  for(int i=0;i<4;i++){
    int r=r0+ty*4+i; float v=acc[i];
    if(EPI==1){ float u=0.7978845608028654f*(v+0.044715f*v*v*v); v=0.5f*v*(1.f+tanhf(u)); }
    if(EPI==2) ((float*)C)[(size_t)r*ldc+c]=v; else ((u16*)C)[(size_t)r*ldc+c]=f2bf(v);
  }
}

__global__ void k_attn(const u16* qkv,u16* o){
  int t=blockIdx.x; int h=threadIdx.x; if(h>=H) return;
  float q[DH]; for(int d=0;d<DH;d++) q[d]=bf2f(qkv[(size_t)t*3*D+h*DH+d]);
  float lg[8]; float mx=-3e38f;
  for(int w=0;w<8;w++){
    int s=t-2*w;
    if(s>=0){ float a=0; for(int d=0;d<DH;d++) a+=q[d]*bf2f(qkv[(size_t)s*3*D+D+h*DH+d]); lg[w]=a*0.23570226039551584f; }
    else lg[w]=-1e9f;
    mx=fmaxf(mx,lg[w]);
  }
  float sum=0; for(int w=0;w<8;w++){ lg[w]=expf(lg[w]-mx); sum+=lg[w]; }
  float acc[DH]; for(int d=0;d<DH;d++) acc[d]=0;
  for(int w=0;w<8;w++){
    int s=t-2*w; if(s<0) s=0;
    float p=lg[w]/sum;
    for(int d=0;d<DH;d++) acc[d]+=p*bf2f(qkv[(size_t)s*3*D+2*D+h*DH+d]);
  }
  for(int d=0;d<DH;d++) o[(size_t)t*D+h*DH+d]=f2bf(acc[d]);
}

__global__ void k_ln2(const u16* z,u16* z2n){
  int row=blockIdx.x; int lane=threadIdx.x;
  float v[36]; float s=0;
  for(int i=0;i<36;i++){ v[i]=bf2f(z[(size_t)row*F+FH+lane+64*i]); s+=v[i]; }
  for(int o=32;o;o>>=1) s+=__shfl_xor(s,o);
  float m=s/FH; float q=0;
  for(int i=0;i<36;i++){ float d=v[i]-m; q+=d*d; }
  for(int o=32;o;o>>=1) q+=__shfl_xor(q,o);
  float r=1.0f/sqrtf(q/FH+1e-5f);
  for(int i=0;i<36;i++) z2n[(size_t)row*FH+lane+64*i]=f2bf((v[i]-m)*r);
}

__global__ void k_h(const float* g,const u16* z,const u16* bsp,u16* h){
  int i=blockIdx.x*256+threadIdx.x; int t=i/FH, f=i%FH;
  float gv=g[i]+bf2f(bsp[t]);
  h[i]=f2bf(bf2f(z[(size_t)t*F+f])*gv);
}

__global__ void k_final(const u16* x,const float* s,const float* g,u16* out){
  int i=blockIdx.x*256+threadIdx.x;
  float gv=g[i]; float sg=gv/(1.f+expf(-gv));
  out[i]=f2bf(bf2f(x[i])+s[i]*sg);
}

extern "C" void kernel_launch(void* const* d_in, const int* in_sizes, int n_in,
                              void* d_out, int out_size, void* d_ws, size_t ws_size,
                              hipStream_t stream) {
  const u16* x=(const u16*)d_in[0]; const u16* wqkv=(const u16*)d_in[1]; const u16* wo=(const u16*)d_in[2];
  const u16* w_in=(const u16*)d_in[4]; const u16* w_sp=(const u16*)d_in[5]; const u16* b_sp=(const u16*)d_in[6]; const u16* w_out=(const u16*)d_in[7];
  char* ws=(char*)d_ws;
  u16* xn=(u16*)ws; ws+=(size_t)S*D*2;
  u16* qkv=(u16*)ws; ws+=(size_t)S*3*D*2;
  u16* o=(u16*)ws; ws+=(size_t)S*D*2;
  u16* z=(u16*)ws; ws+=(size_t)S*F*2;
  u16* z2n=(u16*)ws; ws+=(size_t)S*FH*2;
  u16* h=(u16*)ws; ws+=(size_t)S*FH*2;
  float* g=(float*)ws; ws+=(size_t)S*FH*4;
  float* sbuf=(float*)ws; ws+=(size_t)S*D*4;
  float* gb=(float*)ws; ws+=(size_t)S*D*4;
  k_ln<<<S,64,0,stream>>>(x,xn);
  k_gemm<0><<<dim3(3*D/32,S/32),256,0,stream>>>(xn,D,wqkv,3*D,qkv,3*D,S,3*D,D);
  k_attn<<<S,64,0,stream>>>(qkv,o);
  k_gemm<2><<<dim3(D/32,S/32),256,0,stream>>>(o,D,wo,D,sbuf,D,S,D,D);
  k_gemm<1><<<dim3(F/32,S/32),256,0,stream>>>(xn,D,w_in,F,z,F,S,F,D);
  k_ln2<<<S,64,0,stream>>>(z,z2n);
  k_gemm<2><<<dim3(FH/32,S/32),256,0,stream>>>(w_sp,S,z2n,FH,g,FH,S,FH,S);
  k_h<<<S*FH/256,256,0,stream>>>(g,z,b_sp,h);
  k_gemm<2><<<dim3(D/32,S/32),256,0,stream>>>(h,FH,w_out,D,gb,D,S,D,FH);
  k_final<<<S*D/256,256,0,stream>>>(x,sbuf,gb,(u16*)d_out);
}
```

```cpp
#include <hip/hip_runtime.h>
#include <hip/hip_bf16.h>
#include <stdint.h>

typedef unsigned short u16;
typedef short bf16x8 __attribute__((ext_vector_type(8)));
typedef short s16x4 __attribute__((ext_vector_type(4)));
typedef float f32x4 __attribute__((ext_vector_type(4)));
typedef float f32x2 __attribute__((ext_vector_type(2)));
typedef unsigned u32x4 __attribute__((ext_vector_type(4)));
typedef unsigned u32x2 __attribute__((ext_vector_type(2)));
typedef __bf16 bf16x2_t __attribute__((ext_vector_type(2)));
#define LAS __attribute__((address_space(3)))
#define GAS __attribute__((address_space(1)))
typedef LAS void* lds_vp;
typedef __amdgpu_buffer_rsrc_t rsrc_t;

constexpr int S_ = 2048, D_ = 576, F_ = 4608, FH = 2304, QW = 1728;
constexpr int SLOT = 36864, SPARE = 4 * SLOT, LDS_BYTES = 163840;
constexpr size_t WS_XN = 0;
constexpr size_t WS_Z2T = WS_XN + (size_t)S_ * D_ * 2;
constexpr size_t WS_QKV = WS_Z2T + (size_t)S_ * FH * 2;
constexpr size_t WS_HF = WS_QKV + (size_t)S_ * QW * 2;
constexpr size_t WS_OF = WS_HF + (size_t)S_ * FH * 2;
constexpr size_t WS_ST = WS_OF + (size_t)S_ * D_ * 2;
constexpr size_t WS_END = WS_ST + (size_t)S_ * 16 * 16;

__device__ __forceinline__ float bflo(unsigned v) { return __uint_as_float(v << 16); }
__device__ __forceinline__ float bfhi(unsigned v) { return __uint_as_float(v & 0xffff0000u); }
__device__ __forceinline__ float bf2f(u16 v) { return __uint_as_float(((unsigned)v) << 16); }
__device__ __forceinline__ unsigned pack_bf16(float lo, float hi) {
    f32x2 v = {lo, hi};
    return __builtin_bit_cast(unsigned, __builtin_convertvector(v, bf16x2_t));
}
#define BAR() do { asm volatile("" ::: "memory"); __builtin_amdgcn_s_barrier(); asm volatile("" ::: "memory"); } while (0)
#define WAIT_V(n) asm volatile("s_waitcnt vmcnt(%0)" ::"n"(n) : "memory")
#define WAIT_L0() asm volatile("s_waitcnt lgkmcnt(0)" ::: "memory")
#define SB0() __builtin_amdgcn_sched_barrier(0)

__device__ __forceinline__ rsrc_t mk_rsrc(const void* p, unsigned bytes) { return __builtin_amdgcn_make_buffer_rsrc((void*)p, 0, bytes, 0x00020000); }
__device__ __forceinline__ void dma16(rsrc_t r, LAS char* lds, unsigned voff, unsigned soff) {
    __builtin_amdgcn_raw_ptr_buffer_load_lds(r, (lds_vp)lds, 16, voff, soff, 0, 0);
}
__device__ __forceinline__ bf16x8 lds_b128(LAS char* p) { return *(LAS bf16x8*)p; }
__device__ __forceinline__ bf16x8 lds_tr(LAS char* p, int off1) {
    s16x4 a = __builtin_amdgcn_ds_read_tr16_b64_v4i16((LAS s16x4*)p);
    s16x4 b = __builtin_amdgcn_ds_read_tr16_b64_v4i16((LAS s16x4*)(p + off1));
    return __builtin_shufflevector(a, b, 0, 1, 2, 3, 4, 5, 6, 7);
}
#define MFMA(a, b, c) __builtin_amdgcn_mfma_f32_16x16x32_bf16(a, b, c, 0, 0, 0)
template <int CTRL> __device__ __forceinline__ float dpp_add(float x) {
    return x + __builtin_bit_cast(float, __builtin_amdgcn_update_dpp(0, __builtin_bit_cast(int, x), CTRL, 0xf, 0xf, false));
}
__device__ __forceinline__ float row16_sum(float x) { x = dpp_add<0x128>(x); x = dpp_add<0x124>(x); x = dpp_add<0x122>(x); x = dpp_add<0x121>(x); return x; }
__device__ __forceinline__ float gelu_f(float x) {
    const float t = x * (-2.3022081498f - 0.1029432397f * x * x);
    return x * __builtin_amdgcn_rcpf(1.0f + __builtin_amdgcn_exp2f(t));
}
__device__ __forceinline__ int swap23(int r) { return (r & 0x13) | ((r & 4) << 1) | ((r & 8) >> 1); }

struct Tile { int i, j; };
__device__ __forceinline__ Tile tile_of_block() {
    const int xcd = blockIdx.x & 7, loc = blockIdx.x >> 3;
    return Tile{(xcd & 1) * 8 + (loc & 7), (xcd >> 1) * 4 + (loc >> 3)};
}

__global__ __launch_bounds__(256) void k0_ln(const u16* __restrict__ x, u16* __restrict__ xn) {
    const int lane = threadIdx.x & 63, row = blockIdx.x * 32 + (threadIdx.x >> 3), c = lane & 7;
    const u32x4* src = (const u32x4*)(x + (size_t)row * D_);
    u32x4 v[9];
#pragma unroll
    for (int i = 0; i < 9; ++i) v[i] = src[c + 8 * i];
    float s = 0.f;
#pragma unroll
    for (int i = 0; i < 9; ++i) s += (bflo(v[i].x) + bfhi(v[i].x)) + (bflo(v[i].y) + bfhi(v[i].y)) + (bflo(v[i].z) + bfhi(v[i].z)) + (bflo(v[i].w) + bfhi(v[i].w));
    s += __shfl_xor(s, 1); s += __shfl_xor(s, 2); s += __shfl_xor(s, 4);
    const float m = s * (1.0f / D_);
    float q = 0.f;
#pragma unroll
    for (int i = 0; i < 9; ++i)
#pragma unroll
        for (int e = 0; e < 4; ++e) { const float a = bflo(v[i][e]) - m, b = bfhi(v[i][e]) - m; q += a * a + b * b; }
    q += __shfl_xor(q, 1); q += __shfl_xor(q, 2); q += __shfl_xor(q, 4);
    const float r = 1.0f / sqrtf(q * (1.0f / D_) + 1e-5f);
    u32x4* dst = (u32x4*)(xn + (size_t)row * D_);
#pragma unroll
    for (int i = 0; i < 9; ++i) {
        u32x4 o;
#pragma unroll
        for (int e = 0; e < 4; ++e) o[e] = pack_bf16((bflo(v[i][e]) - m) * r, (bfhi(v[i][e]) - m) * r);
        dst[c + 8 * i] = o;
    }
}

struct RowsImg {
    unsigned v0, v1;
    __device__ __forceinline__ void init(int lane, unsigned rs) {
        const int r8 = lane >> 3, ch = lane & 7;
        v0 = r8 * rs + ((ch ^ ((lane >> 4) & 7)) << 4); v1 = r8 * rs + ((ch ^ ((4 + (lane >> 4)) & 7)) << 4);
    }
    __device__ __forceinline__ void issue(rsrc_t r, LAS char* dst  , unsigned soff, unsigned rs) const {
#pragma unroll
        for (int q = 0; q < 4; ++q) dma16(r, dst + q * 1024, (q & 1) ? v1 : v0, soff + q * 8 * rs);
    }
};
template <int NC> struct WImg {
    static constexpr int CPR = 2 * NC, QMAX = (2 * NC + 3) / 4 + ((2 * NC) % 4 == 0 ? 0 : 0);
    unsigned v[5]; int I0;
    static __device__ __forceinline__ int first(int p) { return NC == 9 ? (p < 2 ? 5 * p : 10 + 4 * (p - 2)) : (p < 2 ? 4 * p : 8 + 3 * (p - 2)); }
    static constexpr int BIG = (NC == 9) ? 5 : 4;
    __device__ __forceinline__ void init(int lane, int p, unsigned ldb) {
        I0 = first(p);
#pragma unroll
        for (int q = 0; q < BIG; ++q) {
            const int n = (I0 + q) * 64 + lane, pos = n / CPR, cc = n - pos * CPR;
            const int krow = (pos & 32) + swap23(pos & 31);
            v[q] = krow * ldb + cc * 16;
        }
    }
    __device__ __forceinline__ void issue(rsrc_t r, LAS char* dst  , unsigned soff, int p) const {
#pragma unroll
        for (int q = 0; q < BIG - 1; ++q) dma16(r, dst + (I0 + q) * 1024, v[q], soff);
        if (p < 2) dma16(r, dst + (I0 + BIG - 1) * 1024, v[BIG - 1], soff);
    }
};
__device__ __forceinline__ void issue_lin(rsrc_t r, LAS char* dst, unsigned vl, unsigned soff, int p) {
    const int I0 = WImg<9>::first(p);
#pragma unroll
    for (int q = 0; q < 4; ++q) dma16(r, dst + (I0 + q) * 1024, vl, soff + (I0 + q) * 1024);
    if (p < 2) dma16(r, dst + (I0 + 4) * 1024, vl, soff + (I0 + 4) * 1024);
}
template <int NB, int NS> __device__ __forceinline__ void wait_pieces(int p) {
    if (p < 2) WAIT_V(NB); else WAIT_V(NS);
}

template <int NC> struct XWFrag { bf16x8 x[2]; bf16x8 w[NC]; };
template <int NC> __device__ __forceinline__ void xw_read(XWFrag<NC>& f, LAS char* lds, unsigned aX, unsigned aW) {
#pragma unroll
    for (int t = 0; t < 2; ++t) f.x[t] = lds_b128(lds + aX + t * 2048);
#pragma unroll
    for (int c = 0; c < NC; ++c) f.w[c] = lds_tr(lds + aW + c * 32, 8 * NC * 32);
}
template <int NC, bool TR> __device__ __forceinline__ void xw_mma(f32x4 (&acc)[2][NC], const XWFrag<NC>& f) {
#pragma unroll
    for (int c = 0; c < NC; ++c)
#pragma unroll
        for (int t = 0; t < 2; ++t) acc[t][c] = TR ? MFMA(f.w[c], f.x[t], acc[t][c]) : MFMA(f.x[t], f.w[c], acc[t][c]);
}
template <int NC> __device__ __forceinline__ void xw_sched() {
#pragma unroll
    for (int i = 0; i < NC + 1; ++i) { __builtin_amdgcn_sched_group_barrier(0x008, 1, 0); __builtin_amdgcn_sched_group_barrier(0x100, 2, 0); }
    __builtin_amdgcn_sched_group_barrier(0x008, NC - 1, 0);
}
template <int NC, bool TR, int NST, bool MORE> __device__ __forceinline__ void xw_gemm(f32x4 (&acc)[2][NC], LAS char* lds, int g0, int m, int lane) {
    const int fr = lane & 15, g = lane >> 4, row = 32 * m + fr;
    const unsigned aX0 = row * 128 + ((g ^ ((row >> 1) & 7)) << 4), aX1 = aX0 ^ 64;
    const unsigned aW0 = 16384 + (16 * (g >> 1) + 4 * (g & 1) + (fr >> 2)) * (NC * 32) + (lane & 3) * 8, aW1 = aW0 + 32 * NC * 32;
#pragma unroll
    for (int t = 0; t < 2; ++t)
#pragma unroll
        for (int c = 0; c < NC; ++c) acc[t][c] = (f32x4){0.f, 0.f, 0.f, 0.f};
    XWFrag<NC> f0, f1;
    xw_read<NC>(f0, lds, aX0 + (g0 & 3) * SLOT, aW0 + (g0 & 3) * SLOT);
    for (int s = 0; s < NST - 1; ++s) {
        const unsigned sb = ((g0 + s) & 3) * SLOT, sn = ((g0 + s + 1) & 3) * SLOT;
        xw_read<NC>(f1, lds, aX1 + sb, aW1 + sb);
        xw_mma<NC, TR>(acc, f0);
        xw_sched<NC>(); SB0();
        WAIT_L0(); BAR(); SB0();
        xw_read<NC>(f0, lds, aX0 + sn, aW0 + sn);
        xw_mma<NC, TR>(acc, f1);
        xw_sched<NC>(); SB0();
    }
    const unsigned sb = ((g0 + NST - 1) & 3) * SLOT;
    xw_read<NC>(f1, lds, aX1 + sb, aW1 + sb);
    xw_mma<NC, TR>(acc, f0);
    xw_sched<NC>(); SB0();
    if (MORE) { WAIT_L0(); BAR(); SB0(); }
    xw_mma<NC, TR>(acc, f1);
}

struct MFrag { bf16x8 w[2]; bf16x8 z[9]; };
__device__ __forceinline__ void m_read(MFrag& f, LAS char* lds, unsigned aW, unsigned aZ) {
#pragma unroll
    for (int t = 0; t < 2; ++t) f.w[t] = lds_b128(lds + aW + t * 2048);
#pragma unroll
    for (int c = 0; c < 9; ++c) f.z[c] = lds_b128(lds + aZ + c * 1024);
}
__device__ __forceinline__ void m_mma(f32x4 (&acc)[2][9], const MFrag& f) {
#pragma unroll
    for (int c = 0; c < 9; ++c)
#pragma unroll
        for (int t = 0; t < 2; ++t) acc[t][c] = MFMA(f.z[c], f.w[t], acc[t][c]);
}
__device__ __forceinline__ void m_sched() {
#pragma unroll
    for (int i = 0; i < 11; ++i) { __builtin_amdgcn_sched_group_barrier(0x008, 1, 0); __builtin_amdgcn_sched_group_barrier(0x100, 1, 0); }
    __builtin_amdgcn_sched_group_barrier(0x008, 7, 0);
}
template <int NST, bool MORE> __device__ __forceinline__ void main_gemm(f32x4 (&acc)[2][9], LAS char* lds, int g0, int m, int lane) {
    const int fr = lane & 15, g = lane >> 4, row = 32 * m + fr;
    const unsigned aW0 = row * 128 + ((g ^ ((row >> 1) & 7)) << 4), aW1 = aW0 ^ 64;
    const unsigned aZ0 = 16384 + lane * 16, aZ1 = aZ0 + 9216;
#pragma unroll
    for (int t = 0; t < 2; ++t)
#pragma unroll
        for (int c = 0; c < 9; ++c) acc[t][c] = (f32x4){0.f, 0.f, 0.f, 0.f};
    MFrag f0, f1;
    m_read(f0, lds, aW0 + (g0 & 3) * SLOT, aZ0 + (g0 & 3) * SLOT);
    for (int s = 0; s < NST - 1; ++s) {
        const unsigned sb = ((g0 + s) & 3) * SLOT, sn = ((g0 + s + 1) & 3) * SLOT;
        m_read(f1, lds, aW1 + sb, aZ1 + sb);
        m_mma(acc, f0);
        m_sched(); SB0();
        WAIT_L0(); BAR(); SB0();
        m_read(f0, lds, aW0 + sn, aZ0 + sn);
        m_mma(acc, f1);
        m_sched(); SB0();
    }
    const unsigned sb = ((g0 + NST - 1) & 3) * SLOT;
    m_read(f1, lds, aW1 + sb, aZ1 + sb);
    m_mma(acc, f0);
    m_sched(); SB0();
    if (MORE) { WAIT_L0(); BAR(); SB0(); }
    m_mma(acc, f1);
}

struct K1Args { const u16* xn; const u16* wqkv; const u16* win; u16* qkv; u16* z2t; u32x4* st; unsigned tag0, tag1; };

__device__ __forceinline__ void store_qkv(const f32x4 (&acc)[2][7], u16* qkv, int i, int j, int m, int lane) {
    const int c0 = (j * 27) >> 2;
    const int cprev_end = j == 0 ? 0 : (((j - 1) * 27) >> 2) + 7;
#pragma unroll
    for (int t = 0; t < 2; ++t) {
        u16* rp = qkv + (size_t)(i * 128 + 32 * m + 16 * t + (lane & 15)) * QW + c0 * 16 + (lane >> 4) * 4;
#pragma unroll
        for (int c = 0; c < 7; ++c) {
            if (c == 0 && c0 < cprev_end) continue;
            u32x2 o; o.x = pack_bf16(acc[t][c][0], acc[t][c][1]); o.y = pack_bf16(acc[t][c][2], acc[t][c][3]);
            *(u32x2*)(rp + c * 16) = o;
        }
    }
}

__global__ __launch_bounds__(512, 2) void k1_kernel(K1Args a) {
    extern __shared__ __attribute__((aligned(16))) char smem[];
    LAS char* lds = (LAS char*)smem;
    const int wid = __builtin_amdgcn_readfirstlane(threadIdx.x >> 6), lane = threadIdx.x & 63;
    const Tile T = tile_of_block();
    if (wid >= 4) {
        const int p = wid - 4;
        const rsrc_t rX = mk_rsrc(a.xn, S_ * D_ * 2), rQ = mk_rsrc(a.wqkv, D_ * QW * 2), rW = mk_rsrc(a.win, D_ * F_ * 2);
        RowsImg xi; xi.init(lane, D_ * 2);
        WImg<7> wq; wq.init(lane, p, QW * 2);
        WImg<9> wz; wz.init(lane, p, F_ * 2);
        const unsigned sX = (unsigned)(T.i * 128 + 32 * p) * (D_ * 2), sQ = (unsigned)((T.j * 27) >> 2) * 32, sZ = (unsigned)(FH + T.j * 144) * 2;
        auto issue = [&](int g) {
            LAS char* slot = lds + (g & 3) * SLOT;
            if (g < 9) { xi.issue(rX, slot + p * 4096, sX + g * 128, D_ * 2); wq.issue(rQ, slot + 16384, sQ + g * 64 * (QW * 2), p); }
            else { xi.issue(rX, slot + p * 4096, sX + (g - 9) * 128, D_ * 2); wz.issue(rW, slot + 16384, sZ + (g - 9) * 64 * (F_ * 2), p); }
        };
        for (int g = 0; g < 4; ++g) issue(g);
        wait_pieces<24, 21>(p); BAR();
        for (int g = 1; g < 18; ++g) {
            if (g < 16) wait_pieces<16, 14>(p); else if (g == 16) wait_pieces<9, 8>(p); else WAIT_V(0);
            BAR();
            if (g + 3 < 18) issue(g + 3);
        }
        return;
    }
    const int m = wid, g = lane >> 4;
    BAR();
    {
        f32x4 acc[2][7];
        xw_gemm<7, true, 9, true>(acc, lds, 0, m, lane);
        store_qkv(acc, a.qkv, T.i, T.j, m, lane);
    }
    f32x4 acc[2][9];
    xw_gemm<9, false, 9, false>(acc, lds, 9, m, lane);
#pragma unroll
    for (int t = 0; t < 2; ++t)
#pragma unroll
        for (int c = 0; c < 9; ++c)
#pragma unroll
            for (int r = 0; r < 4; ++r) acc[t][c][r] = gelu_f(acc[t][c][r]);
    float mean[2][4], m2[2][4];
#pragma unroll
    for (int t = 0; t < 2; ++t)
#pragma unroll
        for (int r = 0; r < 4; ++r) {
            float s = 0.f;
#pragma unroll
            for (int c = 0; c < 9; ++c) s += acc[t][c][r];
            s = row16_sum(s) * (1.0f / 144.0f);
            float q = 0.f;
#pragma unroll
            for (int c = 0; c < 9; ++c) { const float d = acc[t][c][r] - s; q += d * d; }
            mean[t][r] = s; m2[t][r] = row16_sum(q);
        }
    {
        const int idx = lane & 7;
        float mm = mean[0][0], qq = m2[0][0];
#pragma unroll
        for (int k = 1; k < 8; ++k) { mm = idx == k ? mean[k >> 2][k & 3] : mm; qq = idx == k ? m2[k >> 2][k & 3] : qq; }
        const int row = T.i * 128 + 32 * m + 16 * (idx >> 2) + 4 * g + (idx & 3);
        if ((lane & 15) < 8) {
            const rsrc_t rS = mk_rsrc(a.st, S_ * 16 * 16);
            u32x4 v = {__float_as_uint(mm), a.tag0, __float_as_uint(qq), a.tag1};
            __builtin_amdgcn_raw_buffer_store_b128(v, rS, (row * 16 + T.j) * 16, 0, 16);
        }
    }
    unsigned zp[2][9][2];
#pragma unroll
    for (int t = 0; t < 2; ++t)
#pragma unroll
        for (int c = 0; c < 9; ++c) { zp[t][c][0] = pack_bf16(acc[t][c][0], acc[t][c][1]); zp[t][c][1] = pack_bf16(acc[t][c][2], acc[t][c][3]); }
    float mu, rs;
    {
        const rsrc_t rS = mk_rsrc(a.st, S_ * 16 * 16);
        const unsigned off = ((T.i * 128 + 32 * m + (lane >> 1)) * 16 + (lane & 1) * 8) * 16;
        u32x4 v[8]; unsigned spins = 0;
        for (;;) {
            bool ok = true;
#pragma unroll
            for (int k = 0; k < 8; ++k) { v[k] = __builtin_amdgcn_raw_buffer_load_b128(rS, off + k * 16, 0, 16); ok = ok && v[k].y == a.tag0 && v[k].w == a.tag1; }
            if (__all(ok) || ++spins > 200000u) break;
            __builtin_amdgcn_s_sleep(1);
        }
        float s = 0.f;
#pragma unroll
        for (int k = 0; k < 8; ++k) s += __uint_as_float(v[k].x);
        s = dpp_add<0xB1>(s);
        mu = s * (1.0f / 16.0f);
        float q = 0.f;
#pragma unroll
        for (int k = 0; k < 8; ++k) { const float d = __uint_as_float(v[k].x) - mu; q += __uint_as_float(v[k].z) + 144.0f * d * d; }
        q = dpp_add<0xB1>(q);
        rs = 1.0f / sqrtf(q * (1.0f / FH) + 1e-5f);
    }
    float mr[2][4], rr[2][4];
#pragma unroll
    for (int t = 0; t < 2; ++t)
#pragma unroll
        for (int r = 0; r < 4; ++r) { const int src = 2 * (16 * t + 4 * g + r); mr[t][r] = __shfl(mu, src); rr[t][r] = __shfl(rs, src); }
    u16* zo = a.z2t + ((size_t)(T.j * 64 + T.i * 4 + m) * 9) * 512 + lane * 8;
#pragma unroll
    for (int c = 0; c < 9; ++c) {
        unsigned x0, x1, y0, y1;
        x0 = pack_bf16((bflo(zp[0][c][0]) - mr[0][0]) * rr[0][0], (bfhi(zp[0][c][0]) - mr[0][1]) * rr[0][1]);
        x1 = pack_bf16((bflo(zp[0][c][1]) - mr[0][2]) * rr[0][2], (bfhi(zp[0][c][1]) - mr[0][3]) * rr[0][3]);
        y0 = pack_bf16((bflo(zp[1][c][0]) - mr[1][0]) * rr[1][0], (bfhi(zp[1][c][0]) - mr[1][1]) * rr[1][1]);
        y1 = pack_bf16((bflo(zp[1][c][1]) - mr[1][2]) * rr[1][2], (bfhi(zp[1][c][1]) - mr[1][3]) * rr[1][3]);
        auto s0 = __builtin_amdgcn_permlane32_swap(x0, y0, false, false);
        auto s1 = __builtin_amdgcn_permlane32_swap(x1, y1, false, false);
        auto t0 = __builtin_amdgcn_permlane16_swap(s0[0], s0[1], false, false);
        auto t1 = __builtin_amdgcn_permlane16_swap(s1[0], s1[1], false, false);
        u32x4 o = {t0[0], t1[0], t0[1], t1[1]};
        *(u32x4*)(zo + c * 512) = o;
    }
}

constexpr int AT_Q = 0, AT_K = 8 * 1152, AT_V = AT_K + 22 * 1152, AT_END = AT_V + 22 * 1152;
__device__ __forceinline__ void attn_issue(rsrc_t rQ, LAS char* base, int n, int p, int lane) {
#pragma unroll
    for (int q = 0; q < 15; ++q) {
        const int I = p + 4 * q;
        if (I < 59) {
            const int c = I * 64 + lane, row = c / 72, cc = c - row * 72;
            int t, seg;
            if (row < 8) { t = 8 * n + row; seg = 0; } else if (row < 30) { t = 8 * n - 14 + (row - 8); seg = 1; } else { t = 8 * n - 14 + (row - 30); seg = 2; }
            const unsigned voff = (t < 0 || row >= 52) ? 0x7ffffff0u : (unsigned)(t * (QW * 2) + seg * 1152 + cc * 16);
            dma16(rQ, base + I * 1024, voff, 0);
        }
    }
}
__device__ __forceinline__ float dot2(unsigned a, unsigned b, float c) { return __builtin_amdgcn_fdot2_f32_bf16(__builtin_bit_cast(bf16x2_t, a), __builtin_bit_cast(bf16x2_t, b), c, false); }
__device__ __forceinline__ float xchg32(float x) {
    auto r = __builtin_amdgcn_permlane32_swap(__float_as_uint(x), __float_as_uint(x), false, false);
    return __uint_as_float((threadIdx.x & 32) ? r[0] : r[1]);
}
__device__ __forceinline__ void attn_compute(LAS char* base, LAS char* outb, int n, int w, int lane) {
    const int h = lane & 31, hf = lane >> 5, t = 8 * n + w;
    LAS unsigned* q = (LAS unsigned*)(base + AT_Q + w * 1152 + h * 36);
    unsigned qv[9];
#pragma unroll
    for (int d = 0; d < 9; ++d) qv[d] = q[d];
    float lg[4];
#pragma unroll
    for (int k = 0; k < 4; ++k) {
        const int ww = 4 * hf + k;
        LAS unsigned* kp = (LAS unsigned*)(base + AT_K + (14 + w - 2 * ww) * 1152 + h * 36);
        float s = 0.f;
#pragma unroll
        for (int d = 0; d < 9; ++d) s = dot2(qv[d], kp[d], s);
        lg[k] = (t - 2 * ww >= 0) ? s * 0.23570226039551584f : -1e9f;
    }
    float mx = fmaxf(fmaxf(lg[0], lg[1]), fmaxf(lg[2], lg[3]));
    mx = fmaxf(mx, xchg32(mx));
    float pr[8], sum = 0.f;
#pragma unroll
    for (int k = 0; k < 4; ++k) { lg[k] = __builtin_amdgcn_exp2f((lg[k] - mx) * 1.4426950408889634f); sum += lg[k]; }
    sum += xchg32(sum);
    const float inv = __builtin_amdgcn_rcpf(sum);
#pragma unroll
    for (int k = 0; k < 4; ++k) { const float o = xchg32(lg[k]); pr[k] = (hf ? o : lg[k]) * inv; pr[4 + k] = (hf ? lg[k] : o) * inv; }
    float acc[10];
#pragma unroll
    for (int d = 0; d < 10; ++d) acc[d] = 0.f;
#pragma unroll
    for (int ww = 0; ww < 8; ++ww) {
        LAS unsigned* vp = (LAS unsigned*)(base + AT_V + (14 + w - 2 * ww) * 1152 + h * 36 + hf * 16);
#pragma unroll
        for (int d = 0; d < 5; ++d) { const unsigned v = vp[d]; acc[2 * d] += pr[ww] * bflo(v); acc[2 * d + 1] += pr[ww] * bfhi(v); }
    }
#pragma unroll
    for (int d = 0; d < 5; ++d) {
        const int dc = h * 18 + hf * 8 + 2 * d;
        const int ch = (dc >> 5) * 4 + ((dc & 15) >> 2), e = (dc & 3) + 4 * ((dc >> 4) & 1);
        if (!(hf && d == 0)) *(LAS unsigned*)(outb + w * 1152 + ch * 16 + e * 2) = pack_bf16(acc[2 * d], acc[2 * d + 1]);
    }
}

struct K2Args { const u16* xn; const u16* win; const u16* wsp; const u16* bsp; const u16* z2t; const u16* qkv; u16* hf; u16* of; };

__global__ __launch_bounds__(512, 2) void k2_kernel(K2Args a) {
    extern __shared__ __attribute__((aligned(16))) char smem[];
    LAS char* lds = (LAS char*)smem;
    const int wid = __builtin_amdgcn_readfirstlane(threadIdx.x >> 6), lane = threadIdx.x & 63;
    const Tile T = tile_of_block();
    const int n = blockIdx.x;
    LAS char* ab = lds + 2 * SLOT;
    LAS char* ob = lds + SPARE;
    if (wid >= 4) {
        const int p = wid - 4;
        const rsrc_t rX = mk_rsrc(a.xn, S_ * D_ * 2), rW = mk_rsrc(a.win, D_ * F_ * 2), rS = mk_rsrc(a.wsp, S_ * S_ * 2), rZ = mk_rsrc(a.z2t, S_ * FH * 2),
                     rQ = mk_rsrc(a.qkv, S_ * QW * 2);
        RowsImg xi; xi.init(lane, D_ * 2);
        RowsImg si; si.init(lane, S_ * 2);
        WImg<9> wz; wz.init(lane, p, F_ * 2);
        const unsigned sX = (unsigned)(T.i * 128 + 32 * p) * (D_ * 2), sZ1 = (unsigned)(T.j * 144) * 2, sS = (unsigned)(T.i * 128 + 32 * p) * (S_ * 2), sZ = (unsigned)T.j * (64u * 9216u);
        auto issue = [&](int g) {
            LAS char* slot = lds + (g & 3) * SLOT;
            if (g < 9) { xi.issue(rX, slot + p * 4096, sX + g * 128, D_ * 2); wz.issue(rW, slot + 16384, sZ1 + g * 64 * (F_ * 2), p); }
            else { si.issue(rS, slot + p * 4096, sS + (g - 9) * 128, S_ * 2); issue_lin(rZ, slot + 16384, lane * 16, sZ + (g - 9) * 18432, p); }
        };
        attn_issue(rQ, ab, n, p, lane);
        issue(0); issue(1);
        wait_pieces<18, 16>(p); BAR();
        attn_compute(ab, ob, n, wid, lane);
        WAIT_L0(); BAR();
        issue(2); issue(3);
        wait_pieces<27, 24>(p); BAR();
        for (int g = 1; g < 41; ++g) {
            if (g < 39) wait_pieces<18, 16>(p); else if (g == 39) wait_pieces<9, 8>(p); else WAIT_V(0);
            BAR();
            if (g + 3 < 41) issue(g + 3);
        }
        return;
    }
    const int m = wid, fr = lane & 15;
    BAR();
    attn_compute(ab, ob, n, wid, lane);
    WAIT_L0(); BAR();
    {
        for (int c = threadIdx.x; c < 576; c += 256) {
            const int r = c & 7, ch = c >> 3, t = 8 * n + r;
            const u32x4 v = *(LAS u32x4*)(ob + r * 1152 + ch * 16);
            *(u32x4*)(a.of + (((size_t)(t >> 4) * 18 + (ch >> 2)) * 64 + (t & 15) + 16 * (ch & 3)) * 8) = v;
        }
    }
    float bt[2];
#pragma unroll
    for (int t = 0; t < 2; ++t) bt[t] = bf2f(a.bsp[T.i * 128 + 32 * m + 16 * t + fr]);
    BAR();
    unsigned z1p[2][9][2];
    {
        f32x4 acc[2][9];
        xw_gemm<9, true, 9, true>(acc, lds, 0, m, lane);
#pragma unroll
        for (int t = 0; t < 2; ++t)
#pragma unroll
            for (int c = 0; c < 9; ++c) {
                z1p[t][c][0] = pack_bf16(gelu_f(acc[t][c][0]), gelu_f(acc[t][c][1]));
                z1p[t][c][1] = pack_bf16(gelu_f(acc[t][c][2]), gelu_f(acc[t][c][3]));
            }
    }
    f32x4 acc[2][9];
    main_gemm<32, false>(acc, lds, 9, m, lane);
#pragma unroll
    for (int t = 0; t < 2; ++t) {
        u16* hp = a.hf + (size_t)(T.i * 8 + 2 * m + t) * 72 * 512;
#pragma unroll
        for (int pp = 0; pp < 4; ++pp) {
            const f32x4 x = acc[t][2 * pp] + bt[t], y = acc[t][2 * pp + 1] + bt[t];
            u32x4 o;
            o.x = pack_bf16(x[0] * bflo(z1p[t][2 * pp][0]), x[1] * bfhi(z1p[t][2 * pp][0])); o.y = pack_bf16(x[2] * bflo(z1p[t][2 * pp][1]), x[3] * bfhi(z1p[t][2 * pp][1]));
            o.z = pack_bf16(y[0] * bflo(z1p[t][2 * pp + 1][0]), y[1] * bfhi(z1p[t][2 * pp + 1][0])); o.w = pack_bf16(y[2] * bflo(z1p[t][2 * pp + 1][1]), y[3] * bfhi(z1p[t][2 * pp + 1][1]));
            *(u32x4*)(hp + (T.j * 4 + pp) * 512 + lane * 8) = o;
        }
        const f32x4 x = acc[t][8] + bt[t]; u32x2 o;
        o.x = pack_bf16(x[0] * bflo(z1p[t][8][0]), x[1] * bfhi(z1p[t][8][0])); o.y = pack_bf16(x[2] * bflo(z1p[t][8][1]), x[3] * bfhi(z1p[t][8][1]));
        *(u32x2*)(hp + (64 + (T.j >> 1)) * 512 + lane * 8 + (T.j & 1) * 4) = o;
    }
}

struct K3Args { const u16* x; const u16* wo; const u16* wout; const u16* hf; const u16* of; u16* out; };
struct EFrag { bf16x8 w; bf16x8 b[5]; };
__device__ __forceinline__ void e_read(EFrag& f, LAS char* lds, unsigned aW, unsigned aB) {
    f.w = lds_tr(lds + aW, 2048);
#pragma unroll
    for (int t = 0; t < 5; ++t) f.b[t] = lds_b128(lds + aB + t * 1024);
}
__device__ __forceinline__ void e_mma(f32x4 (&acc)[5], const EFrag& f) {
#pragma unroll
    for (int t = 0; t < 5; ++t) acc[t] = MFMA(f.w, f.b[t], acc[t]);
}
template <int NST, int NKS, bool MORE> __device__ __forceinline__ void e_gemm(f32x4 (&acc)[5], LAS char* lds, int g0, int w, int lane) {
    const int g = lane >> 4, q = (lane & 15) >> 2;
    const unsigned aW = (4 * g + q) * 128 + ((w ^ ((2 * g + (q >> 1)) & 3)) << 5) + (lane & 3) * 8, aB = 16384 + lane * 16;
#pragma unroll
    for (int t = 0; t < 5; ++t) acc[t] = (f32x4){0.f, 0.f, 0.f, 0.f};
    for (int s = 0; s < NST; ++s) {
        const unsigned sb = ((g0 + s) & 3) * SLOT;
        EFrag f[4];
        const int nk = NKS - 4 * s;
#pragma unroll
        for (int k = 0; k < 4; ++k) e_read(f[k], lds, aW + sb + k * 4096, aB + sb + k * 5120);
        if (NKS % 4 != 0 && nk < 4) {
#pragma unroll
            for (int k = 0; k < (NKS % 4); ++k) e_mma(acc, f[k]);
        } else {
#pragma unroll
            for (int k = 0; k < 4; ++k) e_mma(acc, f[k]);
        }
        if (s + 1 < NST || MORE) { SB0(); WAIT_L0(); BAR(); SB0(); }
    }
}

__global__ __launch_bounds__(512, 2) void k3_kernel(K3Args a) {
    extern __shared__ __attribute__((aligned(16))) char smem[];
    LAS char* lds = (LAS char*)smem;
    const int wid = __builtin_amdgcn_readfirstlane(threadIdx.x >> 6), lane = threadIdx.x & 63;
    const int nt = (blockIdx.x & 7) * 30 + (blockIdx.x >> 3);
    if ((blockIdx.x >> 3) >= 30 || nt >= 234) return;
    const int rb = nt / 9, cb = nt - rb * 9;
    if (wid >= 4) {
        const int p = wid - 4;
        const rsrc_t rO = mk_rsrc(a.of, S_ * D_ * 2), rH = mk_rsrc(a.hf, S_ * FH * 2), rWo = mk_rsrc(a.wo, D_ * D_ * 2), rW = mk_rsrc(a.wout, FH * D_ * 2);
        const int r8 = lane >> 3, ch = lane & 7;
        const unsigned vW = r8 * (D_ * 2) + (((ch >> 1) ^ ((r8 >> 1) & 3)) << 5) + (ch & 1) * 16, vl = lane * 16;
        int tt[5];
#pragma unroll
        for (int t = 0; t < 5; ++t) tt[t] = min(rb * 5 + t, 127);
        auto issue = [&](int g) {
            LAS char* slot = lds + (g & 3) * SLOT;
            if (g < 5) {
                const int ks = min(4 * g + p, 17);
#pragma unroll
                for (int q = 0; q < 4; ++q) dma16(rWo, slot + p * 4096 + q * 1024, vW, (unsigned)(32 * ks + 8 * q) * (D_ * 2) + cb * 128);
#pragma unroll
                for (int t = 0; t < 5; ++t) dma16(rO, slot + 16384 + p * 5120 + t * 1024, vl, (unsigned)(tt[t] * 18 + ks) * 1024);
            } else {
                const int ks = 4 * (g - 5) + p;
                const int c0 = ks < 64 ? 9 * (ks >> 2) + 2 * (ks & 3) : 18 * (ks - 64) + 8, c1 = ks < 64 ? c0 + 1 : c0 + 9;
#pragma unroll
                for (int q = 0; q < 4; ++q) dma16(rW, slot + p * 4096 + q * 1024, vW, (unsigned)((q < 2 ? c0 : c1) * 16 + 8 * (q & 1)) * (D_ * 2) + cb * 128);
#pragma unroll
                for (int t = 0; t < 5; ++t) dma16(rH, slot + 16384 + p * 5120 + t * 1024, vl, (unsigned)(tt[t] * 72 + ks) * 1024);
            }
        };
        for (int g = 0; g < 4; ++g) issue(g);
        WAIT_V(27); BAR();
        for (int g = 1; g < 23; ++g) {
            if (g < 21) WAIT_V(18); else if (g == 21) WAIT_V(9); else WAIT_V(0);
            BAR();
            if (g + 3 < 23) issue(g + 3);
        }
        return;
    }
    const int w = wid, fr = lane & 15, g = lane >> 4;
    u32x2 xv[5];
#pragma unroll
    for (int t = 0; t < 5; ++t) xv[t] = *(const u32x2*)(a.x + (size_t)(min(rb * 5 + t, 127) * 16 + fr) * D_ + cb * 64 + w * 16 + g * 4);
    BAR();
    f32x4 sa[5], ga[5];
    e_gemm<5, 18, true>(sa, lds, 0, w, lane);
    e_gemm<18, 72, false>(ga, lds, 5, w, lane);
#pragma unroll
    for (int t = 0; t < 5; ++t) {
        if (rb * 5 + t < 128) {
            float o[4];
#pragma unroll
            for (int r = 0; r < 4; ++r) {
                const float gv = ga[t][r], sg = gv * __builtin_amdgcn_rcpf(1.0f + __builtin_amdgcn_exp2f(gv * -1.4426950408889634f));
                const float xx = (r & 1) ? bfhi(xv[t][r >> 1]) : bflo(xv[t][r >> 1]);
                o[r] = xx + sa[t][r] * sg;
            }
            u32x2 ov; ov.x = pack_bf16(o[0], o[1]); ov.y = pack_bf16(o[2], o[3]);
            *(u32x2*)(a.out + (size_t)((rb * 5 + t) * 16 + fr) * D_ + cb * 64 + w * 16 + g * 4) = ov;
        }
    }
}

__global__ void k_clear(u32x4* p, int n) { int i = blockIdx.x * 256 + threadIdx.x; if (i < n) p[i] = (u32x4){0u, 0u, 0u, 0u}; }

extern "C" void kernel_launch(void* const* d_in, const int* in_sizes, int n_in, void* d_out, int out_size, void* d_ws, size_t ws_size, hipStream_t stream) {
    const u16* x = (const u16*)d_in[0]; const u16* wqkv = (const u16*)d_in[1]; const u16* wo = (const u16*)d_in[2];
    const u16* win = (const u16*)d_in[4]; const u16* wsp = (const u16*)d_in[5]; const u16* bsp = (const u16*)d_in[6]; const u16* wout = (const u16*)d_in[7];
    char* ws = (char*)d_ws;
    u16* xn = (u16*)(ws + WS_XN); u16* z2t = (u16*)(ws + WS_Z2T); u16* qkv = (u16*)(ws + WS_QKV); u16* hf = (u16*)(ws + WS_HF); u16* of = (u16*)(ws + WS_OF);
    u32x4* st = (u32x4*)(ws + WS_ST);

    {
        hipFuncSetAttribute((const void*)k1_kernel, hipFuncAttributeMaxDynamicSharedMemorySize, LDS_BYTES);
        hipFuncSetAttribute((const void*)k2_kernel, hipFuncAttributeMaxDynamicSharedMemorySize, LDS_BYTES);
        hipFuncSetAttribute((const void*)k3_kernel, hipFuncAttributeMaxDynamicSharedMemorySize, LDS_BYTES);

    }
    hipMemsetAsync(st, 0, (size_t)S_ * 16 * 16, stream);
    k0_ln<<<64, 256, 0, stream>>>(x, xn);
    K1Args a1{xn, wqkv, win, qkv, z2t, st, 0x5A17C3E9u, 0xA5E83C16u};
    k1_kernel<<<256, 512, LDS_BYTES, stream>>>(a1);
    K2Args a2{xn, win, wsp, bsp, z2t, qkv, hf, of};
    k2_kernel<<<256, 512, LDS_BYTES, stream>>>(a2);
    K3Args a3{x, wo, wout, hf, of, (u16*)d_out};
    k3_kernel<<<256, 512, LDS_BYTES, stream>>>(a3);
}
```

```cpp
#include <hip/hip_runtime.h>
#include <hip/hip_bf16.h>
#include <stdint.h>

typedef unsigned short u16;
typedef unsigned long long u64;
typedef short bf16x8 __attribute__((ext_vector_type(8)));
typedef short s16x4 __attribute__((ext_vector_type(4)));
typedef float f32x4 __attribute__((ext_vector_type(4)));
typedef float f32x2 __attribute__((ext_vector_type(2)));
typedef unsigned u32x4 __attribute__((ext_vector_type(4)));
typedef unsigned u32x2 __attribute__((ext_vector_type(2)));
typedef __bf16 bf16x2_t __attribute__((ext_vector_type(2)));
#define LAS __attribute__((address_space(3)))
#define GAS __attribute__((address_space(1)))
typedef LAS void* lds_vp;
typedef __amdgpu_buffer_rsrc_t rsrc_t;

constexpr int S_ = 2048, D_ = 576, F_ = 4608, FH = 2304, QW = 1728;
constexpr int SLOT = 36864, SPARE = 4 * SLOT, LDS_BYTES = 163840;
constexpr int SP_CS = SPARE, SP_AO = SPARE + 2048;
constexpr size_t WS_Z2T = 0;
constexpr size_t WS_QKV = WS_Z2T + (size_t)S_ * FH * 2;
constexpr size_t WS_HF = WS_QKV + (size_t)S_ * QW * 2;
constexpr size_t WS_OF = WS_HF + (size_t)S_ * FH * 2;
constexpr size_t WS_ST = WS_OF + (size_t)S_ * D_ * 2;
constexpr size_t WS_CTL = WS_ST + (size_t)S_ * 16 * 16;
constexpr size_t CTL_EP = 0, CTL_QF = 1024, CTL_ZF = CTL_QF + 8192, CTL_OF = CTL_ZF + 8192, CTL_HF = CTL_OF + 8192, CTL_END = CTL_HF + 8192;
constexpr int G_QKV = 0, G_Z2 = 9, G_Z1 = 18, G_Z1B = 21, G_MAIN = 27, G_S = 59, G_G = 64, G_END = 82;
constexpr unsigned SPIN_CAP = 400000u;

__device__ __forceinline__ float bflo(unsigned v) { return __uint_as_float(v << 16); }
__device__ __forceinline__ float bfhi(unsigned v) { return __uint_as_float(v & 0xffff0000u); }
__device__ __forceinline__ float bf2f(u16 v) { return __uint_as_float(((unsigned)v) << 16); }
__device__ __forceinline__ unsigned pack_bf16(float lo, float hi) {
    f32x2 v = {lo, hi};
    return __builtin_bit_cast(unsigned, __builtin_convertvector(v, bf16x2_t));
}
#define BAR() do { asm volatile("" ::: "memory"); __builtin_amdgcn_s_barrier(); asm volatile("" ::: "memory"); } while (0)
#define WAIT_V(n) asm volatile("s_waitcnt vmcnt(%0)" ::"n"(n) : "memory")
#define WAIT_L0() asm volatile("s_waitcnt lgkmcnt(0)" ::: "memory")
#define SB0() __builtin_amdgcn_sched_barrier(0)
#define ST_DECL
#define ST(k)
#define ST_END(who)

__device__ __forceinline__ rsrc_t mk_rsrc(const void* p, unsigned bytes) { return __builtin_amdgcn_make_buffer_rsrc((void*)p, 0, bytes, 0x00020000); }
__device__ __forceinline__ void dma16(rsrc_t r, LAS char* lds, unsigned voff, unsigned soff) {
    __builtin_amdgcn_raw_ptr_buffer_load_lds(r, (lds_vp)lds, 16, voff, soff, 0, 0);
}
__device__ __forceinline__ void dma16_dev(rsrc_t r, LAS char* lds, unsigned voff, unsigned soff) {
    __builtin_amdgcn_raw_ptr_buffer_load_lds(r, (lds_vp)lds, 16, voff, soff, 0, 16);
}
#define ST_AUX 16
__device__ __forceinline__ void st128_dev(rsrc_t r, unsigned off, u32x4 v) { __builtin_amdgcn_raw_buffer_store_b128(v, r, off, 0, ST_AUX); }
__device__ __forceinline__ void st64_dev(rsrc_t r, unsigned off, u32x2 v) { __builtin_amdgcn_raw_buffer_store_b64(v, r, off, 0, ST_AUX); }
__device__ __forceinline__ bf16x8 lds_b128(LAS char* p) { return *(LAS bf16x8*)p; }
__device__ __forceinline__ bf16x8 lds_tr(LAS char* p, int off1) {
    s16x4 a = __builtin_amdgcn_ds_read_tr16_b64_v4i16((LAS s16x4*)p);
    s16x4 b = __builtin_amdgcn_ds_read_tr16_b64_v4i16((LAS s16x4*)(p + off1));
    return __builtin_shufflevector(a, b, 0, 1, 2, 3, 4, 5, 6, 7);
}
#define MFMA(a, b, c) __builtin_amdgcn_mfma_f32_16x16x32_bf16(a, b, c, 0, 0, 0)
template <int CTRL> __device__ __forceinline__ float dpp_add(float x) {
    return x + __builtin_bit_cast(float, __builtin_amdgcn_update_dpp(0, __builtin_bit_cast(int, x), CTRL, 0xf, 0xf, false));
}
__device__ __forceinline__ float row16_sum(float x) { x = dpp_add<0x128>(x); x = dpp_add<0x124>(x); x = dpp_add<0x122>(x); x = dpp_add<0x121>(x); return x; }
__device__ __forceinline__ float gelu_f(float x) {
    const float t = x * (-2.3022081498f - 0.1029432397f * x * x);
    return x * __builtin_amdgcn_rcpf(1.0f + __builtin_amdgcn_exp2f(t));
}
__device__ __forceinline__ f32x2 gelu2(f32x2 x) {
    const f32x2 t = x * (x * x * -0.1029432397f + -2.3022081498f);
    f32x2 e; e.x = __builtin_amdgcn_exp2f(t.x); e.y = __builtin_amdgcn_exp2f(t.y);
    const f32x2 d = e + 1.0f;
    f32x2 r; r.x = __builtin_amdgcn_rcpf(d.x); r.y = __builtin_amdgcn_rcpf(d.y);
    return x * r;
}
__device__ __forceinline__ f32x2 lo2(f32x4 v) { return (f32x2){v[0], v[1]}; }
__device__ __forceinline__ f32x2 hi2(f32x4 v) { return (f32x2){v[2], v[3]}; }
__device__ __forceinline__ unsigned pack2(f32x2 v) { return __builtin_bit_cast(unsigned, __builtin_convertvector(v, bf16x2_t)); }
__device__ __forceinline__ f32x2 unpack2(unsigned v) { return (f32x2){__uint_as_float(v << 16), __uint_as_float(v & 0xffff0000u)}; }
__device__ __forceinline__ int swap23(int r) { return (r & 0x13) | ((r & 4) << 1) | ((r & 8) >> 1); }
__device__ __forceinline__ float xchg32(float x) {
    auto r = __builtin_amdgcn_permlane32_swap(__float_as_uint(x), __float_as_uint(x), false, false);
    return __uint_as_float((threadIdx.x & 32) ? r[0] : r[1]);
}
__device__ __forceinline__ u32x4 gather8(unsigned x0, unsigned x1, unsigned y0, unsigned y1) {
    auto s0 = __builtin_amdgcn_permlane32_swap(x0, y0, false, false);
    auto s1 = __builtin_amdgcn_permlane32_swap(x1, y1, false, false);
    auto t0 = __builtin_amdgcn_permlane16_swap(s0[0], s0[1], false, false);
    auto t1 = __builtin_amdgcn_permlane16_swap(s1[0], s1[1], false, false);
    return (u32x4){t0[0], t1[0], t0[1], t1[1]};
}
__device__ __forceinline__ void publish(char* ctl, size_t word, u64 tag, int lane) {
    asm volatile("s_waitcnt vmcnt(0)" ::: "memory");
    if (lane == 0) __hip_atomic_store((GAS u64*)(ctl + word), tag, __ATOMIC_RELAXED, __HIP_MEMORY_SCOPE_AGENT);
}
__device__ __forceinline__ bool flags_ready(const char* f0, int n0, const char* f1, int n1, u64 tag, int lane) {
    u64 a = tag, b = tag;
    if (lane < n0) a = __hip_atomic_load((const GAS u64*)(f0 + lane * 8), __ATOMIC_RELAXED, __HIP_MEMORY_SCOPE_AGENT);
    if (lane < n1) b = __hip_atomic_load((const GAS u64*)(f1 + lane * 8), __ATOMIC_RELAXED, __HIP_MEMORY_SCOPE_AGENT);
    return __all(a == tag && b == tag);
}

struct Args { const u16* x; const u16* wqkv; const u16* wo; const u16* win; const u16* wsp; const u16* bsp; const u16* wout; u16* out; char* ws; };

struct RowsImg {
    unsigned v0, v1;
    __device__ __forceinline__ void init(int lane, unsigned rs) {
        const int r8 = lane >> 3, ch = lane & 7;
        v0 = r8 * rs + ((ch ^ ((lane >> 4) & 7)) << 4); v1 = r8 * rs + ((ch ^ ((4 + (lane >> 4)) & 7)) << 4);
    }
    __device__ __forceinline__ void issue(rsrc_t r, LAS char* dst, unsigned soff, unsigned rs) const {
#pragma unroll
        for (int q = 0; q < 4; ++q) dma16(r, dst + q * 1024, (q & 1) ? v1 : v0, soff + q * 8 * rs);
    }
};
template <int NC> struct WImg {
    static constexpr int CPR = 2 * NC, BIG = (NC == 9) ? 5 : 4;
    unsigned v[BIG];
    static __device__ __forceinline__ int first(int p) { return NC == 9 ? (p < 2 ? 5 * p : 10 + 4 * (p - 2)) : (p < 2 ? 4 * p : 8 + 3 * (p - 2)); }
    __device__ __forceinline__ void init(int lane, int p, unsigned ldb) {
        const int I0 = first(p);
#pragma unroll
        for (int q = 0; q < BIG; ++q) {
            const int n = (I0 + q) * 64 + lane, pos = n / CPR, cc = n - pos * CPR;
            v[q] = ((pos & 32) + swap23(pos & 31)) * ldb + cc * 16;
        }
    }
    __device__ __forceinline__ void issue(rsrc_t r, LAS char* dst, unsigned soff, int p) const {
        const int I0 = first(p);
#pragma unroll
        for (int q = 0; q < BIG - 1; ++q) dma16(r, dst + (I0 + q) * 1024, v[q], soff);
        if (p < 2) dma16(r, dst + (I0 + BIG - 1) * 1024, v[BIG - 1], soff);
    }
};

#define MMA(acc, a, b) asm volatile("v_mfma_f32_16x16x32_bf16 %0, %1, %2, %0" : "+v"(acc) : "v"(a), "v"(b))
#define MMA_DRAIN() do { SB0(); asm volatile("s_nop 7\n\ts_nop 3"); SB0(); } while (0)
struct NoHook { __device__ __forceinline__ void operator()(int) const {} };
__device__ __forceinline__ s16x4 lds_tr1(LAS char* p) { return __builtin_amdgcn_ds_read_tr16_b64_v4i16((LAS s16x4*)p); }
__device__ __forceinline__ bf16x8 cat8(s16x4 a, s16x4 b) { return __builtin_shufflevector(a, b, 0, 1, 2, 3, 4, 5, 6, 7); }

template <int NC, bool XST> struct XWAcc { f32x4 a[2][NC]; f32x4 cs[3]; f32x4 sx[2], sq[2]; };
template <int NC> struct XF { bf16x8 x[2]; s16x4 w[NC][2]; };
template <int NC> __device__ __forceinline__ void xa_read1(XF<NC>& f, int i, LAS char* pX, LAS char* pW) {
    if (i < 2) f.x[i] = lds_b128(pX + i * 2048);
    else f.w[(i - 2) >> 1][i & 1] = lds_tr1(pW + ((i - 2) >> 1) * 32 + (i & 1) * (8 * NC * 32));
}
template <int NC, bool TR, bool XST, int M, int DBL, bool LOAD>
__device__ __forceinline__ void xa_step(XWAcc<NC, XST>& A, const XF<NC>& f, XF<NC>& n, LAS char* pX, LAS char* pW, const bf16x8& ones) {
    constexpr int R = 2 + 2 * NC;
    int rd = 0, gap = 0;
#define XA_GAP() do { if (LOAD) { if (rd < R) { xa_read1<NC>(n, rd, pX, pW); ++rd; } if (gap < DBL && rd < R) { xa_read1<NC>(n, rd, pX, pW); ++rd; } } ++gap; SB0(); } while (0)
#pragma unroll
    for (int c = 0; c < NC; ++c) {
        const bf16x8 w = cat8(f.w[c][0], f.w[c][1]);
#pragma unroll
        for (int t = 0; t < 2; ++t) {
            if (TR) MMA(A.a[t][c], w, f.x[t]); else MMA(A.a[t][c], f.x[t], w);
            XA_GAP();
        }
        if ((c & 3) == M) {
            if (TR) MMA(A.cs[c >> 2], w, ones); else MMA(A.cs[c >> 2], ones, w);
            XA_GAP();
        }
    }
    if (XST) {
#pragma unroll
        for (int t = 0; t < 2; ++t) { MMA(A.sx[t], ones, f.x[t]); XA_GAP(); MMA(A.sq[t], f.x[t], f.x[t]); XA_GAP(); }
    }
#pragma unroll
    for (int k = 0; k < 4; ++k) if (LOAD && rd < R) { xa_read1<NC>(n, rd, pX, pW); ++rd; }
    SB0();
#undef XA_GAP
}
template <int NC, bool XST> __device__ __forceinline__ void xw_zero(XWAcc<NC, XST>& A) {
    const f32x4 z = {0.f, 0.f, 0.f, 0.f};
#pragma unroll
    for (int t = 0; t < 2; ++t) {
#pragma unroll
        for (int c = 0; c < NC; ++c) A.a[t][c] = z;
        A.sx[t] = z; A.sq[t] = z;
    }
    A.cs[0] = z; A.cs[1] = z; A.cs[2] = z;
}
template <int NC, bool TR, bool XST, int NST, int M, class Hook>
__device__ __forceinline__ void xa_gemm(XWAcc<NC, XST>& A, LAS char* lds, int g0, int lane, Hook hook) {
    const int fr = lane & 15, g = lane >> 4, row = 32 * M + fr;
    constexpr int G = 2 * NC + (NC - M + 3) / 4 + (XST ? 4 : 0), R = 2 + 2 * NC;
    constexpr int DA = R > G - 7 ? R - (G - 7) : 0, DB = R > G ? R - G : 0;
    LAS char* pX0 = lds + row * 128 + ((g ^ ((row >> 1) & 7)) << 4);
    LAS char* pX1 = lds + ((row * 128 + ((g ^ ((row >> 1) & 7)) << 4)) ^ 64);
    LAS char* pW0 = lds + 16384 + (16 * (g >> 1) + 4 * (g & 1) + (fr >> 2)) * (NC * 32) + (lane & 3) * 8;
    LAS char* pW1 = pW0 + 32 * NC * 32;
    XF<NC> f0, f1;
    bf16x8 ones = {0x3F80, 0x3F80, 0x3F80, 0x3F80, 0x3F80, 0x3F80, 0x3F80, 0x3F80};
    asm volatile("" : "+v"(ones));
    {
        const unsigned sb = (g0 & 3) * SLOT;
#pragma unroll
        for (int i = 0; i < R; ++i) xa_read1<NC>(f0, i, pX0 + sb, pW0 + sb);
    }
    asm volatile("s_nop 1"); SB0();
#pragma unroll 1
    for (int s = 0; s < NST - 1; ++s) {
        const unsigned sb = ((g0 + s) & 3) * SLOT, sn = ((g0 + s + 1) & 3) * SLOT;
        LAS char* nX = pX0 + sn; LAS char* nW = pW0 + sn;
        xa_step<NC, TR, XST, M, DA, true>(A, f0, f1, pX1 + sb, pW1 + sb, ones);
        asm volatile("" : "+v"(nX), "+v"(nW));
        WAIT_L0(); BAR(); SB0();
        hook(s);
        xa_step<NC, TR, XST, M, DB, true>(A, f1, f0, nX, nW, ones);
    }
    const unsigned sb = ((g0 + NST - 1) & 3) * SLOT;
    xa_step<NC, TR, XST, M, DA, true>(A, f0, f1, pX1 + sb, pW1 + sb, ones);
    WAIT_L0(); BAR(); SB0();
    xa_step<NC, TR, XST, M, 0, false>(A, f1, f0, pX0, pW0, ones);
    MMA_DRAIN();
}
template <int NC, bool TR, bool XST, int NST, class Hook>
__device__ __forceinline__ void xw_gemm(XWAcc<NC, XST>& A, LAS char* lds, int g0, int m, int lane, Hook hook) {
    if (m == 0) xa_gemm<NC, TR, XST, NST, 0>(A, lds, g0, lane, hook);
    else if (m == 1) xa_gemm<NC, TR, XST, NST, 1>(A, lds, g0, lane, hook);
    else if (m == 2) xa_gemm<NC, TR, XST, NST, 2>(A, lds, g0, lane, hook);
    else xa_gemm<NC, TR, XST, NST, 3>(A, lds, g0, lane, hook);
}
template <int NC, bool TR, bool XST> __device__ __forceinline__ void share_colsum(const XWAcc<NC, XST>& A, LAS char* lds, int m, int lane) {
    LAS float* tab = (LAS float*)(lds + SP_CS);
    const int fr = lane & 15, g = lane >> 4;
#pragma unroll
    for (int k = 0; k < 3; ++k) {
        const int c = m + 4 * k;
        if (c < NC) {
            if (TR) { if (fr == 0) *(LAS f32x4*)(tab + c * 16 + 4 * g) = A.cs[k]; }
            else { if (g == 0) tab[c * 16 + fr] = A.cs[k][0]; }
        }
    }
    WAIT_L0(); BAR();
}

struct MFrag { bf16x8 w[2]; bf16x8 z[9]; };
template <bool LOAD> __device__ __forceinline__ void m_step(f32x4 (&acc)[2][9], const MFrag& f, MFrag& n, LAS char* pW, LAS char* pZ) {
#pragma unroll
    for (int i = 0; i < 18; ++i) {
        MMA(acc[i & 1][i >> 1], f.z[i >> 1], f.w[i & 1]);
        if (LOAD) { if (i < 2) n.w[i] = lds_b128(pW + i * 2048); else if (i < 11) n.z[i - 2] = lds_b128(pZ + (i - 2) * 1024); }
        SB0();
    }
}
template <int NST> __device__ __forceinline__ void main_gemm(f32x4 (&acc)[2][9], LAS char* lds, int g0, int m, int lane) {
    const int fr = lane & 15, g = lane >> 4, row = 32 * m + fr;
    LAS char* pW0 = lds + row * 128 + ((g ^ ((row >> 1) & 7)) << 4);
    LAS char* pW1 = lds + ((row * 128 + ((g ^ ((row >> 1) & 7)) << 4)) ^ 64);
    LAS char* pZ0 = lds + 16384 + lane * 16;
    LAS char* pZ1 = pZ0 + 9216;
#pragma unroll
    for (int t = 0; t < 2; ++t)
#pragma unroll
        for (int c = 0; c < 9; ++c) acc[t][c] = (f32x4){0.f, 0.f, 0.f, 0.f};
    MFrag f0, f1;
    {
        const unsigned sb = (g0 & 3) * SLOT;
#pragma unroll
        for (int i = 0; i < 2; ++i) f0.w[i] = lds_b128(pW0 + sb + i * 2048);
#pragma unroll
        for (int c = 0; c < 9; ++c) f0.z[c] = lds_b128(pZ0 + sb + c * 1024);
    }
    asm volatile("s_nop 1"); SB0();
#pragma unroll 1
    for (int s = 0; s < NST - 1; ++s) {
        const unsigned sb = ((g0 + s) & 3) * SLOT, sn = ((g0 + s + 1) & 3) * SLOT;
        LAS char* nW = pW0 + sn; LAS char* nZ = pZ0 + sn;
        m_step<true>(acc, f0, f1, pW1 + sb, pZ1 + sb);
        asm volatile("" : "+v"(nW), "+v"(nZ));
        WAIT_L0(); BAR(); SB0();
        m_step<true>(acc, f1, f0, nW, nZ);
    }
    const unsigned sb = ((g0 + NST - 1) & 3) * SLOT;
    m_step<true>(acc, f0, f1, pW1 + sb, pZ1 + sb);
    WAIT_L0(); BAR(); SB0();
    m_step<false>(acc, f1, f0, pW0, pZ0);
    MMA_DRAIN();
}

struct EFrag { s16x4 w[2]; bf16x8 b[5]; };
template <bool LOAD, bool MM> __device__ __forceinline__ void e_step(f32x4 (&acc)[5], const EFrag& f, EFrag& n, LAS char* pW, LAS char* pB) {
    const bf16x8 w = cat8(f.w[0], f.w[1]);
#pragma unroll
    for (int t = 0; t < 5; ++t) {
        if (MM) MMA(acc[t], w, f.b[t]);
        if (LOAD) {
            if (t == 0) { n.w[0] = lds_tr1(pW); n.w[1] = lds_tr1(pW + 2048); }
            else if (t == 1) { n.b[0] = lds_b128(pB); n.b[1] = lds_b128(pB + 1024); }
            else n.b[t] = lds_b128(pB + t * 1024);
        }
        SB0();
    }
}
template <int NST, int NKS, bool MORE, class Hook> __device__ __forceinline__ void e_gemm(f32x4 (&acc)[5], LAS char* lds, int g0, int w, int lane, Hook hook) {
    const int g = lane >> 4, q = (lane & 15) >> 2;
    LAS char* pW = lds + (4 * g + q) * 128 + ((w ^ ((2 * g + (q >> 1)) & 3)) << 5) + (lane & 3) * 8;
    LAS char* pB = lds + 16384 + lane * 16;
#pragma unroll
    for (int t = 0; t < 5; ++t) acc[t] = (f32x4){0.f, 0.f, 0.f, 0.f};
    EFrag f0, f1;
    e_step<true, false>(acc, f1, f0, pW + (g0 & 3) * SLOT, pB + (g0 & 3) * SLOT);
    asm volatile("s_nop 1"); SB0();
#pragma unroll 1
    for (int s = 0; s < NST - 1; ++s) {
        const unsigned sb = ((g0 + s) & 3) * SLOT, sn = ((g0 + s + 1) & 3) * SLOT;
        e_step<true, true>(acc, f0, f1, pW + sb + 4096, pB + sb + 5120);
        e_step<true, true>(acc, f1, f0, pW + sb + 8192, pB + sb + 10240);
        e_step<true, true>(acc, f0, f1, pW + sb + 12288, pB + sb + 15360);
        WAIT_L0(); BAR(); SB0();
        hook(s);
        e_step<true, true>(acc, f1, f0, pW + sn, pB + sn);
    }
    const unsigned sb = ((g0 + NST - 1) & 3) * SLOT;
    constexpr int LAST = NKS - 4 * (NST - 1);
    e_step<true, true>(acc, f0, f1, pW + sb + 4096, pB + sb + 5120);
    if (LAST == 4) {
        e_step<true, true>(acc, f1, f0, pW + sb + 8192, pB + sb + 10240);
        e_step<true, true>(acc, f0, f1, pW + sb + 12288, pB + sb + 15360);
    }
    if (MORE) { WAIT_L0(); BAR(); SB0(); }
    e_step<false, true>(acc, f1, f0, pW, pB);
    MMA_DRAIN();
}

__device__ __forceinline__ float dot2(unsigned a, unsigned b, float c) { return __builtin_amdgcn_fdot2_f32_bf16(__builtin_bit_cast(bf16x2_t, a), __builtin_bit_cast(bf16x2_t, b), c, false); }
__device__ __forceinline__ unsigned at_region(int r, int p) { return (unsigned)((r >> 1) * SLOT + ((r & 1) ? 16384 + WImg<9>::first(p) * 1024 : p * 4096)); }
__device__ __forceinline__ unsigned at_row(int rho, int p) { const int r = rho / 14; return at_region(r, p) + (unsigned)(rho - r * 14) * 288u; }
__device__ __forceinline__ void attn_issue(rsrc_t rQ, LAS char* lds, int n, int p, int lane, int half) {
#pragma unroll
    for (int rr = 0; rr < 2; ++rr)
#pragma unroll
        for (int q = 0; q < 4; ++q) {
            const int r = 2 * half + rr, c = q * 64 + lane, lr = c / 18, cc = c - lr * 18, rho = r * 14 + lr;
            int t, seg;
            if (rho < 22) { t = 8 * n - 14 + rho; seg = 1; } else if (rho < 44) { t = 8 * n - 14 + (rho - 22); seg = 2; } else { t = 8 * n + (rho - 44); seg = 0; }
            const unsigned voff = (t < 0 || rho >= 52 || lr >= 14) ? 0x7ffffff0u : (unsigned)(t * (QW * 2) + (seg * D_ + 144 * p) * 2 + cc * 16);
            dma16_dev(rQ, lds + at_region(r, p) + q * 1024, voff, 0);
        }
}
__device__ __forceinline__ void attn_compute(LAS char* lds, rsrc_t rO, int n, int p, int lane) {
    const int i = lane >> 3, hh = lane & 7, t = 8 * n + i;
    unsigned qv[9];
    {
        LAS unsigned* q = (LAS unsigned*)(lds + at_row(44 + i, p) + hh * 36);
#pragma unroll
        for (int d = 0; d < 9; ++d) qv[d] = q[d];
    }
    float lg[8];
#pragma unroll
    for (int w = 0; w < 8; ++w) {
        LAS unsigned* kp = (LAS unsigned*)(lds + at_row(14 + i - 2 * w, p) + hh * 36);
        float s = 0.f;
#pragma unroll
        for (int d = 0; d < 9; ++d) s = dot2(qv[d], kp[d], s);
        lg[w] = (t - 2 * w >= 0) ? s * 0.23570226039551584f : -1e9f;
    }
    float mx = lg[0];
#pragma unroll
    for (int w = 1; w < 8; ++w) mx = fmaxf(mx, lg[w]);
    float sum = 0.f;
#pragma unroll
    for (int w = 0; w < 8; ++w) { lg[w] = __builtin_amdgcn_exp2f((lg[w] - mx) * 1.4426950408889634f); sum += lg[w]; }
    const float inv = __builtin_amdgcn_rcpf(sum);
    float acc[18];
#pragma unroll
    for (int d = 0; d < 18; ++d) acc[d] = 0.f;
#pragma unroll
    for (int w = 0; w < 8; ++w) {
        LAS unsigned* vp = (LAS unsigned*)(lds + at_row(22 + 14 + i - 2 * w, p) + hh * 36);
        const float pw = lg[w] * inv;
#pragma unroll
        for (int d = 0; d < 9; ++d) { const unsigned v = vp[d]; acc[2 * d] += pw * bflo(v); acc[2 * d + 1] += pw * bfhi(v); }
    }
    LAS char* ob = lds + SP_AO + p * 2304;
#pragma unroll
    for (int d = 0; d < 9; ++d) *(LAS unsigned*)(ob + i * 288 + hh * 36 + d * 4) = pack_bf16(acc[2 * d], acc[2 * d + 1]);
    WAIT_L0();
    {
        const int r = lane & 7, g = (lane >> 3) & 3, tt = n >> 1, rl = (8 * n + r) & 15;
#pragma unroll
        for (int q = 0; q < 2; ++q) {
            const int a = (lane >> 5) + 2 * q;
            const u32x2 lo = *(LAS u32x2*)(ob + r * 288 + (32 * a + 4 * g) * 2), hi = *(LAS u32x2*)(ob + r * 288 + (32 * a + 16 + 4 * g) * 2);
            st128_dev(rO, ((tt * 18 + 4 * p + a) * 64 + rl + 16 * g) * 16, (u32x4){lo.x, lo.y, hi.x, hi.y});
        }
        const u32x2 v = *(LAS u32x2*)(ob + r * 288 + (128 + 4 * g) * 2);
        if (lane < 32) st64_dev(rO, ((tt * 18 + 16 + (p >> 1)) * 64 + rl + 16 * g) * 16 + (p & 1) * 8, v);
    }
}

__global__ __launch_bounds__(512, 2) void fused_kernel(Args a) {
    extern __shared__ __attribute__((aligned(16))) char smem[];
    LAS char* lds = (LAS char*)smem;
    const int wid = __builtin_amdgcn_readfirstlane(threadIdx.x >> 6), lane = threadIdx.x & 63;
    const int xcd = blockIdx.x & 7, loc = blockIdx.x >> 3;
    const int ti = (xcd & 1) * 8 + (loc & 7), tj = (xcd >> 1) * 4 + (loc >> 3);
    const int nt = xcd * 30 + loc;
    const bool fin = loc < 30 && nt < 234;
    const int rb = nt / 9, cb = nt - rb * 9;
    char* ctl = a.ws + WS_CTL;
    u64 tag;
    {
        const u64 e = __hip_atomic_load((const GAS u64*)(ctl + CTL_EP), __ATOMIC_RELAXED, __HIP_MEMORY_SCOPE_AGENT);
        const u64 tg = e * 0x9E3779B97F4A7C15ull + 0xD1B54A32D192ED03ull;
        tag = ((u64)(unsigned)__builtin_amdgcn_readfirstlane((unsigned)(tg >> 32)) << 32) | (unsigned)__builtin_amdgcn_readfirstlane((unsigned)tg);
    }
    const int an = ti * 16 + tj;

    if (wid >= 4) {
        const int p = wid - 4;
        ST_DECL; ST(0);
#define rX mk_rsrc(a.x, S_ * D_ * 2)
#define rQW mk_rsrc(a.wqkv, D_ * QW * 2)
#define rW mk_rsrc(a.win, D_ * F_ * 2)
#define rS mk_rsrc(a.wsp, S_ * S_ * 2)
#define rZ mk_rsrc(a.ws + WS_Z2T, S_ * FH * 2)
#define rQ mk_rsrc(a.ws + WS_QKV, S_ * QW * 2)
#define rO mk_rsrc(a.ws + WS_OF, S_ * D_ * 2)
        RowsImg xi; xi.init(lane, D_ * 2);
        RowsImg si; si.init(lane, S_ * 2);
        WImg<7> wq; wq.init(lane, p, QW * 2);
        WImg<9> wz; wz.init(lane, p, F_ * 2);
        const unsigned sX = (unsigned)(ti * 128 + 32 * p) * (D_ * 2), sQ = (unsigned)((tj * 27) >> 2) * 32, sZ1 = (unsigned)(tj * 144) * 2, sZ2 = sZ1 + FH * 2;
        const unsigned sS = (unsigned)(ti * 128 + 32 * p) * (S_ * 2), sZ = (unsigned)tj * (64u * 9216u);
        const unsigned vl = lane * 16;
        const unsigned vE = (lane >> 3) * (D_ * 2) + ((((lane & 7) >> 1) ^ ((lane >> 4) & 3)) << 5) + (lane & 1) * 16;
        const int zI0 = WImg<9>::first(p);
        auto iss_q = [&](int g) {
            LAS char* slot = lds + (g & 3) * SLOT;
            xi.issue(rX, slot + p * 4096, sX + g * 128, D_ * 2); wq.issue(rQW, slot + 16384, sQ + g * 64 * (QW * 2), p);
        };
        auto iss_z = [&](int g, int s, unsigned col) {
            LAS char* slot = lds + (g & 3) * SLOT;
            xi.issue(rX, slot + p * 4096, sX + s * 128, D_ * 2); wz.issue(rW, slot + 16384, col + s * 64 * (F_ * 2), p);
        };
        auto iss_m = [&](int g) {
            LAS char* slot = lds + (g & 3) * SLOT; const int s = g - G_MAIN;
            si.issue(rS, slot + p * 4096, sS + s * 128, S_ * 2);
#pragma unroll
            for (int q = 0; q < 4; ++q) dma16_dev(rZ, slot + 16384 + (zI0 + q) * 1024, vl, sZ + s * 18432 + (zI0 + q) * 1024);
            if (p < 2) dma16_dev(rZ, slot + 16384 + (zI0 + 4) * 1024, vl, sZ + s * 18432 + (zI0 + 4) * 1024);
        };
        unsigned eT[5];
#pragma unroll
        for (int t = 0; t < 5; ++t) eT[t] = (unsigned)__builtin_amdgcn_readfirstlane(min(rb * 5 + t, 127));
        auto iss_e = [&](int g, bool isS) {
            LAS char* slot = lds + (g & 3) * SLOT;
            const int lim = isS ? 16 : 64, nks = isS ? 18 : 72;
            int ks = 4 * (g - (isS ? G_S : G_G)) + p; if (isS && ks > 17) ks = 17;
            const int c0 = ks < lim ? 9 * (ks >> 2) + 2 * (ks & 3) : 18 * (ks - lim) + 8, c1 = ks < lim ? c0 + 1 : c0 + 9;
            const rsrc_t rw = mk_rsrc(isS ? a.wo : a.wout, isS ? D_ * D_ * 2 : FH * D_ * 2), rb_ = mk_rsrc(a.ws + (isS ? WS_OF : WS_HF), isS ? S_ * D_ * 2 : S_ * FH * 2);
#pragma unroll
            for (int q = 0; q < 4; ++q) dma16(rw, slot + p * 4096 + q * 1024, vE, (unsigned)((q < 2 ? c0 : c1) * 16 + 8 * (q & 1)) * (D_ * 2) + cb * 128);
#pragma unroll
            for (int t = 0; t < 5; ++t) dma16_dev(rb_, slot + 16384 + p * 5120 + t * 1024, vl, (eT[t] * nks + ks) * 1024);
        };
#define PSTEP(K, PH) do { WAIT_V(7 * (K)); BAR(); } while (0)
#define SPIN_UNTIL(cond) do { unsigned spins_ = 0; while (!(cond) && ++spins_ < SPIN_CAP) __builtin_amdgcn_s_sleep(2); asm volatile("" ::: "memory"); } while (0)
        iss_q(0); iss_q(1); iss_q(2); iss_q(3);
        PSTEP(3, 0);
#pragma unroll 1
        for (int g = 1; g <= 5; ++g) { PSTEP(2, 0); iss_q(g + 3); }
#pragma unroll 1
        for (int g = 6; g <= 14; ++g) { PSTEP(2, 1); iss_z(g + 3, g + 3 - G_Z2, sZ2); if (g == G_Z2) BAR(); }
        PSTEP(2, 1); iss_z(18, 0, sZ1);
        PSTEP(2, 1); iss_z(19, 1, sZ1);
        PSTEP(2, 1);
        {
            const int pa = (8 * an - 14) >> 7;
            SPIN_UNTIL(flags_ready(ctl + CTL_QF + ti * 512, 64, ctl + CTL_QF + (pa < 0 ? ti : pa) * 512, 64, tag, lane));
            attn_issue(rQ, lds, an, p, lane, 0);
        }
        PSTEP(1, 2);
        attn_issue(rQ, lds, an, p, lane, 1);
        BAR();
        ST(1);
        WAIT_V(0);
        attn_compute(lds, rO, an, p, lane);
        ST(2);
        iss_z(20, 2, sZ1); iss_z(21, 3, sZ1);
        publish(ctl, CTL_OF + (an * 4 + p) * 8, tag, lane);
        ST(3);
#pragma unroll 1
        for (int g = 19; g <= 23; ++g) { PSTEP(2, 2); iss_z(g + 3, g + 3 - G_Z1, sZ1); }
        PSTEP(2, 2); PSTEP(1, 2); PSTEP(0, 2);
        SPIN_UNTIL(flags_ready(ctl + CTL_ZF + tj * 512, 64, ctl, 0, tag, lane));
        iss_m(27); iss_m(28); iss_m(29);
        PSTEP(2, 3); iss_m(30); BAR();
#pragma unroll 1
        for (int g = 28; g <= 55; ++g) { PSTEP(2, 3); iss_m(g + 3); }
        if (!fin) { PSTEP(2, 3); PSTEP(1, 3); PSTEP(0, 3); BAR(); }
        else {
            PSTEP(2, 3);
            SPIN_UNTIL(flags_ready(ctl + CTL_OF + rb * 320, rb == 25 ? 24 : 40, ctl, 0, tag, lane));
            iss_e(59, true);
#pragma unroll 1
            for (int g = 57; g <= 60; ++g) { PSTEP(2, 4); iss_e(g + 3, true); }
            PSTEP(2, 4); PSTEP(1, 4); PSTEP(0, 4);
            {
                const int p0 = (rb * 80) >> 7, p1 = min(rb * 80 + 79, S_ - 1) >> 7;
                SPIN_UNTIL(flags_ready(ctl + CTL_HF + p0 * 512, 64, ctl + CTL_HF + p1 * 512, 64, tag, lane));
            }
            iss_e(64, false); iss_e(65, false); iss_e(66, false);
#pragma unroll 1
            for (int g = 64; g <= 78; ++g) { PSTEP(2, 5); iss_e(g + 3, false); }
            PSTEP(2, 5); PSTEP(1, 5); PSTEP(0, 5);
        }
        ST_END(1);
        return;
    }

    const int m = wid, fr = lane & 15, g4 = lane >> 4;
    ST_DECL; ST(0);
    const rsrc_t rQs = mk_rsrc(a.ws + WS_QKV, S_ * QW * 2), rZs = mk_rsrc(a.ws + WS_Z2T, S_ * FH * 2), rHs = mk_rsrc(a.ws + WS_HF, S_ * FH * 2), rSt = mk_rsrc(a.ws + WS_ST, S_ * 16 * 16);
    LAS float* cst = (LAS float*)(lds + SP_CS);
    float xm[2], xr[2];
    BAR();
    ST(1);
    {
        XWAcc<7, true> A; xw_zero(A);
        xw_gemm<7, true, true, 9>(A, lds, G_QKV, m, lane, NoHook());
        ST(2);
        share_colsum<7, true, true>(A, lds, m, lane);
#pragma unroll
        for (int t = 0; t < 2; ++t) {
            const int r3 = fr & 3;
            const float dq = r3 == 0 ? A.sq[t][0] : r3 == 1 ? A.sq[t][1] : r3 == 2 ? A.sq[t][2] : A.sq[t][3];
            const float sq = __shfl(dq, (fr >> 2) * 16 + fr);
            xm[t] = A.sx[t][0] * (1.0f / D_);
            xr[t] = 1.0f / sqrtf(fmaxf(sq * (1.0f / D_) - xm[t] * xm[t], 0.f) + 1e-5f);
        }
        const int c0 = (tj * 27) >> 2, dup = tj > 0 && c0 < (((tj - 1) * 27) >> 2) + 7;
#pragma unroll
        for (int t = 0; t < 2; ++t) {
            unsigned pk[7][2];
#pragma unroll
            for (int c = 0; c < 7; ++c) {
                const f32x4 cs = *(LAS f32x4*)(cst + c * 16 + 4 * g4);
                pk[c][0] = pack2((lo2(A.a[t][c]) - lo2(cs) * xm[t]) * xr[t]); pk[c][1] = pack2((hi2(A.a[t][c]) - hi2(cs) * xm[t]) * xr[t]);
            }
            const unsigned ro = (unsigned)(ti * 128 + 32 * m + 16 * t + fr) * (QW * 2) + c0 * 32;
#pragma unroll
            for (int pp = 0; pp < 3; ++pp) st128_dev(rQs, ro + (1 + 2 * pp) * 32 + g4 * 16, gather8(pk[1 + 2 * pp][0], pk[1 + 2 * pp][1], pk[2 + 2 * pp][0], pk[2 + 2 * pp][1]));
            if (!dup) st64_dev(rQs, ro + g4 * 8, (u32x2){pk[0][0], pk[0][1]});
        }
    }
    ST(3);
    unsigned zp[2][9][2];
    {
        XWAcc<9, false> A; xw_zero(A);
        xw_gemm<9, false, false, 9>(A, lds, G_Z2, m, lane, [&](int s) { if (s == 1) publish(ctl, CTL_QF + ((ti * 16 + tj) * 4 + m) * 8, tag, lane); });
        ST(4);
        share_colsum<9, false, false>(A, lds, m, lane);
        f32x2 rm[2][2], rr[2][2];
#pragma unroll
        for (int t = 0; t < 2; ++t)
#pragma unroll
            for (int h = 0; h < 2; ++h) {
                rm[t][h] = (f32x2){__shfl(xm[t], 4 * g4 + 2 * h), __shfl(xm[t], 4 * g4 + 2 * h + 1)};
                rr[t][h] = (f32x2){__shfl(xr[t], 4 * g4 + 2 * h), __shfl(xr[t], 4 * g4 + 2 * h + 1)};
            }
        f32x2 s1[2][2], s2[2][2];
#pragma unroll
        for (int t = 0; t < 2; ++t)
#pragma unroll
            for (int h = 0; h < 2; ++h) { s1[t][h] = (f32x2){0.f, 0.f}; s2[t][h] = (f32x2){0.f, 0.f}; }
#pragma unroll
        for (int c = 0; c < 9; ++c) {
            const float cs = cst[c * 16 + fr];
#pragma unroll
            for (int t = 0; t < 2; ++t)
#pragma unroll
                for (int h = 0; h < 2; ++h) {
                    const f32x2 v = gelu2(((h ? hi2(A.a[t][c]) : lo2(A.a[t][c])) - rm[t][h] * cs) * rr[t][h]);
                    s1[t][h] += v; s2[t][h] += v * v;
                    zp[t][c][h] = pack2(v);
                    asm volatile("" : "+v"(zp[t][c][h]));
                }
        }
        float mm = 0.f, qq = 0.f;
        {
            const int idx = lane & 7;
            float a1[8], a2[8];
#pragma unroll
            for (int k = 0; k < 8; ++k) { a1[k] = s1[k >> 2][(k >> 1) & 1][k & 1]; a2[k] = s2[k >> 2][(k >> 1) & 1][k & 1]; }
#pragma unroll
            for (int k = 0; k < 8; ++k) { a1[k] = dpp_add<0x128>(a1[k]); a2[k] = dpp_add<0x128>(a2[k]); }
#pragma unroll
            for (int k = 0; k < 8; ++k) { a1[k] = dpp_add<0x124>(a1[k]); a2[k] = dpp_add<0x124>(a2[k]); }
#pragma unroll
            for (int k = 0; k < 8; ++k) { a1[k] = dpp_add<0x122>(a1[k]); a2[k] = dpp_add<0x122>(a2[k]); }
#pragma unroll
            for (int k = 0; k < 8; ++k) { a1[k] = dpp_add<0x121>(a1[k]); a2[k] = dpp_add<0x121>(a2[k]); }
            float u1 = a1[0], u2 = a2[0];
#pragma unroll
            for (int k = 1; k < 8; ++k) { u1 = idx == k ? a1[k] : u1; u2 = idx == k ? a2[k] : u2; }
            mm = u1 * (1.0f / 144.0f);
            qq = fmaxf(u2 - u1 * mm, 0.f);
            if (fr < 8) st128_dev(rSt, ((ti * 16 + tj) * 128 + 32 * m + 16 * (idx >> 2) + 4 * g4 + (idx & 3)) * 16,
                                  (u32x4){__float_as_uint(mm), (unsigned)tag, __float_as_uint(qq), (unsigned)(tag >> 32)});
        }
    }
    ST(5);
    unsigned z1p[2][9][2];
    {
        ST(6);
        float mu, rs;
        {
            const unsigned off = ((ti * 16 + (lane >> 5) * 8) * 128 + 32 * m + (lane & 31)) * 16;
            u32x4 v[8]; unsigned spins = 0;
            for (;;) {
#pragma unroll
                for (int k = 0; k < 8; ++k) v[k] = __builtin_amdgcn_raw_buffer_load_b128(rSt, off + k * 2048, 0, 16);
                unsigned bad = 0;
#pragma unroll
                for (int k = 0; k < 8; ++k) bad |= (v[k].y ^ (unsigned)tag) | (v[k].w ^ (unsigned)(tag >> 32));
                if (__all(bad == 0) || ++spins > SPIN_CAP) break;
                __builtin_amdgcn_s_sleep(2);
            }
            float s = 0.f;
#pragma unroll
            for (int k = 0; k < 8; ++k) s += __uint_as_float(v[k].x);
            s += xchg32(s);
            mu = s * (1.0f / 16.0f);
            float q = 0.f;
#pragma unroll
            for (int k = 0; k < 8; ++k) { const float d = __uint_as_float(v[k].x) - mu; q += __uint_as_float(v[k].z) + 144.0f * d * d; }
            q += xchg32(q);
            rs = 1.0f / sqrtf(q * (1.0f / FH) + 1e-5f);
        }
        ST(7);
        f32x2 mr[2][2], rr[2][2];
#pragma unroll
        for (int t = 0; t < 2; ++t)
#pragma unroll
            for (int h = 0; h < 2; ++h) {
                const int src = 16 * t + 4 * g4 + 2 * h;
                mr[t][h] = (f32x2){__shfl(mu, src), __shfl(mu, src + 1)}; rr[t][h] = (f32x2){__shfl(rs, src), __shfl(rs, src + 1)};
            }
        const unsigned zo = (unsigned)((tj * 64 + ti * 4 + m) * 9) * 1024 + lane * 16;
#pragma unroll
        for (int c = 0; c < 9; ++c) {
            const unsigned x0 = pack2((unpack2(zp[0][c][0]) - mr[0][0]) * rr[0][0]), x1 = pack2((unpack2(zp[0][c][1]) - mr[0][1]) * rr[0][1]);
            const unsigned y0 = pack2((unpack2(zp[1][c][0]) - mr[1][0]) * rr[1][0]), y1 = pack2((unpack2(zp[1][c][1]) - mr[1][1]) * rr[1][1]);
            st128_dev(rZs, zo + c * 1024, gather8(x0, x1, y0, y1));
        }
        ST(8);
        XWAcc<9, false> A; xw_zero(A);
        xw_gemm<9, true, false, 9>(A, lds, G_Z1, m, lane, [&](int s) { if (s == 0) publish(ctl, CTL_ZF + ((tj * 16 + ti) * 4 + m) * 8, tag, lane); });
        ST(9);
        share_colsum<9, true, false>(A, lds, m, lane);
#pragma unroll
        for (int c = 0; c < 9; ++c) {
            const f32x4 cs = *(LAS f32x4*)(cst + c * 16 + 4 * g4);
#pragma unroll
            for (int t = 0; t < 2; ++t) {
                z1p[t][c][0] = pack2(gelu2((lo2(A.a[t][c]) - lo2(cs) * xm[t]) * xr[t])); z1p[t][c][1] = pack2(gelu2((hi2(A.a[t][c]) - hi2(cs) * xm[t]) * xr[t]));
            }
        }
#pragma unroll
        for (int t = 0; t < 2; ++t)
#pragma unroll
            for (int c = 0; c < 9; ++c) asm volatile("" : "+v"(z1p[t][c][0]), "+v"(z1p[t][c][1]));
    }
    ST(10);
    {
        f32x4 acc[2][9];
        float bt[2];
#pragma unroll
        for (int t = 0; t < 2; ++t) bt[t] = bf2f(a.bsp[ti * 128 + 32 * m + 16 * t + fr]);
        main_gemm<32>(acc, lds, G_MAIN, m, lane);
        ST(11);
#pragma unroll
        for (int t = 0; t < 2; ++t) {
            const unsigned hp = (unsigned)(ti * 8 + 2 * m + t) * 72 * 1024 + lane * 16;
#pragma unroll
            for (int pp = 0; pp < 4; ++pp) {
                u32x4 o;
                o.x = pack2((lo2(acc[t][2 * pp]) + bt[t]) * unpack2(z1p[t][2 * pp][0])); o.y = pack2((hi2(acc[t][2 * pp]) + bt[t]) * unpack2(z1p[t][2 * pp][1]));
                o.z = pack2((lo2(acc[t][2 * pp + 1]) + bt[t]) * unpack2(z1p[t][2 * pp + 1][0])); o.w = pack2((hi2(acc[t][2 * pp + 1]) + bt[t]) * unpack2(z1p[t][2 * pp + 1][1]));
                st128_dev(rHs, hp + (tj * 4 + pp) * 1024, o);
            }
            u32x2 o;
            o.x = pack2((lo2(acc[t][8]) + bt[t]) * unpack2(z1p[t][8][0])); o.y = pack2((hi2(acc[t][8]) + bt[t]) * unpack2(z1p[t][8][1]));
            st64_dev(rHs, hp + (64 + (tj >> 1)) * 1024 + (tj & 1) * 8, o);
        }
    }
    ST(12);
    const size_t hflag = CTL_HF + ((ti * 16 + tj) * 4 + m) * 8;
    if (!fin) { publish(ctl, hflag, tag, lane); ST_END(0); return; }
    {
        f32x4 sa[5], ga[5];
        e_gemm<5, 18, true>(sa, lds, G_S, m, lane, [&](int s) { if (s == 0) publish(ctl, hflag, tag, lane); });
        ST(13);
        u32x2 xv[5];
#pragma unroll
        for (int t = 0; t < 5; ++t) xv[t] = *(const u32x2*)(a.x + (size_t)(min(rb * 5 + t, 127) * 16 + fr) * D_ + cb * 64 + m * 16 + g4 * 4);
        e_gemm<18, 72, false>(ga, lds, G_G, m, lane, NoHook());
        ST(14);
#pragma unroll
        for (int t = 0; t < 5; ++t) {
            if (rb * 5 + t < 128) {
                float o[4];
#pragma unroll
                for (int r = 0; r < 4; ++r) {
                    const float gv = ga[t][r], sg = gv * __builtin_amdgcn_rcpf(1.0f + __builtin_amdgcn_exp2f(gv * -1.4426950408889634f));
                    const float xx = (r & 1) ? bfhi(xv[t][r >> 1]) : bflo(xv[t][r >> 1]);
                    o[r] = xx + sa[t][r] * sg;
                }
                u32x2 ov; ov.x = pack_bf16(o[0], o[1]); ov.y = pack_bf16(o[2], o[3]);
                *(u32x2*)(a.out + (size_t)((rb * 5 + t) * 16 + fr) * D_ + cb * 64 + m * 16 + g4 * 4) = ov;
            }
        }
    }
    if (blockIdx.x == 0 && threadIdx.x == 0) __hip_atomic_store((GAS u64*)(ctl + CTL_EP), tag, __ATOMIC_RELAXED, __HIP_MEMORY_SCOPE_AGENT);
    ST(15);
    ST_END(0);
}

extern "C" void kernel_launch(void* const* d_in, const int* in_sizes, int n_in, void* d_out, int out_size, void* d_ws, size_t ws_size, hipStream_t stream) {
    Args a;
    a.x = (const u16*)d_in[0]; a.wqkv = (const u16*)d_in[1]; a.wo = (const u16*)d_in[2]; a.win = (const u16*)d_in[4]; a.wsp = (const u16*)d_in[5];
    a.bsp = (const u16*)d_in[6]; a.wout = (const u16*)d_in[7]; a.out = (u16*)d_out; a.ws = (char*)d_ws;
    (void)hipFuncSetAttribute((const void*)fused_kernel, hipFuncAttributeMaxDynamicSharedMemorySize, LDS_BYTES);
    fused_kernel<<<256, 512, LDS_BYTES, stream>>>(a);
}
```

```cpp
#include <hip/hip_runtime.h>
#include <hip/hip_bf16.h>
#include <stdint.h>

typedef unsigned short u16;
typedef unsigned long long u64;
typedef short bf16x8 __attribute__((ext_vector_type(8)));
typedef short s16x4 __attribute__((ext_vector_type(4)));
typedef float f32x4 __attribute__((ext_vector_type(4)));
typedef float f32x2 __attribute__((ext_vector_type(2)));
typedef unsigned u32x4 __attribute__((ext_vector_type(4)));
typedef unsigned u32x2 __attribute__((ext_vector_type(2)));
typedef __bf16 bf16x2_t __attribute__((ext_vector_type(2)));
#define LAS __attribute__((address_space(3)))
#define GAS __attribute__((address_space(1)))
typedef LAS void* lds_vp;
typedef __amdgpu_buffer_rsrc_t rsrc_t;

constexpr int S_ = 2048, D_ = 576, F_ = 4608, FH = 2304, QW = 1728;
constexpr int SLOT = 36864, SPARE = 4 * SLOT, LDS_BYTES = 163840;
constexpr int SP_CS = SPARE, SP_PF = SPARE + 1024, SP_AO = SPARE + 4096;
constexpr size_t WS_Z2T = 0;
constexpr size_t WS_QKV = WS_Z2T + (size_t)S_ * FH * 2;
constexpr size_t WS_HF = WS_QKV + (size_t)S_ * QW * 2;
constexpr size_t WS_OF = WS_HF + (size_t)S_ * FH * 2;
constexpr size_t WS_ST = WS_OF + (size_t)S_ * D_ * 2;
constexpr size_t WS_CTL = WS_ST + (size_t)S_ * 16 * 16;
constexpr size_t CTL_EP = 0, CTL_QF = 1024, CTL_ZF = CTL_QF + 8192, CTL_OF = CTL_ZF + 8192, CTL_HF = CTL_OF + 8192, CTL_END = CTL_HF + 8192;
constexpr int G_QKV = 0, G_Z2 = 9, G_Z1 = 18, G_Z1B = 21, G_MAIN = 27, G_S = 59, G_G = 64, G_END = 82;
constexpr unsigned SPIN_CAP = 400000u;

__device__ __forceinline__ float bflo(unsigned v) { return __uint_as_float(v << 16); }
__device__ __forceinline__ float bfhi(unsigned v) { return __uint_as_float(v & 0xffff0000u); }
__device__ __forceinline__ float bf2f(u16 v) { return __uint_as_float(((unsigned)v) << 16); }
__device__ __forceinline__ unsigned pack_bf16(float lo, float hi) {
    f32x2 v = {lo, hi};
    return __builtin_bit_cast(unsigned, __builtin_convertvector(v, bf16x2_t));
}
#define BAR() do { asm volatile("" ::: "memory"); __builtin_amdgcn_s_barrier(); asm volatile("" ::: "memory"); } while (0)
#define WAIT_V(n) asm volatile("s_waitcnt vmcnt(%0)" ::"n"(n) : "memory")
#define WAIT_L0() asm volatile("s_waitcnt lgkmcnt(0)" ::: "memory")
#define SB0() __builtin_amdgcn_sched_barrier(0)
#define ST_DECL
#define ST(k)
#define ST_END(who)

__device__ __forceinline__ rsrc_t mk_rsrc(const void* p, unsigned bytes) { return __builtin_amdgcn_make_buffer_rsrc((void*)p, 0, bytes, 0x00020000); }
__device__ __forceinline__ void dma16(rsrc_t r, LAS char* lds, unsigned voff, unsigned soff) {
    __builtin_amdgcn_raw_ptr_buffer_load_lds(r, (lds_vp)lds, 16, voff, soff, 0, 0);
}
__device__ __forceinline__ void dma16_dev(rsrc_t r, LAS char* lds, unsigned voff, unsigned soff) {
    __builtin_amdgcn_raw_ptr_buffer_load_lds(r, (lds_vp)lds, 16, voff, soff, 0, 16);
}
#define ATT_PRIO 1
#define ST_AUX 16
__device__ __forceinline__ void touch_lines(rsrc_t r, LAS char* scratch, unsigned first, unsigned cnt, int lane) {
    __builtin_amdgcn_raw_ptr_buffer_load_lds(r, (lds_vp)scratch, 4, (unsigned)lane < cnt ? (first + lane) * 128u : 0x7ffffff0u, 0, 0, 0);
}
__device__ __forceinline__ void st128_dev(rsrc_t r, unsigned off, u32x4 v) { __builtin_amdgcn_raw_buffer_store_b128(v, r, off, 0, ST_AUX); }
__device__ __forceinline__ void st64_dev(rsrc_t r, unsigned off, u32x2 v) { __builtin_amdgcn_raw_buffer_store_b64(v, r, off, 0, ST_AUX); }
__device__ __forceinline__ bf16x8 lds_b128(LAS char* p) { return *(LAS bf16x8*)p; }
__device__ __forceinline__ bf16x8 lds_tr(LAS char* p, int off1) {
    s16x4 a = __builtin_amdgcn_ds_read_tr16_b64_v4i16((LAS s16x4*)p);
    s16x4 b = __builtin_amdgcn_ds_read_tr16_b64_v4i16((LAS s16x4*)(p + off1));
    return __builtin_shufflevector(a, b, 0, 1, 2, 3, 4, 5, 6, 7);
}
#define MFMA(a, b, c) __builtin_amdgcn_mfma_f32_16x16x32_bf16(a, b, c, 0, 0, 0)
template <int CTRL> __device__ __forceinline__ float dpp_add(float x) {
    return x + __builtin_bit_cast(float, __builtin_amdgcn_update_dpp(0, __builtin_bit_cast(int, x), CTRL, 0xf, 0xf, false));
}
__device__ __forceinline__ float row16_sum(float x) { x = dpp_add<0x128>(x); x = dpp_add<0x124>(x); x = dpp_add<0x122>(x); x = dpp_add<0x121>(x); return x; }
__device__ __forceinline__ float gelu_f(float x) {
    const float t = x * (-2.3022081498f - 0.1029432397f * x * x);
    return x * __builtin_amdgcn_rcpf(1.0f + __builtin_amdgcn_exp2f(t));
}
__device__ __forceinline__ f32x2 gelu2(f32x2 x) {
    const f32x2 t = x * (x * x * -0.1029432397f + -2.3022081498f);
    f32x2 e; e.x = __builtin_amdgcn_exp2f(t.x); e.y = __builtin_amdgcn_exp2f(t.y);
    const f32x2 d = e + 1.0f;
    f32x2 r; r.x = __builtin_amdgcn_rcpf(d.x); r.y = __builtin_amdgcn_rcpf(d.y);
    return x * r;
}
__device__ __forceinline__ f32x2 lo2(f32x4 v) { return (f32x2){v[0], v[1]}; }
__device__ __forceinline__ f32x2 hi2(f32x4 v) { return (f32x2){v[2], v[3]}; }
__device__ __forceinline__ unsigned pack2(f32x2 v) { return __builtin_bit_cast(unsigned, __builtin_convertvector(v, bf16x2_t)); }
__device__ __forceinline__ f32x2 unpack2(unsigned v) { return (f32x2){__uint_as_float(v << 16), __uint_as_float(v & 0xffff0000u)}; }
__device__ __forceinline__ int swap23(int r) { return (r & 0x13) | ((r & 4) << 1) | ((r & 8) >> 1); }
__device__ __forceinline__ float xchg32(float x) {
    auto r = __builtin_amdgcn_permlane32_swap(__float_as_uint(x), __float_as_uint(x), false, false);
    return __uint_as_float((threadIdx.x & 32) ? r[0] : r[1]);
}
__device__ __forceinline__ u32x4 gather8(unsigned x0, unsigned x1, unsigned y0, unsigned y1) {
    auto s0 = __builtin_amdgcn_permlane32_swap(x0, y0, false, false);
    auto s1 = __builtin_amdgcn_permlane32_swap(x1, y1, false, false);
    auto t0 = __builtin_amdgcn_permlane16_swap(s0[0], s0[1], false, false);
    auto t1 = __builtin_amdgcn_permlane16_swap(s1[0], s1[1], false, false);
    return (u32x4){t0[0], t1[0], t0[1], t1[1]};
}
__device__ __forceinline__ void publish(char* ctl, size_t word, u64 tag, int lane) {
    asm volatile("s_waitcnt vmcnt(0)" ::: "memory");
    if (lane == 0) __hip_atomic_store((GAS u64*)(ctl + word), tag, __ATOMIC_RELAXED, __HIP_MEMORY_SCOPE_AGENT);
}
__device__ __forceinline__ bool flags_ready(const char* f0, int n0, const char* f1, int n1, u64 tag, int lane) {
    u64 a = tag, b = tag;
    if (lane < n0) a = __hip_atomic_load((const GAS u64*)(f0 + lane * 8), __ATOMIC_RELAXED, __HIP_MEMORY_SCOPE_AGENT);
    if (lane < n1) b = __hip_atomic_load((const GAS u64*)(f1 + lane * 8), __ATOMIC_RELAXED, __HIP_MEMORY_SCOPE_AGENT);
    return __all(a == tag && b == tag);
}

struct Args { const u16* x; const u16* wqkv; const u16* wo; const u16* win; const u16* wsp; const u16* bsp; const u16* wout; u16* out; char* ws; };

struct RowsImg {
    unsigned v0, v1;
    __device__ __forceinline__ void init(int lane, unsigned rs) {
        const int r8 = lane >> 3, ch = lane & 7;
        v0 = r8 * rs + ((ch ^ ((lane >> 4) & 7)) << 4); v1 = r8 * rs + ((ch ^ ((4 + (lane >> 4)) & 7)) << 4);
    }
    __device__ __forceinline__ void issue(rsrc_t r, LAS char* dst, unsigned soff, unsigned rs) const {
#pragma unroll
        for (int q = 0; q < 4; ++q) dma16(r, dst + q * 1024, (q & 1) ? v1 : v0, soff + q * 8 * rs);
    }
};
template <int NC> struct WImg {
    static constexpr int CPR = 2 * NC, BIG = (NC == 9) ? 5 : 4;
    unsigned v[BIG];
    static __device__ __forceinline__ int first(int p) { return NC == 9 ? (p < 2 ? 5 * p : 10 + 4 * (p - 2)) : (p < 2 ? 4 * p : 8 + 3 * (p - 2)); }
    __device__ __forceinline__ void init(int lane, int p, unsigned ldb) {
        const int I0 = first(p);
#pragma unroll
        for (int q = 0; q < BIG; ++q) {
            const int n = (I0 + q) * 64 + lane, pos = n / CPR, cc = n - pos * CPR;
            v[q] = ((pos & 32) + swap23(pos & 31)) * ldb + cc * 16;
        }
    }
    __device__ __forceinline__ void issue(rsrc_t r, LAS char* dst, unsigned soff, int p) const {
        const int I0 = first(p);
#pragma unroll
        for (int q = 0; q < BIG - 1; ++q) dma16(r, dst + (I0 + q) * 1024, v[q], soff);
        if (p < 2) dma16(r, dst + (I0 + BIG - 1) * 1024, v[BIG - 1], soff);
    }
};

#define MMA(acc, a, b) asm volatile("v_mfma_f32_16x16x32_bf16 %0, %1, %2, %0" : "+v"(acc) : "v"(a), "v"(b))
#define MMA_DRAIN() do { SB0(); asm volatile("s_nop 7\n\ts_nop 3"); SB0(); } while (0)
struct NoHook { __device__ __forceinline__ void operator()(int) const {} };
__device__ __forceinline__ s16x4 lds_tr1(LAS char* p) { return __builtin_amdgcn_ds_read_tr16_b64_v4i16((LAS s16x4*)p); }
__device__ __forceinline__ bf16x8 cat8(s16x4 a, s16x4 b) { return __builtin_shufflevector(a, b, 0, 1, 2, 3, 4, 5, 6, 7); }

template <int NC, bool XST> struct XWAcc { f32x4 a[2][NC]; f32x4 cs[3]; f32x4 sx[2], sq[2]; };
template <int NC> struct XF { bf16x8 x[2]; s16x4 w[NC][2]; };
template <int NC> struct XRot { int o[3]; };
template <int NC> __device__ __forceinline__ void xa_read1(XF<NC>& f, int i, LAS char* pX, LAS char* pW, const XRot<NC>& rot) {
    if (i < 2) f.x[i] = lds_b128(pX + i * 2048);
    else {
        const int c = (i - 2) >> 1;
        LAS char* q = c >= NC - 3 ? pW + rot.o[c >= NC - 3 ? c - (NC - 3) : 0] : pW;
        f.w[c][i & 1] = lds_tr1(q + c * 32 + (i & 1) * (8 * NC * 32));
    }
}
template <int NC, bool TR, bool XST, int DBL, bool LOAD>
__device__ __forceinline__ void xa_step(XWAcc<NC, XST>& A, const XF<NC>& f, XF<NC>& n, LAS char* pX, LAS char* pW, const XRot<NC>& rot, const bf16x8& ones) {
    constexpr int R = 2 + 2 * NC;
    int rd = 0, gap = 0;
#define XA_GAP() do { if (LOAD) { if (rd < R) { xa_read1<NC>(n, rd, pX, pW, rot); ++rd; } if (gap < DBL && rd < R) { xa_read1<NC>(n, rd, pX, pW, rot); ++rd; } } ++gap; SB0(); } while (0)
#pragma unroll
    for (int c = 0; c < NC; ++c) {
        const bf16x8 w = cat8(f.w[c][0], f.w[c][1]);
#pragma unroll
        for (int t = 0; t < 2; ++t) {
            if (TR) MMA(A.a[t][c], w, f.x[t]); else MMA(A.a[t][c], f.x[t], w);
            XA_GAP();
        }
        if ((c & 3) == 0) {
            if (TR) MMA(A.cs[c >> 2], w, ones); else MMA(A.cs[c >> 2], ones, w);
            XA_GAP();
        }
    }
    if (XST) {
#pragma unroll
        for (int t = 0; t < 2; ++t) { MMA(A.sx[t], ones, f.x[t]); XA_GAP(); MMA(A.sq[t], f.x[t], f.x[t]); XA_GAP(); }
    }
#pragma unroll
    for (int k = 0; k < 4; ++k) if (LOAD && rd < R) { xa_read1<NC>(n, rd, pX, pW, rot); ++rd; }
    SB0();
#undef XA_GAP
}
template <int NC, bool XST> __device__ __forceinline__ void xw_zero(XWAcc<NC, XST>& A) {
    const f32x4 z = {0.f, 0.f, 0.f, 0.f};
#pragma unroll
    for (int t = 0; t < 2; ++t) {
#pragma unroll
        for (int c = 0; c < NC; ++c) { A.a[t][c] = z; asm volatile("" : "+v"(A.a[t][c])); }
        if (XST) { A.sx[t] = z; A.sq[t] = z; asm volatile("" : "+v"(A.sx[t]), "+v"(A.sq[t])); }
    }
    A.cs[0] = z; A.cs[1] = z; asm volatile("" : "+v"(A.cs[0]), "+v"(A.cs[1]));
    if (NC > 8) { A.cs[2] = z; asm volatile("" : "+v"(A.cs[2])); }
}
template <int NC, bool TR, bool XST, int NST, class Hook>
__device__ __forceinline__ void xw_gemm(XWAcc<NC, XST>& A, LAS char* lds, int g0, int m, int lane, Hook hook) {
    const int fr = lane & 15, g = lane >> 4, row = 32 * m + fr;
    constexpr int G = 2 * NC + (NC + 3) / 4 + (XST ? 4 : 0), R = 2 + 2 * NC;
    constexpr int DA = R > G - 7 ? R - (G - 7) : 0, DB = R > G ? R - G : 0;
    LAS char* pX0 = lds + row * 128 + ((g ^ ((row >> 1) & 7)) << 4);
    LAS char* pX1 = lds + ((row * 128 + ((g ^ ((row >> 1) & 7)) << 4)) ^ 64);
    LAS char* pW0 = lds + 16384 + (16 * (g >> 1) + 4 * (g & 1) + (fr >> 2)) * (NC * 32) + (lane & 3) * 8 + m * 32;
    LAS char* pW1 = pW0 + 32 * NC * 32;
    XRot<NC> rot;
#pragma unroll
    for (int k = 0; k < 3; ++k) rot.o[k] = (NC - 3 + k + m >= NC) ? -NC * 32 : 0;
    XF<NC> f0, f1;
    bf16x8 ones = {0x3F80, 0x3F80, 0x3F80, 0x3F80, 0x3F80, 0x3F80, 0x3F80, 0x3F80};
    asm volatile("" : "+v"(ones));
    {
        const unsigned sb = (g0 & 3) * SLOT;
#pragma unroll
        for (int i = 0; i < R; ++i) xa_read1<NC>(f0, i, pX0 + sb, pW0 + sb, rot);
    }
    asm volatile("s_nop 1"); SB0();
#pragma unroll 1
    for (int s = 0; s < NST - 1; ++s) {
        const unsigned sb = ((g0 + s) & 3) * SLOT, sn = ((g0 + s + 1) & 3) * SLOT;
        LAS char* nX = pX0 + sn; LAS char* nW = pW0 + sn;
        xa_step<NC, TR, XST, DA, true>(A, f0, f1, pX1 + sb, pW1 + sb, rot, ones);
        asm volatile("" : "+v"(nX), "+v"(nW));
        WAIT_L0(); BAR(); SB0();
        hook(s);
        xa_step<NC, TR, XST, DB, true>(A, f1, f0, nX, nW, rot, ones);
    }
    const unsigned sb = ((g0 + NST - 1) & 3) * SLOT;
    xa_step<NC, TR, XST, DA, true>(A, f0, f1, pX1 + sb, pW1 + sb, rot, ones);
    WAIT_L0(); BAR(); SB0();
    xa_step<NC, TR, XST, 0, false>(A, f1, f0, pX0, pW0, rot, ones);
    MMA_DRAIN();
}
template <int NC, bool TR, bool XST> __device__ __forceinline__ void share_colsum(const XWAcc<NC, XST>& A, LAS char* lds, int m, int lane) {
    LAS float* tab = (LAS float*)(lds + SP_CS);
    const int fr = lane & 15, g = lane >> 4;
#pragma unroll
    for (int k = 0; k < (NC + 3) / 4; ++k) {
        int c = m + 4 * k; c = c >= NC ? c - NC : c;
        if (TR) { if (fr == 0) *(LAS f32x4*)(tab + c * 16 + 4 * g) = A.cs[k]; }
        else { if (g == 0) tab[c * 16 + fr] = A.cs[k][0]; }
    }
    WAIT_L0(); BAR();
}
template <int NC> __device__ __forceinline__ void unrotate(unsigned (&v)[NC][2], int m) {
    unsigned u[NC][2];
#define UNROT(M) _Pragma("unroll") for (int c = 0; c < NC; ++c) { u[(c + M) % NC][0] = v[c][0]; u[(c + M) % NC][1] = v[c][1]; }
    if (m == 0) { UNROT(0) } else if (m == 1) { UNROT(1) } else if (m == 2) { UNROT(2) } else { UNROT(3) }
#undef UNROT
#pragma unroll
    for (int c = 0; c < NC; ++c) { v[c][0] = u[c][0]; v[c][1] = u[c][1]; asm volatile("" : "+v"(v[c][0]), "+v"(v[c][1])); }
}

struct MFrag { bf16x8 w[2]; bf16x8 z[9]; };
template <bool LOAD> __device__ __forceinline__ void m_step(f32x4 (&acc)[2][9], const MFrag& f, MFrag& n, LAS char* pW, LAS char* pZ) {
#pragma unroll
    for (int i = 0; i < 18; ++i) {
        MMA(acc[i & 1][i >> 1], f.z[i >> 1], f.w[i & 1]);
        if (LOAD) { if (i < 2) n.w[i] = lds_b128(pW + i * 2048); else if (i < 11) n.z[i - 2] = lds_b128(pZ + (i - 2) * 1024); }
        SB0();
    }
}
template <int NST> __device__ __forceinline__ void main_gemm(f32x4 (&acc)[2][9], LAS char* lds, int g0, int m, int lane) {
    const int fr = lane & 15, g = lane >> 4, row = 32 * m + fr;
    LAS char* pW0 = lds + row * 128 + ((g ^ ((row >> 1) & 7)) << 4);
    LAS char* pW1 = lds + ((row * 128 + ((g ^ ((row >> 1) & 7)) << 4)) ^ 64);
    LAS char* pZ0 = lds + 16384 + lane * 16;
    LAS char* pZ1 = pZ0 + 9216;
#pragma unroll
    for (int t = 0; t < 2; ++t)
#pragma unroll
        for (int c = 0; c < 9; ++c) { acc[t][c] = (f32x4){0.f, 0.f, 0.f, 0.f}; asm volatile("" : "+v"(acc[t][c])); }
    MFrag f0, f1;
    {
        const unsigned sb = (g0 & 3) * SLOT;
#pragma unroll
        for (int i = 0; i < 2; ++i) f0.w[i] = lds_b128(pW0 + sb + i * 2048);
#pragma unroll
        for (int c = 0; c < 9; ++c) f0.z[c] = lds_b128(pZ0 + sb + c * 1024);
    }
    asm volatile("s_nop 1"); SB0();
#pragma unroll 1
    for (int s = 0; s < NST - 1; ++s) {
        const unsigned sb = ((g0 + s) & 3) * SLOT, sn = ((g0 + s + 1) & 3) * SLOT;
        LAS char* nW = pW0 + sn; LAS char* nZ = pZ0 + sn;
        m_step<true>(acc, f0, f1, pW1 + sb, pZ1 + sb);
        asm volatile("" : "+v"(nW), "+v"(nZ));
        WAIT_L0(); BAR(); SB0();
        m_step<true>(acc, f1, f0, nW, nZ);
    }
    const unsigned sb = ((g0 + NST - 1) & 3) * SLOT;
    m_step<true>(acc, f0, f1, pW1 + sb, pZ1 + sb);
    WAIT_L0(); BAR(); SB0();
    m_step<false>(acc, f1, f0, pW0, pZ0);
    MMA_DRAIN();
}

struct EFrag { s16x4 w[2]; bf16x8 b[5]; };
template <bool LOAD, bool MM> __device__ __forceinline__ void e_step(f32x4 (&acc)[5], const EFrag& f, EFrag& n, LAS char* pW, LAS char* pB) {
    const bf16x8 w = cat8(f.w[0], f.w[1]);
#pragma unroll
    for (int t = 0; t < 5; ++t) {
        if (MM) MMA(acc[t], w, f.b[t]);
        if (LOAD) {
            if (t == 0) { n.w[0] = lds_tr1(pW); n.w[1] = lds_tr1(pW + 2048); }
            else if (t == 1) { n.b[0] = lds_b128(pB); n.b[1] = lds_b128(pB + 1024); }
            else n.b[t] = lds_b128(pB + t * 1024);
        }
        SB0();
    }
}
template <int NST, int NKS, bool MORE, class Hook> __device__ __forceinline__ void e_gemm(f32x4 (&acc)[5], LAS char* lds, int g0, int w, int lane, Hook hook) {
    const int g = lane >> 4, q = (lane & 15) >> 2;
    LAS char* pW = lds + (4 * g + q) * 128 + ((w ^ ((2 * g + (q >> 1)) & 3)) << 5) + (lane & 3) * 8;
    LAS char* pB = lds + 16384 + lane * 16;
#pragma unroll
    for (int t = 0; t < 5; ++t) { acc[t] = (f32x4){0.f, 0.f, 0.f, 0.f}; asm volatile("" : "+v"(acc[t])); }
    EFrag f0, f1;
    e_step<true, false>(acc, f1, f0, pW + (g0 & 3) * SLOT, pB + (g0 & 3) * SLOT);
    asm volatile("s_nop 1"); SB0();
#pragma unroll 1
    for (int s = 0; s < NST - 1; ++s) {
        const unsigned sb = ((g0 + s) & 3) * SLOT, sn = ((g0 + s + 1) & 3) * SLOT;
        e_step<true, true>(acc, f0, f1, pW + sb + 4096, pB + sb + 5120);
        e_step<true, true>(acc, f1, f0, pW + sb + 8192, pB + sb + 10240);
        e_step<true, true>(acc, f0, f1, pW + sb + 12288, pB + sb + 15360);
        WAIT_L0(); BAR(); SB0();
        hook(s);
        e_step<true, true>(acc, f1, f0, pW + sn, pB + sn);
    }
    const unsigned sb = ((g0 + NST - 1) & 3) * SLOT;
    constexpr int LAST = NKS - 4 * (NST - 1);
    e_step<true, true>(acc, f0, f1, pW + sb + 4096, pB + sb + 5120);
    if (LAST == 4) {
        e_step<true, true>(acc, f1, f0, pW + sb + 8192, pB + sb + 10240);
        e_step<true, true>(acc, f0, f1, pW + sb + 12288, pB + sb + 15360);
    }
    if (MORE) { WAIT_L0(); BAR(); SB0(); }
    e_step<false, true>(acc, f1, f0, pW, pB);
    MMA_DRAIN();
}

__device__ __forceinline__ float dot2(unsigned a, unsigned b, float c) { return __builtin_amdgcn_fdot2_f32_bf16(__builtin_bit_cast(bf16x2_t, a), __builtin_bit_cast(bf16x2_t, b), c, false); }
__device__ __forceinline__ unsigned at_region(int r, int p) { return (unsigned)((r >> 1) * SLOT + ((r & 1) ? 16384 + WImg<9>::first(p) * 1024 : p * 4096)); }
__device__ __forceinline__ unsigned at_row(int rho, int p) { const int r = rho / 14; return at_region(r, p) + (unsigned)(rho - r * 14) * 288u; }
__device__ __forceinline__ void attn_issue(rsrc_t rQ, LAS char* lds, int n, int p, int lane, int half) {
#pragma unroll
    for (int rr = 0; rr < 2; ++rr)
#pragma unroll
        for (int q = 0; q < 4; ++q) {
            const int r = 2 * half + rr, c = q * 64 + lane, lr = c / 18, cc = c - lr * 18, rho = r * 14 + lr;
            int t, seg;
            if (rho < 22) { t = 8 * n - 14 + rho; seg = 1; } else if (rho < 44) { t = 8 * n - 14 + (rho - 22); seg = 2; } else { t = 8 * n + (rho - 44); seg = 0; }
            const unsigned voff = (t < 0 || rho >= 52 || lr >= 14) ? 0x7ffffff0u : (unsigned)(t * (QW * 2) + (seg * D_ + 144 * p) * 2 + cc * 16);
            dma16_dev(rQ, lds + at_region(r, p) + q * 1024, voff, 0);
        }
}
__device__ __forceinline__ void attn_compute(LAS char* lds, rsrc_t rO, int n, int p, int lane) {
    const int i = lane >> 3, hh = lane & 7, t = 8 * n + i;
    const unsigned hb = hh * 36, r0 = at_region(0, p) + hb, r1 = at_region(1, p) + hb - 14 * 288, r2 = at_region(2, p) + hb - 28 * 288, r3 = at_region(3, p) + hb - 42 * 288;
    unsigned qv[9];
    {
        LAS unsigned* q = (LAS unsigned*)(lds + r3 + (44 + i) * 288);
#pragma unroll
        for (int d = 0; d < 9; ++d) qv[d] = q[d];
    }
    float lg[8];
#pragma unroll
    for (int w = 0; w < 8; ++w) {
        const int rho = 14 + i - 2 * w;
        LAS unsigned* kp = (LAS unsigned*)(lds + (rho < 14 ? r0 : r1) + rho * 288);
        float s = 0.f;
#pragma unroll
        for (int d = 0; d < 9; ++d) s = dot2(qv[d], kp[d], s);
        lg[w] = (t - 2 * w >= 0) ? s * (0.23570226039551584f * 1.4426950408889634f) : -1e9f;
    }
    float mx = fmaxf(fmaxf(fmaxf(lg[0], lg[1]), fmaxf(lg[2], lg[3])), fmaxf(fmaxf(lg[4], lg[5]), fmaxf(lg[6], lg[7])));
    float sum = 0.f;
#pragma unroll
    for (int w = 0; w < 8; ++w) { lg[w] = __builtin_amdgcn_exp2f(lg[w] - mx); sum += lg[w]; }
    const float inv = __builtin_amdgcn_rcpf(sum);
    unsigned pp[4];
#pragma unroll
    for (int w = 0; w < 4; ++w) pp[w] = pack_bf16(lg[2 * w] * inv, lg[2 * w + 1] * inv);
    float acc[18];
#pragma unroll
    for (int d = 0; d < 18; ++d) acc[d] = 0.f;
#pragma unroll
    for (int w = 0; w < 4; ++w) {
        const int ra = 22 + 14 + i - 4 * w, rb = ra - 2;
        LAS unsigned* va = (LAS unsigned*)(lds + (ra < 28 ? r1 : ra < 42 ? r2 : r3) + ra * 288);
        LAS unsigned* vb = (LAS unsigned*)(lds + (rb < 28 ? r1 : rb < 42 ? r2 : r3) + rb * 288);
#pragma unroll
        for (int d = 0; d < 9; ++d) {
            const unsigned x = va[d], y = vb[d];
            acc[2 * d] = dot2(__builtin_amdgcn_perm(y, x, 0x05040100u), pp[w], acc[2 * d]);
            acc[2 * d + 1] = dot2(__builtin_amdgcn_perm(y, x, 0x07060302u), pp[w], acc[2 * d + 1]);
        }
    }
    LAS char* ob = lds + SP_AO + p * 2304;
#pragma unroll
    for (int d = 0; d < 9; ++d) *(LAS unsigned*)(ob + i * 288 + hh * 36 + d * 4) = pack_bf16(acc[2 * d], acc[2 * d + 1]);
    WAIT_L0();
    {
        const int r = lane & 7, g = (lane >> 3) & 3, tt = n >> 1, rl = (8 * n + r) & 15;
#pragma unroll
        for (int q = 0; q < 2; ++q) {
            const int a = (lane >> 5) + 2 * q;
            const u32x2 lo = *(LAS u32x2*)(ob + r * 288 + (32 * a + 4 * g) * 2), hi = *(LAS u32x2*)(ob + r * 288 + (32 * a + 16 + 4 * g) * 2);
            st128_dev(rO, ((tt * 18 + 4 * p + a) * 64 + rl + 16 * g) * 16, (u32x4){lo.x, lo.y, hi.x, hi.y});
        }
        const u32x2 v = *(LAS u32x2*)(ob + r * 288 + (128 + 4 * g) * 2);
        if (lane < 32) st64_dev(rO, ((tt * 18 + 16 + (p >> 1)) * 64 + rl + 16 * g) * 16 + (p & 1) * 8, v);
    }
}

__global__ __launch_bounds__(512, 2) void fused_kernel(Args a) {
    extern __shared__ __attribute__((aligned(16))) char smem[];
    LAS char* lds = (LAS char*)smem;
    const int wid = __builtin_amdgcn_readfirstlane(threadIdx.x >> 6), lane = threadIdx.x & 63;
    const int xcd = blockIdx.x & 7, loc = blockIdx.x >> 3;
    const int ti = (xcd & 1) * 8 + (loc & 7), tj = (xcd >> 1) * 4 + (loc >> 3);
    const int nt = xcd * 30 + loc;
    const bool fin = loc < 30 && nt < 234;
    const int rb = nt / 9, cb = nt - rb * 9;
    char* ctl = a.ws + WS_CTL;
    u64 tag;
    {
        const u64 e = __hip_atomic_load((const GAS u64*)(ctl + CTL_EP), __ATOMIC_RELAXED, __HIP_MEMORY_SCOPE_AGENT);
        const u64 tg = e * 0x9E3779B97F4A7C15ull + 0xD1B54A32D192ED03ull;
        tag = ((u64)(unsigned)__builtin_amdgcn_readfirstlane((unsigned)(tg >> 32)) << 32) | (unsigned)__builtin_amdgcn_readfirstlane((unsigned)tg);
    }
    const int an = ti * 16 + tj;

    if (wid >= 4) {
        const int p = wid - 4;
        ST_DECL; ST(0);
#define rX mk_rsrc(a.x, S_ * D_ * 2)
#define rQW mk_rsrc(a.wqkv, D_ * QW * 2)
#define rW mk_rsrc(a.win, D_ * F_ * 2)
#define rS mk_rsrc(a.wsp, S_ * S_ * 2)
#define rZ mk_rsrc(a.ws + WS_Z2T, S_ * FH * 2)
#define rQ mk_rsrc(a.ws + WS_QKV, S_ * QW * 2)
#define rO mk_rsrc(a.ws + WS_OF, S_ * D_ * 2)
        RowsImg xi; xi.init(lane, D_ * 2);
        RowsImg si; si.init(lane, S_ * 2);
        WImg<7> wq; wq.init(lane, p, QW * 2);
        WImg<9> wz; wz.init(lane, p, F_ * 2);
        const unsigned sX = (unsigned)(ti * 128 + 32 * p) * (D_ * 2), sQ = (unsigned)((tj * 27) >> 2) * 32, sZ1 = (unsigned)(tj * 144) * 2, sZ2 = sZ1 + FH * 2;
        const unsigned sS = (unsigned)(ti * 128 + 32 * p) * (S_ * 2), sZ = (unsigned)tj * (64u * 9216u);
        const unsigned vl = lane * 16;
        const unsigned vE = (lane >> 3) * (D_ * 2) + ((((lane & 7) >> 1) ^ ((lane >> 4) & 3)) << 5) + (lane & 1) * 16;
        const int zI0 = WImg<9>::first(p);
        auto iss_q = [&](int g) {
            LAS char* slot = lds + (g & 3) * SLOT;
            xi.issue(rX, slot + p * 4096, sX + g * 128, D_ * 2); wq.issue(rQW, slot + 16384, sQ + g * 64 * (QW * 2), p);
        };
        auto iss_z = [&](int g, int s, unsigned col) {
            LAS char* slot = lds + (g & 3) * SLOT;
            xi.issue(rX, slot + p * 4096, sX + s * 128, D_ * 2); wz.issue(rW, slot + 16384, col + s * 64 * (F_ * 2), p);
        };
        auto iss_m = [&](int g) {
            LAS char* slot = lds + (g & 3) * SLOT; const int s = g - G_MAIN;
            si.issue(rS, slot + p * 4096, sS + s * 128, S_ * 2);
#pragma unroll
            for (int q = 0; q < 4; ++q) dma16_dev(rZ, slot + 16384 + (zI0 + q) * 1024, vl, sZ + s * 18432 + (zI0 + q) * 1024);
            if (p < 2) dma16_dev(rZ, slot + 16384 + (zI0 + 4) * 1024, vl, sZ + s * 18432 + (zI0 + 4) * 1024);
        };
        unsigned eT[5];
#pragma unroll
        for (int t = 0; t < 5; ++t) eT[t] = (unsigned)__builtin_amdgcn_readfirstlane(min(rb * 5 + t, 127));
        auto iss_e = [&](int g, bool isS) {
            LAS char* slot = lds + (g & 3) * SLOT;
            const int lim = isS ? 16 : 64, nks = isS ? 18 : 72;
            int ks = 4 * (g - (isS ? G_S : G_G)) + p; if (isS && ks > 17) ks = 17;
            const int c0 = ks < lim ? 9 * (ks >> 2) + 2 * (ks & 3) : 18 * (ks - lim) + 8, c1 = ks < lim ? c0 + 1 : c0 + 9;
            const rsrc_t rw = mk_rsrc(isS ? a.wo : a.wout, isS ? D_ * D_ * 2 : FH * D_ * 2), rb_ = mk_rsrc(a.ws + (isS ? WS_OF : WS_HF), isS ? S_ * D_ * 2 : S_ * FH * 2);
#pragma unroll
            for (int q = 0; q < 4; ++q) dma16(rw, slot + p * 4096 + q * 1024, vE, (unsigned)((q < 2 ? c0 : c1) * 16 + 8 * (q & 1)) * (D_ * 2) + cb * 128);
#pragma unroll
            for (int t = 0; t < 5; ++t) dma16_dev(rb_, slot + 16384 + p * 5120 + t * 1024, vl, (eT[t] * nks + ks) * 1024);
        };
#define PSTEP(K, PH) do { WAIT_V(7 * (K)); BAR(); } while (0)
#define SPIN_UNTIL(cond) do { unsigned spins_ = 0; while (!(cond) && ++spins_ < SPIN_CAP) __builtin_amdgcn_s_sleep(4); asm volatile("" ::: "memory"); } while (0)
        iss_q(0); iss_q(1); iss_q(2); iss_q(3);
        {
            const unsigned W = blockIdx.x * 4 + p;
            touch_lines(rX, lds + SP_PF + wid * 256, W * 18, 18, lane);
            touch_lines(rQW, lds + SP_PF + wid * 256, W * 16, 16, lane);
            touch_lines(rW, lds + SP_PF + wid * 256, W * 41, 41, lane);
        }
        PSTEP(3, 0);
#pragma unroll 1
        for (int g = 1; g <= 5; ++g) { PSTEP(2, 0); iss_q(g + 3); }
#pragma unroll 1
        for (int g = 6; g <= 14; ++g) { PSTEP(2, 1); iss_z(g + 3, g + 3 - G_Z2, sZ2); if (g == G_Z2) BAR(); }
        PSTEP(2, 1); iss_z(18, 0, sZ1);
        PSTEP(2, 1); iss_z(19, 1, sZ1);
        PSTEP(2, 1);
        {
            const int pa = (8 * an - 14) >> 7;
            SPIN_UNTIL(flags_ready(ctl + CTL_QF + ti * 512, 64, ctl + CTL_QF + (pa < 0 ? ti : pa) * 512, 64, tag, lane));
            attn_issue(rQ, lds, an, p, lane, 0);
        }
        PSTEP(1, 2);
        attn_issue(rQ, lds, an, p, lane, 1);
        {
            const unsigned W = blockIdx.x * 4 + p;
            touch_lines(rS, lds + SP_PF + wid * 256, W * 64, 64, lane);
            touch_lines(mk_rsrc(a.wout, FH * D_ * 2), lds + SP_PF + wid * 256, W * 21, 21, lane);
            touch_lines(mk_rsrc(a.wo, D_ * D_ * 2), lds + SP_PF + wid * 256, W * 6, 6, lane);
        }
        BAR();
        ST(1);
        WAIT_V(3);
        __builtin_amdgcn_s_setprio(ATT_PRIO);
        attn_compute(lds, rO, an, p, lane);
        __builtin_amdgcn_s_setprio(0);
        ST(2);
        iss_z(20, 2, sZ1); iss_z(21, 3, sZ1);
        publish(ctl, CTL_OF + (an * 4 + p) * 8, tag, lane);
        ST(3);
#pragma unroll 1
        for (int g = 19; g <= 23; ++g) { PSTEP(2, 2); iss_z(g + 3, g + 3 - G_Z1, sZ1); }
        PSTEP(2, 2); PSTEP(1, 2); PSTEP(0, 2);
        SPIN_UNTIL(flags_ready(ctl + CTL_ZF + tj * 512, 64, ctl, 0, tag, lane));
        iss_m(27); iss_m(28); iss_m(29);
        PSTEP(2, 3); iss_m(30); BAR();
#pragma unroll 1
        for (int g = 28; g <= 55; ++g) { PSTEP(2, 3); iss_m(g + 3); }
        if (!fin) { PSTEP(2, 3); PSTEP(1, 3); PSTEP(0, 3); BAR(); }
        else {
            PSTEP(2, 3);
            SPIN_UNTIL(flags_ready(ctl + CTL_OF + rb * 320, rb == 25 ? 24 : 40, ctl, 0, tag, lane));
            iss_e(59, true);
#pragma unroll 1
            for (int g = 57; g <= 60; ++g) { PSTEP(2, 4); iss_e(g + 3, true); }
            PSTEP(2, 4); PSTEP(1, 4); PSTEP(0, 4);
            {
                const int p0 = (rb * 80) >> 7, p1 = min(rb * 80 + 79, S_ - 1) >> 7;
                SPIN_UNTIL(flags_ready(ctl + CTL_HF + p0 * 512, 64, ctl + CTL_HF + p1 * 512, 64, tag, lane));
            }
            iss_e(64, false); iss_e(65, false); iss_e(66, false);
#pragma unroll 1
            for (int g = 64; g <= 78; ++g) { PSTEP(2, 5); iss_e(g + 3, false); }
            PSTEP(2, 5); PSTEP(1, 5); PSTEP(0, 5);
        }
        ST_END(1);
        return;
    }

    const int m = wid, fr = lane & 15, g4 = lane >> 4;
    ST_DECL; ST(0);
    const rsrc_t rQs = mk_rsrc(a.ws + WS_QKV, S_ * QW * 2), rZs = mk_rsrc(a.ws + WS_Z2T, S_ * FH * 2), rHs = mk_rsrc(a.ws + WS_HF, S_ * FH * 2), rSt = mk_rsrc(a.ws + WS_ST, S_ * 16 * 16);
    LAS float* cst = (LAS float*)(lds + SP_CS);
    float xm[2], xr[2];
    BAR();
    ST(1);
    {
        XWAcc<7, true> A; xw_zero(A);
        xw_gemm<7, true, true, 9>(A, lds, G_QKV, m, lane, NoHook());
        ST(2);
        share_colsum<7, true, true>(A, lds, m, lane);
#pragma unroll
        for (int t = 0; t < 2; ++t) {
            const int r3 = fr & 3;
            const float dq = r3 == 0 ? A.sq[t][0] : r3 == 1 ? A.sq[t][1] : r3 == 2 ? A.sq[t][2] : A.sq[t][3];
            const float sq = __shfl(dq, (fr >> 2) * 16 + fr);
            xm[t] = A.sx[t][0] * (1.0f / D_);
            xr[t] = __builtin_amdgcn_rsqf(fmaxf(sq * (1.0f / D_) - xm[t] * xm[t], 0.f) + 1e-5f);
        }
        const int c0 = (tj * 27) >> 2, dup = tj > 0 && c0 < (((tj - 1) * 27) >> 2) + 7;
#pragma unroll
        for (int t = 0; t < 2; ++t) {
            unsigned pk[7][2];
#pragma unroll
            for (int c = 0; c < 7; ++c) {
                const int tile = c + m >= 7 ? c + m - 7 : c + m;
                const f32x4 cs = *(LAS f32x4*)(cst + tile * 16 + 4 * g4);
                pk[c][0] = pack2((lo2(A.a[t][c]) - lo2(cs) * xm[t]) * xr[t]); pk[c][1] = pack2((hi2(A.a[t][c]) - hi2(cs) * xm[t]) * xr[t]);
            }
            unrotate<7>(pk, m);
            const unsigned ro = (unsigned)(ti * 128 + 32 * m + 16 * t + fr) * (QW * 2) + c0 * 32;
#pragma unroll
            for (int pp = 0; pp < 3; ++pp) st128_dev(rQs, ro + (1 + 2 * pp) * 32 + g4 * 16, gather8(pk[1 + 2 * pp][0], pk[1 + 2 * pp][1], pk[2 + 2 * pp][0], pk[2 + 2 * pp][1]));
            if (!dup) st64_dev(rQs, ro + g4 * 8, (u32x2){pk[0][0], pk[0][1]});
        }
    }
    ST(3);
    unsigned zp[2][9][2];
    {
        XWAcc<9, false> A; xw_zero(A);
        xw_gemm<9, false, false, 9>(A, lds, G_Z2, m, lane, [&](int s) { if (s == 4) publish(ctl, CTL_QF + ((ti * 16 + tj) * 4 + m) * 8, tag, lane); });
        ST(4);
        share_colsum<9, false, false>(A, lds, m, lane);
        f32x2 rm[2][2], rr[2][2];
#pragma unroll
        for (int t = 0; t < 2; ++t)
#pragma unroll
            for (int h = 0; h < 2; ++h) {
                rm[t][h] = (f32x2){__shfl(xm[t], 4 * g4 + 2 * h), __shfl(xm[t], 4 * g4 + 2 * h + 1)};
                rr[t][h] = (f32x2){__shfl(xr[t], 4 * g4 + 2 * h), __shfl(xr[t], 4 * g4 + 2 * h + 1)};
            }
        f32x2 s1[2][2], s2[2][2];
#pragma unroll
        for (int t = 0; t < 2; ++t)
#pragma unroll
            for (int h = 0; h < 2; ++h) { s1[t][h] = (f32x2){0.f, 0.f}; s2[t][h] = (f32x2){0.f, 0.f}; }
#pragma unroll
        for (int c = 0; c < 9; ++c) {
            const float cs = cst[(c + m >= 9 ? c + m - 9 : c + m) * 16 + fr];
#pragma unroll
            for (int t = 0; t < 2; ++t)
#pragma unroll
                for (int h = 0; h < 2; ++h) {
                    const f32x2 v = gelu2(((h ? hi2(A.a[t][c]) : lo2(A.a[t][c])) - rm[t][h] * cs) * rr[t][h]);
                    s1[t][h] += v; s2[t][h] += v * v;
                    zp[t][c][h] = pack2(v);
                    asm volatile("" : "+v"(zp[t][c][h]));
                }
        }
        float mm = 0.f, qq = 0.f;
        {
            const int idx = lane & 7;
            float a1[8], a2[8];
#pragma unroll
            for (int k = 0; k < 8; ++k) { a1[k] = s1[k >> 2][(k >> 1) & 1][k & 1]; a2[k] = s2[k >> 2][(k >> 1) & 1][k & 1]; }
#pragma unroll
            for (int k = 0; k < 8; ++k) { a1[k] = dpp_add<0x128>(a1[k]); a2[k] = dpp_add<0x128>(a2[k]); }
#pragma unroll
            for (int k = 0; k < 8; ++k) { a1[k] = dpp_add<0x124>(a1[k]); a2[k] = dpp_add<0x124>(a2[k]); }
#pragma unroll
            for (int k = 0; k < 8; ++k) { a1[k] = dpp_add<0x122>(a1[k]); a2[k] = dpp_add<0x122>(a2[k]); }
#pragma unroll
            for (int k = 0; k < 8; ++k) { a1[k] = dpp_add<0x121>(a1[k]); a2[k] = dpp_add<0x121>(a2[k]); }
            float u1 = a1[0], u2 = a2[0];
#pragma unroll
            for (int k = 1; k < 8; ++k) { u1 = idx == k ? a1[k] : u1; u2 = idx == k ? a2[k] : u2; }
            mm = u1 * (1.0f / 144.0f);
            qq = fmaxf(u2 - u1 * mm, 0.f);
            if (fr < 8) st128_dev(rSt, ((ti * 16 + tj) * 128 + 32 * m + 16 * (idx >> 2) + 4 * g4 + (idx & 3)) * 16,
                                  (u32x4){__float_as_uint(mm), (unsigned)tag, __float_as_uint(qq), (unsigned)(tag >> 32)});
        }
    }
    ST(5);
    unsigned z1p[2][9][2];
    {
        XWAcc<9, false> A; xw_zero(A);
        xw_gemm<9, true, false, 2>(A, lds, G_Z1, m, lane, NoHook());
        ST(6);
        float mu, rs;
        {
            const unsigned off = ((ti * 16 + (lane >> 5) * 8) * 128 + 32 * m + (lane & 31)) * 16;
            u32x4 v[8]; unsigned spins = 0;
            for (;;) {
#pragma unroll
                for (int k = 0; k < 8; ++k) v[k] = __builtin_amdgcn_raw_buffer_load_b128(rSt, off + k * 2048, 0, 16);
                unsigned bad = 0;
#pragma unroll
                for (int k = 0; k < 8; ++k) bad |= (v[k].y ^ (unsigned)tag) | (v[k].w ^ (unsigned)(tag >> 32));
                if (__all(bad == 0) || ++spins > SPIN_CAP) break;
                __builtin_amdgcn_s_sleep(2);
            }
            float s = 0.f;
#pragma unroll
            for (int k = 0; k < 8; ++k) s += __uint_as_float(v[k].x);
            s += xchg32(s);
            mu = s * (1.0f / 16.0f);
            float q = 0.f;
#pragma unroll
            for (int k = 0; k < 8; ++k) { const float d = __uint_as_float(v[k].x) - mu; q += __uint_as_float(v[k].z) + 144.0f * d * d; }
            q += xchg32(q);
            rs = __builtin_amdgcn_rsqf(q * (1.0f / FH) + 1e-5f);
        }
        ST(7);
        f32x2 mr[2][2], rr[2][2];
#pragma unroll
        for (int t = 0; t < 2; ++t)
#pragma unroll
            for (int h = 0; h < 2; ++h) {
                const int src = 16 * t + 4 * g4 + 2 * h;
                mr[t][h] = (f32x2){__shfl(mu, src), __shfl(mu, src + 1)}; rr[t][h] = (f32x2){__shfl(rs, src), __shfl(rs, src + 1)};
            }
        const unsigned zo = (unsigned)((tj * 64 + ti * 4 + m) * 9) * 1024 + lane * 16;
#pragma unroll
        for (int c = 0; c < 9; ++c) {
            const unsigned x0 = pack2((unpack2(zp[0][c][0]) - mr[0][0]) * rr[0][0]), x1 = pack2((unpack2(zp[0][c][1]) - mr[0][1]) * rr[0][1]);
            const unsigned y0 = pack2((unpack2(zp[1][c][0]) - mr[1][0]) * rr[1][0]), y1 = pack2((unpack2(zp[1][c][1]) - mr[1][1]) * rr[1][1]);
            st128_dev(rZs, zo + (c + m >= 9 ? c + m - 9 : c + m) * 1024, gather8(x0, x1, y0, y1));
        }
        ST(8);
        xw_gemm<9, true, false, 7>(A, lds, G_Z1 + 2, m, lane, [&](int s) { if (s == 1) publish(ctl, CTL_ZF + ((tj * 16 + ti) * 4 + m) * 8, tag, lane); });
        ST(9);
        share_colsum<9, true, false>(A, lds, m, lane);
#pragma unroll
        for (int c = 0; c < 9; ++c) {
            const f32x4 cs = *(LAS f32x4*)(cst + (c + m >= 9 ? c + m - 9 : c + m) * 16 + 4 * g4);
#pragma unroll
            for (int t = 0; t < 2; ++t) {
                z1p[t][c][0] = pack2(gelu2((lo2(A.a[t][c]) - lo2(cs) * xm[t]) * xr[t])); z1p[t][c][1] = pack2(gelu2((hi2(A.a[t][c]) - hi2(cs) * xm[t]) * xr[t]));
                asm volatile("" : "+v"(z1p[t][c][0]), "+v"(z1p[t][c][1]));
            }
        }
        unrotate<9>(z1p[0], m); unrotate<9>(z1p[1], m);
    }
    ST(10);
    {
        f32x4 acc[2][9];
        float bt[2];
#pragma unroll
        for (int t = 0; t < 2; ++t) bt[t] = bf2f(a.bsp[ti * 128 + 32 * m + 16 * t + fr]);
        main_gemm<32>(acc, lds, G_MAIN, m, lane);
        ST(11);
#pragma unroll
        for (int t = 0; t < 2; ++t) {
            const unsigned hp = (unsigned)(ti * 8 + 2 * m + t) * 72 * 1024 + lane * 16;
#pragma unroll
            for (int pp = 0; pp < 4; ++pp) {
                u32x4 o;
                o.x = pack2((lo2(acc[t][2 * pp]) + bt[t]) * unpack2(z1p[t][2 * pp][0])); o.y = pack2((hi2(acc[t][2 * pp]) + bt[t]) * unpack2(z1p[t][2 * pp][1]));
                o.z = pack2((lo2(acc[t][2 * pp + 1]) + bt[t]) * unpack2(z1p[t][2 * pp + 1][0])); o.w = pack2((hi2(acc[t][2 * pp + 1]) + bt[t]) * unpack2(z1p[t][2 * pp + 1][1]));
                st128_dev(rHs, hp + (tj * 4 + pp) * 1024, o);
            }
            u32x2 o;
            o.x = pack2((lo2(acc[t][8]) + bt[t]) * unpack2(z1p[t][8][0])); o.y = pack2((hi2(acc[t][8]) + bt[t]) * unpack2(z1p[t][8][1]));
            st64_dev(rHs, hp + (64 + (tj >> 1)) * 1024 + (tj & 1) * 8, o);
        }
    }
    ST(12);
    const size_t hflag = CTL_HF + ((ti * 16 + tj) * 4 + m) * 8;
    if (!fin) { publish(ctl, hflag, tag, lane); ST_END(0); return; }
    {
        f32x4 sa[5], ga[5];
        e_gemm<5, 18, true>(sa, lds, G_S, m, lane, [&](int s) { if (s == 1) publish(ctl, hflag, tag, lane); });
        ST(13);
        u32x2 xv[5];
#pragma unroll
        for (int t = 0; t < 5; ++t) xv[t] = *(const u32x2*)(a.x + (size_t)(min(rb * 5 + t, 127) * 16 + fr) * D_ + cb * 64 + m * 16 + g4 * 4);
        e_gemm<18, 72, false>(ga, lds, G_G, m, lane, NoHook());
        ST(14);
#pragma unroll
        for (int t = 0; t < 5; ++t) {
            if (rb * 5 + t < 128) {
                float o[4];
#pragma unroll
                for (int r = 0; r < 4; ++r) {
                    const float gv = ga[t][r], sg = gv * __builtin_amdgcn_rcpf(1.0f + __builtin_amdgcn_exp2f(gv * -1.4426950408889634f));
                    const float xx = (r & 1) ? bfhi(xv[t][r >> 1]) : bflo(xv[t][r >> 1]);
                    o[r] = xx + sa[t][r] * sg;
                }
                u32x2 ov; ov.x = pack_bf16(o[0], o[1]); ov.y = pack_bf16(o[2], o[3]);
                *(u32x2*)(a.out + (size_t)((rb * 5 + t) * 16 + fr) * D_ + cb * 64 + m * 16 + g4 * 4) = ov;
            }
        }
    }
    if (blockIdx.x == 0 && threadIdx.x == 0) __hip_atomic_store((GAS u64*)(ctl + CTL_EP), tag, __ATOMIC_RELAXED, __HIP_MEMORY_SCOPE_AGENT);
    ST(15);
    ST_END(0);
}

extern "C" void kernel_launch(void* const* d_in, const int* in_sizes, int n_in, void* d_out, int out_size, void* d_ws, size_t ws_size, hipStream_t stream) {
    Args a;
    a.x = (const u16*)d_in[0]; a.wqkv = (const u16*)d_in[1]; a.wo = (const u16*)d_in[2]; a.win = (const u16*)d_in[4]; a.wsp = (const u16*)d_in[5];
    a.bsp = (const u16*)d_in[6]; a.wout = (const u16*)d_in[7]; a.out = (u16*)d_out; a.ws = (char*)d_ws;
    (void)hipFuncSetAttribute((const void*)fused_kernel, hipFuncAttributeMaxDynamicSharedMemorySize, LDS_BYTES);
    fused_kernel<<<256, 512, LDS_BYTES, stream>>>(a);
}
```

```cpp
#include <hip/hip_runtime.h>
#include <hip/hip_bf16.h>
#include <stdint.h>

typedef unsigned short u16;
typedef unsigned long long u64;
typedef short bf16x8 __attribute__((ext_vector_type(8)));
typedef short s16x4 __attribute__((ext_vector_type(4)));
typedef float f32x4 __attribute__((ext_vector_type(4)));
typedef float f32x2 __attribute__((ext_vector_type(2)));
typedef unsigned u32x4 __attribute__((ext_vector_type(4)));
typedef unsigned u32x2 __attribute__((ext_vector_type(2)));
typedef __bf16 bf16x2_t __attribute__((ext_vector_type(2)));
#define LAS __attribute__((address_space(3)))
#define GAS __attribute__((address_space(1)))
typedef LAS void* lds_vp;
typedef __amdgpu_buffer_rsrc_t rsrc_t;

constexpr int S_ = 2048, D_ = 576, F_ = 4608, FH = 2304, QW = 1728;
constexpr int SLOT = 36864, SPARE = 4 * SLOT, LDS_BYTES = 163840;
constexpr int SP_CS = SPARE, SP_PF = SPARE + 1024, SP_AO = SPARE + 4096;
constexpr size_t WS_Z2T = 0;
constexpr size_t WS_QKV = WS_Z2T + (size_t)S_ * FH * 2;
constexpr size_t WS_HF = WS_QKV + (size_t)S_ * QW * 2;
constexpr size_t WS_OF = WS_HF + (size_t)S_ * FH * 2;
constexpr size_t WS_ST = WS_OF + (size_t)S_ * D_ * 2;
constexpr size_t WS_CTL = WS_ST + (size_t)S_ * 16 * 16;
constexpr size_t CTL_EP = 0, CTL_QF = 1024, CTL_ZF = CTL_QF + 8192, CTL_OF = CTL_ZF + 8192, CTL_HF = CTL_OF + 8192, CTL_END = CTL_HF + 8192;
constexpr int G_QKV = 0, G_Z2 = 9, G_Z1 = 18, G_Z1B = 21, G_MAIN = 27, G_S = 59, G_G = 64, G_END = 82;
constexpr unsigned SPIN_CAP = 400000u;

__device__ __forceinline__ float bflo(unsigned v) { return __uint_as_float(v << 16); }
__device__ __forceinline__ float bfhi(unsigned v) { return __uint_as_float(v & 0xffff0000u); }
__device__ __forceinline__ float bf2f(u16 v) { return __uint_as_float(((unsigned)v) << 16); }
__device__ __forceinline__ unsigned pack_bf16(float lo, float hi) {
    f32x2 v = {lo, hi};
    return __builtin_bit_cast(unsigned, __builtin_convertvector(v, bf16x2_t));
}
#define BAR() do { asm volatile("" ::: "memory"); __builtin_amdgcn_s_barrier(); asm volatile("" ::: "memory"); } while (0)
#define WAIT_V(n) asm volatile("s_waitcnt vmcnt(%0)" ::"n"(n) : "memory")
#define WAIT_L0() asm volatile("s_waitcnt lgkmcnt(0)" ::: "memory")
#define SB0() __builtin_amdgcn_sched_barrier(0)
#define ST_DECL
#define ST(k)
#define ST_END(who)

__device__ __forceinline__ rsrc_t mk_rsrc(const void* p, unsigned bytes) { return __builtin_amdgcn_make_buffer_rsrc((void*)p, 0, bytes, 0x00020000); }
__device__ __forceinline__ void dma16(rsrc_t r, LAS char* lds, unsigned voff, unsigned soff) {
    __builtin_amdgcn_raw_ptr_buffer_load_lds(r, (lds_vp)lds, 16, voff, soff, 0, 0);
}
__device__ __forceinline__ void dma16_dev(rsrc_t r, LAS char* lds, unsigned voff, unsigned soff) {
    __builtin_amdgcn_raw_ptr_buffer_load_lds(r, (lds_vp)lds, 16, voff, soff, 0, 16);
}
#define ATT_PRIO 1
#define STAGE_ST 0
#define ST_AUX 16
__device__ __forceinline__ void touch_lines(rsrc_t r, LAS char* scratch, unsigned first, unsigned cnt, int lane) {
    __builtin_amdgcn_raw_ptr_buffer_load_lds(r, (lds_vp)scratch, 4, (unsigned)lane < cnt ? (first + lane) * 128u : 0x7ffffff0u, 0, 0, 0);
}
__device__ __forceinline__ void st128_dev(rsrc_t r, unsigned off, u32x4 v) { __builtin_amdgcn_raw_buffer_store_b128(v, r, off, 0, ST_AUX); }
__device__ __forceinline__ void st64_dev(rsrc_t r, unsigned off, u32x2 v) { __builtin_amdgcn_raw_buffer_store_b64(v, r, off, 0, ST_AUX); }
__device__ __forceinline__ bf16x8 lds_b128(LAS char* p) { return *(LAS bf16x8*)p; }
__device__ __forceinline__ bf16x8 lds_tr(LAS char* p, int off1) {
    s16x4 a = __builtin_amdgcn_ds_read_tr16_b64_v4i16((LAS s16x4*)p);
    s16x4 b = __builtin_amdgcn_ds_read_tr16_b64_v4i16((LAS s16x4*)(p + off1));
    return __builtin_shufflevector(a, b, 0, 1, 2, 3, 4, 5, 6, 7);
}
#define MFMA(a, b, c) __builtin_amdgcn_mfma_f32_16x16x32_bf16(a, b, c, 0, 0, 0)
template <int CTRL> __device__ __forceinline__ float dpp_add(float x) {
    return x + __builtin_bit_cast(float, __builtin_amdgcn_update_dpp(0, __builtin_bit_cast(int, x), CTRL, 0xf, 0xf, false));
}
__device__ __forceinline__ float row16_sum(float x) { x = dpp_add<0x128>(x); x = dpp_add<0x124>(x); x = dpp_add<0x122>(x); x = dpp_add<0x121>(x); return x; }
__device__ __forceinline__ float gelu_f(float x) {
    const float t = x * (-2.3022081498f - 0.1029432397f * x * x);
    return x * __builtin_amdgcn_rcpf(1.0f + __builtin_amdgcn_exp2f(t));
}
__device__ __forceinline__ f32x2 gelu2(f32x2 x) {
    const f32x2 t = x * (x * x * -0.1029432397f + -2.3022081498f);
    f32x2 e; e.x = __builtin_amdgcn_exp2f(t.x); e.y = __builtin_amdgcn_exp2f(t.y);
    const f32x2 d = e + 1.0f;
    f32x2 r; r.x = __builtin_amdgcn_rcpf(d.x); r.y = __builtin_amdgcn_rcpf(d.y);
    return x * r;
}
__device__ __forceinline__ f32x2 lo2(f32x4 v) { return (f32x2){v[0], v[1]}; }
__device__ __forceinline__ f32x2 hi2(f32x4 v) { return (f32x2){v[2], v[3]}; }
__device__ __forceinline__ unsigned pack2(f32x2 v) { return __builtin_bit_cast(unsigned, __builtin_convertvector(v, bf16x2_t)); }
__device__ __forceinline__ f32x2 unpack2(unsigned v) { return (f32x2){__uint_as_float(v << 16), __uint_as_float(v & 0xffff0000u)}; }
__device__ __forceinline__ int swap23(int r) { return (r & 0x13) | ((r & 4) << 1) | ((r & 8) >> 1); }
__device__ __forceinline__ float xchg32(float x) {
    auto r = __builtin_amdgcn_permlane32_swap(__float_as_uint(x), __float_as_uint(x), false, false);
    return __uint_as_float((threadIdx.x & 32) ? r[0] : r[1]);
}
__device__ __forceinline__ u32x4 gather8(unsigned x0, unsigned x1, unsigned y0, unsigned y1) {
    auto s0 = __builtin_amdgcn_permlane32_swap(x0, y0, false, false);
    auto s1 = __builtin_amdgcn_permlane32_swap(x1, y1, false, false);
    auto t0 = __builtin_amdgcn_permlane16_swap(s0[0], s0[1], false, false);
    auto t1 = __builtin_amdgcn_permlane16_swap(s1[0], s1[1], false, false);
    return (u32x4){t0[0], t1[0], t0[1], t1[1]};
}
__device__ __forceinline__ void publish(char* ctl, size_t word, u64 tag, int lane) {
    asm volatile("s_waitcnt vmcnt(0)" ::: "memory");
    if (lane == 0) __hip_atomic_store((GAS u64*)(ctl + word), tag, __ATOMIC_RELAXED, __HIP_MEMORY_SCOPE_AGENT);
}
__device__ __forceinline__ bool flags_ready(const char* f0, int n0, const char* f1, int n1, u64 tag, int lane) {
    u64 a = tag, b = tag;
    if (lane < n0) a = __hip_atomic_load((const GAS u64*)(f0 + lane * 8), __ATOMIC_RELAXED, __HIP_MEMORY_SCOPE_AGENT);
    if (lane < n1) b = __hip_atomic_load((const GAS u64*)(f1 + lane * 8), __ATOMIC_RELAXED, __HIP_MEMORY_SCOPE_AGENT);
    return __all(a == tag && b == tag);
}

struct Args { const u16* x; const u16* wqkv; const u16* wo; const u16* win; const u16* wsp; const u16* bsp; const u16* wout; u16* out; char* ws; };

struct RowsImg {
    unsigned v0, v1;
    __device__ __forceinline__ void init(int lane, unsigned rs) {
        const int r8 = lane >> 3, ch = lane & 7;
        v0 = r8 * rs + ((ch ^ ((lane >> 4) & 7)) << 4); v1 = r8 * rs + ((ch ^ ((4 + (lane >> 4)) & 7)) << 4);
    }
    __device__ __forceinline__ void issue(rsrc_t r, LAS char* dst, unsigned soff, unsigned rs) const {
#pragma unroll
        for (int q = 0; q < 4; ++q) dma16(r, dst + q * 1024, (q & 1) ? v1 : v0, soff + q * 8 * rs);
    }
    __device__ __forceinline__ void issue35(rsrc_t r, LAS char* dst, unsigned soff, unsigned rs, int p) const {
        const int q0 = p < 2 ? 3 * p : 6 + 5 * (p - 2);
        const unsigned va = (q0 & 1) ? v1 : v0, vb = (q0 & 1) ? v0 : v1;
#pragma unroll
        for (int q = 0; q < 3; ++q) dma16(r, dst + (q0 + q) * 1024, (q & 1) ? vb : va, soff + (q0 + q) * 8 * rs);
        if (p >= 2) {
#pragma unroll
            for (int q = 3; q < 5; ++q) dma16(r, dst + (q0 + q) * 1024, (q & 1) ? vb : va, soff + (q0 + q) * 8 * rs);
        }
    }
};
template <int NC> struct WImg {
    static constexpr int CPR = 2 * NC, BIG = (NC == 9) ? 5 : 4;
    unsigned v[BIG];
    static __device__ __forceinline__ int first(int p) { return NC == 9 ? (p < 2 ? 5 * p : 10 + 4 * (p - 2)) : (p < 2 ? 4 * p : 8 + 3 * (p - 2)); }
    __device__ __forceinline__ void init(int lane, int p, unsigned ldb) {
        const int I0 = first(p);
#pragma unroll
        for (int q = 0; q < BIG; ++q) {
            const int n = (I0 + q) * 64 + lane, pos = n / CPR, cc = n - pos * CPR;
            v[q] = ((pos & 32) + swap23(pos & 31)) * ldb + cc * 16;
        }
    }
    __device__ __forceinline__ void issue(rsrc_t r, LAS char* dst, unsigned soff, int p) const {
        const int I0 = first(p);
#pragma unroll
        for (int q = 0; q < BIG - 1; ++q) dma16(r, dst + (I0 + q) * 1024, v[q], soff);
        if (p < 2) dma16(r, dst + (I0 + BIG - 1) * 1024, v[BIG - 1], soff);
    }
};

#define MMA(acc, a, b) asm volatile("v_mfma_f32_16x16x32_bf16 %0, %1, %2, %0" : "+v"(acc) : "v"(a), "v"(b))
#define MMA_DRAIN() do { SB0(); asm volatile("s_nop 7\n\ts_nop 3"); SB0(); } while (0)
struct NoHook { __device__ __forceinline__ void operator()(int) const {} };
__device__ __forceinline__ s16x4 lds_tr1(LAS char* p) { return __builtin_amdgcn_ds_read_tr16_b64_v4i16((LAS s16x4*)p); }
__device__ __forceinline__ bf16x8 cat8(s16x4 a, s16x4 b) { return __builtin_shufflevector(a, b, 0, 1, 2, 3, 4, 5, 6, 7); }

template <int NC, bool XST> struct XWAcc { f32x4 a[2][NC]; f32x4 cs[3]; f32x4 sx[2], sq[2]; };
template <int NC> struct XF { bf16x8 x[2]; s16x4 w[NC][2]; };
template <int NC> struct XRot { int o[3]; };
template <int NC> __device__ __forceinline__ void xa_read1(XF<NC>& f, int i, LAS char* pX, LAS char* pW, const XRot<NC>& rot) {
    if (i < 2) f.x[i] = lds_b128(pX + i * 2048);
    else {
        const int c = (i - 2) >> 1;
        LAS char* q = c >= NC - 3 ? pW + rot.o[c >= NC - 3 ? c - (NC - 3) : 0] : pW;
        f.w[c][i & 1] = lds_tr1(q + c * 32 + (i & 1) * (8 * NC * 32));
    }
}
template <int NC, bool TR, bool XST, int DBL, bool LOAD>
__device__ __forceinline__ void xa_step(XWAcc<NC, XST>& A, const XF<NC>& f, XF<NC>& n, LAS char* pX, LAS char* pW, const XRot<NC>& rot, const bf16x8& ones) {
    constexpr int R = 2 + 2 * NC;
    int rd = 0, gap = 0;
#define XA_GAP() do { if (LOAD) { if (rd < R) { xa_read1<NC>(n, rd, pX, pW, rot); ++rd; } if (gap < DBL && rd < R) { xa_read1<NC>(n, rd, pX, pW, rot); ++rd; } } ++gap; SB0(); } while (0)
#pragma unroll
    for (int c = 0; c < NC; ++c) {
        const bf16x8 w = cat8(f.w[c][0], f.w[c][1]);
#pragma unroll
        for (int t = 0; t < 2; ++t) {
            if (TR) MMA(A.a[t][c], w, f.x[t]); else MMA(A.a[t][c], f.x[t], w);
            XA_GAP();
        }
        if ((c & 3) == 0) {
            if (TR) MMA(A.cs[c >> 2], w, ones); else MMA(A.cs[c >> 2], ones, w);
            XA_GAP();
        }
    }
    if (XST) {
#pragma unroll
        for (int t = 0; t < 2; ++t) { MMA(A.sx[t], ones, f.x[t]); XA_GAP(); MMA(A.sq[t], f.x[t], f.x[t]); XA_GAP(); }
    }
#pragma unroll
    for (int k = 0; k < 4; ++k) if (LOAD && rd < R) { xa_read1<NC>(n, rd, pX, pW, rot); ++rd; }
    SB0();
#undef XA_GAP
}
template <int NC, bool XST> __device__ __forceinline__ void xw_zero(XWAcc<NC, XST>& A) {
    const f32x4 z = {0.f, 0.f, 0.f, 0.f};
#pragma unroll
    for (int t = 0; t < 2; ++t) {
#pragma unroll
        for (int c = 0; c < NC; ++c) { A.a[t][c] = z; asm volatile("" : "+v"(A.a[t][c])); }
        if (XST) { A.sx[t] = z; A.sq[t] = z; asm volatile("" : "+v"(A.sx[t]), "+v"(A.sq[t])); }
    }
    A.cs[0] = z; A.cs[1] = z; asm volatile("" : "+v"(A.cs[0]), "+v"(A.cs[1]));
    if (NC > 8) { A.cs[2] = z; asm volatile("" : "+v"(A.cs[2])); }
}
template <int NC, bool TR, bool XST, int NST, class Hook>
__device__ __forceinline__ void xw_gemm(XWAcc<NC, XST>& A, LAS char* lds, int g0, int m, int lane, Hook hook) {
    const int fr = lane & 15, g = lane >> 4, row = 32 * m + fr;
    constexpr int G = 2 * NC + (NC + 3) / 4 + (XST ? 4 : 0), R = 2 + 2 * NC;
    constexpr int DA = R > G - 7 ? R - (G - 7) : 0, DB = R > G ? R - G : 0;
    LAS char* pX0 = lds + row * 128 + ((g ^ ((row >> 1) & 7)) << 4);
    LAS char* pX1 = lds + ((row * 128 + ((g ^ ((row >> 1) & 7)) << 4)) ^ 64);
    LAS char* pW0 = lds + 16384 + (16 * (g >> 1) + 4 * (g & 1) + (fr >> 2)) * (NC * 32) + (lane & 3) * 8 + m * 32;
    LAS char* pW1 = pW0 + 32 * NC * 32;
    XRot<NC> rot;
#pragma unroll
    for (int k = 0; k < 3; ++k) rot.o[k] = (NC - 3 + k + m >= NC) ? -NC * 32 : 0;
    XF<NC> f0, f1;
    bf16x8 ones = {0x3F80, 0x3F80, 0x3F80, 0x3F80, 0x3F80, 0x3F80, 0x3F80, 0x3F80};
    asm volatile("" : "+v"(ones));
    {
        const unsigned sb = (g0 & 3) * SLOT;
#pragma unroll
        for (int i = 0; i < R; ++i) xa_read1<NC>(f0, i, pX0 + sb, pW0 + sb, rot);
    }
    asm volatile("s_nop 1"); SB0();
#pragma unroll 1
    for (int s = 0; s < NST - 1; ++s) {
        const unsigned sb = ((g0 + s) & 3) * SLOT, sn = ((g0 + s + 1) & 3) * SLOT;
        LAS char* nX = pX0 + sn; LAS char* nW = pW0 + sn;
        xa_step<NC, TR, XST, DA, true>(A, f0, f1, pX1 + sb, pW1 + sb, rot, ones);
        asm volatile("" : "+v"(nX), "+v"(nW));
        WAIT_L0(); BAR(); SB0();
        hook(s);
        xa_step<NC, TR, XST, DB, true>(A, f1, f0, nX, nW, rot, ones);
    }
    const unsigned sb = ((g0 + NST - 1) & 3) * SLOT;
    xa_step<NC, TR, XST, DA, true>(A, f0, f1, pX1 + sb, pW1 + sb, rot, ones);
    WAIT_L0(); BAR(); SB0();
    xa_step<NC, TR, XST, 0, false>(A, f1, f0, pX0, pW0, rot, ones);
    MMA_DRAIN();
}
template <int NC, bool TR, bool XST> __device__ __forceinline__ void share_colsum(const XWAcc<NC, XST>& A, LAS char* lds, int m, int lane) {
    LAS float* tab = (LAS float*)(lds + SP_CS);
    const int fr = lane & 15, g = lane >> 4;
#pragma unroll
    for (int k = 0; k < (NC + 3) / 4; ++k) {
        int c = m + 4 * k; c = c >= NC ? c - NC : c;
        if (TR) { if (fr == 0) *(LAS f32x4*)(tab + c * 16 + 4 * g) = A.cs[k]; }
        else { if (g == 0) tab[c * 16 + fr] = A.cs[k][0]; }
    }
    WAIT_L0(); BAR();
}
template <int NC> __device__ __forceinline__ void unrotate(unsigned (&v)[NC][2], int m) {
    unsigned u[NC][2];
#define UNROT(M) _Pragma("unroll") for (int c = 0; c < NC; ++c) { u[(c + M) % NC][0] = v[c][0]; u[(c + M) % NC][1] = v[c][1]; }
    if (m == 0) { UNROT(0) } else if (m == 1) { UNROT(1) } else if (m == 2) { UNROT(2) } else { UNROT(3) }
#undef UNROT
#pragma unroll
    for (int c = 0; c < NC; ++c) { v[c][0] = u[c][0]; v[c][1] = u[c][1]; asm volatile("" : "+v"(v[c][0]), "+v"(v[c][1])); }
}

struct MFrag { bf16x8 w[2]; bf16x8 z[9]; };
template <bool LOAD> __device__ __forceinline__ void m_step(f32x4 (&acc)[2][9], const MFrag& f, MFrag& n, LAS char* pW, LAS char* pZ) {
#pragma unroll
    for (int i = 0; i < 18; ++i) {
        MMA(acc[i & 1][i >> 1], f.z[i >> 1], f.w[i & 1]);
        if (LOAD) { if (i < 2) n.w[i] = lds_b128(pW + i * 2048); else if (i < 11) n.z[i - 2] = lds_b128(pZ + (i - 2) * 1024); }
        SB0();
    }
}
__device__ __forceinline__ void o_read(MFrag& f, LAS char* pW, LAS char* pZ) {
#pragma unroll
    for (int t = 0; t < 2; ++t) f.w[t] = lds_b128(pW + t * 2048);
#pragma unroll
    for (int c = 0; c < 9; ++c) f.z[c] = lds_b128(pZ + c * 1024);
}
__device__ __forceinline__ void o_mma(f32x4 (&acc)[2][9], const MFrag& f) {
#pragma unroll
    for (int c = 0; c < 9; ++c)
#pragma unroll
        for (int t = 0; t < 2; ++t) acc[t][c] = MFMA(f.z[c], f.w[t], acc[t][c]);
}
__device__ __forceinline__ void o_sched() {
#pragma unroll
    for (int i = 0; i < 11; ++i) { __builtin_amdgcn_sched_group_barrier(0x008, 1, 0); __builtin_amdgcn_sched_group_barrier(0x100, 1, 0); }
    __builtin_amdgcn_sched_group_barrier(0x008, 7, 0);
    SB0();
}
template <int NST> __device__ __forceinline__ void main_gemm(f32x4 (&acc)[2][9], LAS char* lds, int g0, int m, int lane) {
    const int fr = lane & 15, g = lane >> 4, row = 32 * m + fr;
    LAS char* pW0 = lds + row * 128 + ((g ^ ((row >> 1) & 7)) << 4);
    LAS char* pW1 = lds + ((row * 128 + ((g ^ ((row >> 1) & 7)) << 4)) ^ 64);
    LAS char* pZ0 = lds + 16384 + lane * 16;
    LAS char* pZ1 = pZ0 + 9216;
#pragma unroll
    for (int t = 0; t < 2; ++t)
#pragma unroll
        for (int c = 0; c < 9; ++c) acc[t][c] = (f32x4){0.f, 0.f, 0.f, 0.f};
    MFrag f0, f1;
    o_read(f0, pW0 + (g0 & 3) * SLOT, pZ0 + (g0 & 3) * SLOT);
#pragma unroll 1
    for (int s = 0; s < NST - 1; ++s) {
        const unsigned sb = ((g0 + s) & 3) * SLOT, sn = ((g0 + s + 1) & 3) * SLOT;
        o_read(f1, pW1 + sb, pZ1 + sb);
        o_mma(acc, f0);
        o_sched();
        WAIT_L0(); BAR(); SB0();
        o_read(f0, pW0 + sn, pZ0 + sn);
        o_mma(acc, f1);
        o_sched();
    }
    const unsigned sb = ((g0 + NST - 1) & 3) * SLOT;
    o_read(f1, pW1 + sb, pZ1 + sb);
    o_mma(acc, f0);
    o_sched();
    WAIT_L0(); BAR(); SB0();
    o_mma(acc, f1);
}

struct EFrag { s16x4 w[2]; bf16x8 b[5]; };
template <bool LOAD, bool MM> __device__ __forceinline__ void e_step(f32x4 (&acc)[5], const EFrag& f, EFrag& n, LAS char* pW, LAS char* pB) {
    const bf16x8 w = cat8(f.w[0], f.w[1]);
#pragma unroll
    for (int t = 0; t < 5; ++t) {
        if (MM) MMA(acc[t], w, f.b[t]);
        if (LOAD) {
            if (t == 0) { n.w[0] = lds_tr1(pW); n.w[1] = lds_tr1(pW + 2048); }
            else if (t == 1) { n.b[0] = lds_b128(pB); n.b[1] = lds_b128(pB + 1024); }
            else n.b[t] = lds_b128(pB + t * 1024);
        }
        SB0();
    }
}
template <int NST, int NKS, bool MORE, class Hook> __device__ __forceinline__ void e_gemm(f32x4 (&acc)[5], LAS char* lds, int g0, int w, int lane, Hook hook) {
    const int g = lane >> 4, q = (lane & 15) >> 2;
    LAS char* pW = lds + (4 * g + q) * 128 + ((w ^ ((2 * g + (q >> 1)) & 3)) << 5) + (lane & 3) * 8;
    LAS char* pB = lds + 16384 + lane * 16;
#pragma unroll
    for (int t = 0; t < 5; ++t) { acc[t] = (f32x4){0.f, 0.f, 0.f, 0.f}; asm volatile("" : "+v"(acc[t])); }
    EFrag f0, f1;
    e_step<true, false>(acc, f1, f0, pW + (g0 & 3) * SLOT, pB + (g0 & 3) * SLOT);
    asm volatile("s_nop 1"); SB0();
#pragma unroll 1
    for (int s = 0; s < NST - 1; ++s) {
        const unsigned sb = ((g0 + s) & 3) * SLOT, sn = ((g0 + s + 1) & 3) * SLOT;
        e_step<true, true>(acc, f0, f1, pW + sb + 4096, pB + sb + 5120);
        e_step<true, true>(acc, f1, f0, pW + sb + 8192, pB + sb + 10240);
        e_step<true, true>(acc, f0, f1, pW + sb + 12288, pB + sb + 15360);
        WAIT_L0(); BAR(); SB0();
        hook(s);
        e_step<true, true>(acc, f1, f0, pW + sn, pB + sn);
    }
    const unsigned sb = ((g0 + NST - 1) & 3) * SLOT;
    constexpr int LAST = NKS - 4 * (NST - 1);
    e_step<true, true>(acc, f0, f1, pW + sb + 4096, pB + sb + 5120);
    if (LAST == 4) {
        e_step<true, true>(acc, f1, f0, pW + sb + 8192, pB + sb + 10240);
        e_step<true, true>(acc, f0, f1, pW + sb + 12288, pB + sb + 15360);
    }
    if (MORE) { WAIT_L0(); BAR(); SB0(); }
    e_step<false, true>(acc, f1, f0, pW, pB);
    MMA_DRAIN();
}

__device__ __forceinline__ float dot2(unsigned a, unsigned b, float c) { return __builtin_amdgcn_fdot2_f32_bf16(__builtin_bit_cast(bf16x2_t, a), __builtin_bit_cast(bf16x2_t, b), c, false); }
__device__ __forceinline__ unsigned at_region(int r, int p) { return (unsigned)((r >> 1) * SLOT + ((r & 1) ? 16384 + WImg<9>::first(p) * 1024 : p * 4096)); }
__device__ __forceinline__ unsigned at_row(int rho, int p) { const int r = rho / 14; return at_region(r, p) + (unsigned)(rho - r * 14) * 288u; }
__device__ __forceinline__ void attn_issue(rsrc_t rQ, LAS char* lds, int n, int p, int lane, int half) {
#pragma unroll
    for (int rr = 0; rr < 2; ++rr)
#pragma unroll
        for (int q = 0; q < 4; ++q) {
            const int r = 2 * half + rr, c = q * 64 + lane, lr = c / 18, cc = c - lr * 18, rho = r * 14 + lr;
            int t, seg;
            if (rho < 22) { t = 8 * n - 14 + rho; seg = 1; } else if (rho < 44) { t = 8 * n - 14 + (rho - 22); seg = 2; } else { t = 8 * n + (rho - 44); seg = 0; }
            const unsigned voff = (t < 0 || rho >= 52 || lr >= 14) ? 0x7ffffff0u : (unsigned)(t * (QW * 2) + (seg * D_ + 144 * p) * 2 + cc * 16);
            dma16_dev(rQ, lds + at_region(r, p) + q * 1024, voff, 0);
        }
}
__device__ __forceinline__ void attn_compute(LAS char* lds, rsrc_t rO, int n, int p, int lane) {
    const int i = lane >> 3, hh = lane & 7, t = 8 * n + i;
    const unsigned hb = hh * 36, r0 = at_region(0, p) + hb, r1 = at_region(1, p) + hb - 14 * 288, r2 = at_region(2, p) + hb - 28 * 288, r3 = at_region(3, p) + hb - 42 * 288;
    unsigned qv[9];
    {
        LAS unsigned* q = (LAS unsigned*)(lds + r3 + (44 + i) * 288);
#pragma unroll
        for (int d = 0; d < 9; ++d) qv[d] = q[d];
    }
    float lg[8];
#pragma unroll
    for (int w = 0; w < 8; ++w) {
        const int rho = 14 + i - 2 * w;
        LAS unsigned* kp = (LAS unsigned*)(lds + (rho < 14 ? r0 : r1) + rho * 288);
        float s = 0.f;
#pragma unroll
        for (int d = 0; d < 9; ++d) s = dot2(qv[d], kp[d], s);
        lg[w] = (t - 2 * w >= 0) ? s * (0.23570226039551584f * 1.4426950408889634f) : -1e9f;
    }
    float mx = fmaxf(fmaxf(fmaxf(lg[0], lg[1]), fmaxf(lg[2], lg[3])), fmaxf(fmaxf(lg[4], lg[5]), fmaxf(lg[6], lg[7])));
    float sum = 0.f;
#pragma unroll
    for (int w = 0; w < 8; ++w) { lg[w] = __builtin_amdgcn_exp2f(lg[w] - mx); sum += lg[w]; }
    const float inv = __builtin_amdgcn_rcpf(sum);
    unsigned pp[4];
#pragma unroll
    for (int w = 0; w < 4; ++w) pp[w] = pack_bf16(lg[2 * w] * inv, lg[2 * w + 1] * inv);
    float acc[18];
#pragma unroll
    for (int d = 0; d < 18; ++d) acc[d] = 0.f;
#pragma unroll
    for (int w = 0; w < 4; ++w) {
        const int ra = 22 + 14 + i - 4 * w, rb = ra - 2;
        LAS unsigned* va = (LAS unsigned*)(lds + (ra < 28 ? r1 : ra < 42 ? r2 : r3) + ra * 288);
        LAS unsigned* vb = (LAS unsigned*)(lds + (rb < 28 ? r1 : rb < 42 ? r2 : r3) + rb * 288);
#pragma unroll
        for (int d = 0; d < 9; ++d) {
            const unsigned x = va[d], y = vb[d];
            acc[2 * d] = dot2(__builtin_amdgcn_perm(y, x, 0x05040100u), pp[w], acc[2 * d]);
            acc[2 * d + 1] = dot2(__builtin_amdgcn_perm(y, x, 0x07060302u), pp[w], acc[2 * d + 1]);
        }
    }
    LAS char* ob = lds + SP_AO + p * 2304;
#pragma unroll
    for (int d = 0; d < 9; ++d) *(LAS unsigned*)(ob + i * 288 + hh * 36 + d * 4) = pack_bf16(acc[2 * d], acc[2 * d + 1]);
    WAIT_L0();
    {
        const int r = lane & 7, g = (lane >> 3) & 3, tt = n >> 1, rl = (8 * n + r) & 15;
#pragma unroll
        for (int q = 0; q < 2; ++q) {
            const int a = (lane >> 5) + 2 * q;
            const u32x2 lo = *(LAS u32x2*)(ob + r * 288 + (32 * a + 4 * g) * 2), hi = *(LAS u32x2*)(ob + r * 288 + (32 * a + 16 + 4 * g) * 2);
            st128_dev(rO, ((tt * 18 + 4 * p + a) * 64 + rl + 16 * g) * 16, (u32x4){lo.x, lo.y, hi.x, hi.y});
        }
        const u32x2 v = *(LAS u32x2*)(ob + r * 288 + (128 + 4 * g) * 2);
        if (lane < 32) st64_dev(rO, ((tt * 18 + 16 + (p >> 1)) * 64 + rl + 16 * g) * 16 + (p & 1) * 8, v);
    }
}

__global__ __launch_bounds__(512, 2) void fused_kernel(Args a) {
    extern __shared__ __attribute__((aligned(16))) char smem[];
    LAS char* lds = (LAS char*)smem;
    const int wid = __builtin_amdgcn_readfirstlane(threadIdx.x >> 6), lane = threadIdx.x & 63;
    const int xcd = blockIdx.x & 7, loc = blockIdx.x >> 3;
    const int ti = (xcd & 1) * 8 + (loc & 7), tj = (xcd >> 1) * 4 + (loc >> 3);
    const int nt = xcd * 30 + loc;
    const bool fin = loc < 30 && nt < 234;
    const int rb = nt / 9, cb = nt - rb * 9;
    char* ctl = a.ws + WS_CTL;
    u64 tag;
    {
        const u64 e = __hip_atomic_load((const GAS u64*)(ctl + CTL_EP), __ATOMIC_RELAXED, __HIP_MEMORY_SCOPE_AGENT);
        const u64 tg = e * 0x9E3779B97F4A7C15ull + 0xD1B54A32D192ED03ull;
        tag = ((u64)(unsigned)__builtin_amdgcn_readfirstlane((unsigned)(tg >> 32)) << 32) | (unsigned)__builtin_amdgcn_readfirstlane((unsigned)tg);
    }
    const int an = ti * 16 + tj;

    if (wid >= 4) {
        const int p = wid - 4;
        ST_DECL; ST(0);
#define rX mk_rsrc(a.x, S_ * D_ * 2)
#define rQW mk_rsrc(a.wqkv, D_ * QW * 2)
#define rW mk_rsrc(a.win, D_ * F_ * 2)
#define rS mk_rsrc(a.wsp, S_ * S_ * 2)
#define rZ mk_rsrc(a.ws + WS_Z2T, S_ * FH * 2)
#define rQ mk_rsrc(a.ws + WS_QKV, S_ * QW * 2)
#define rO mk_rsrc(a.ws + WS_OF, S_ * D_ * 2)
        RowsImg xi; xi.init(lane, D_ * 2);
        RowsImg si; si.init(lane, S_ * 2);
        WImg<7> wq; wq.init(lane, p, QW * 2);
        WImg<9> wz; wz.init(lane, p, F_ * 2);
        const unsigned sX0 = (unsigned)(ti * 128) * (D_ * 2), sQ = (unsigned)((tj * 27) >> 2) * 32, sZ1 = (unsigned)(tj * 144) * 2, sZ2 = sZ1 + FH * 2;
        const unsigned sS = (unsigned)(ti * 128 + 32 * p) * (S_ * 2), sZ = (unsigned)tj * (64u * 9216u);
        const unsigned vl = lane * 16;
        const unsigned vE = (lane >> 3) * (D_ * 2) + ((((lane & 7) >> 1) ^ ((lane >> 4) & 3)) << 5) + (lane & 1) * 16;
        const int zI0 = WImg<9>::first(p);
        auto iss_q = [&](int g) {
            LAS char* slot = lds + (g & 3) * SLOT;
            xi.issue35(rX, slot, sX0 + g * 128, D_ * 2, p); wq.issue(rQW, slot + 16384, sQ + g * 64 * (QW * 2), p);
        };
        auto iss_z = [&](int g, int s, unsigned col, bool even = false) {
            LAS char* slot = lds + (g & 3) * SLOT;
            if (even) xi.issue(rX, slot + p * 4096, sX0 + (unsigned)(32 * p) * (D_ * 2) + s * 128, D_ * 2); else xi.issue35(rX, slot, sX0 + s * 128, D_ * 2, p);
            wz.issue(rW, slot + 16384, col + s * 64 * (F_ * 2), p);
        };
        auto iss_m = [&](int g) {
            LAS char* slot = lds + (g & 3) * SLOT; const int s = g - G_MAIN;
            si.issue(rS, slot + p * 4096, sS + s * 128, S_ * 2);
#pragma unroll
            for (int q = 0; q < 4; ++q) dma16_dev(rZ, slot + 16384 + (zI0 + q) * 1024, vl, sZ + s * 18432 + (zI0 + q) * 1024);
            if (p < 2) dma16_dev(rZ, slot + 16384 + (zI0 + 4) * 1024, vl, sZ + s * 18432 + (zI0 + 4) * 1024);
        };
        unsigned eT[5];
#pragma unroll
        for (int t = 0; t < 5; ++t) eT[t] = (unsigned)__builtin_amdgcn_readfirstlane(min(rb * 5 + t, 127));
        auto iss_e = [&](int g, bool isS) {
            LAS char* slot = lds + (g & 3) * SLOT;
            const int lim = isS ? 16 : 64, nks = isS ? 18 : 72;
            int ks = 4 * (g - (isS ? G_S : G_G)) + p; if (isS && ks > 17) ks = 17;
            const int c0 = ks < lim ? 9 * (ks >> 2) + 2 * (ks & 3) : 18 * (ks - lim) + 8, c1 = ks < lim ? c0 + 1 : c0 + 9;
            const rsrc_t rw = mk_rsrc(isS ? a.wo : a.wout, isS ? D_ * D_ * 2 : FH * D_ * 2), rb_ = mk_rsrc(a.ws + (isS ? WS_OF : WS_HF), isS ? S_ * D_ * 2 : S_ * FH * 2);
#pragma unroll
            for (int q = 0; q < 4; ++q) dma16(rw, slot + p * 4096 + q * 1024, vE, (unsigned)((q < 2 ? c0 : c1) * 16 + 8 * (q & 1)) * (D_ * 2) + cb * 128);
#pragma unroll
            for (int t = 0; t < 5; ++t) dma16_dev(rb_, slot + 16384 + p * 5120 + t * 1024, vl, (eT[t] * nks + ks) * 1024);
        };
#define PSTEP(K, PH) do { WAIT_V(7 * (K)); BAR(); } while (0)
#define SPIN_UNTIL(cond) do { unsigned spins_ = 0; while (!(cond) && ++spins_ < SPIN_CAP) __builtin_amdgcn_s_sleep(4); asm volatile("" ::: "memory"); } while (0)
        iss_q(0); iss_q(1); iss_q(2); iss_q(3);
        {
            const unsigned W = blockIdx.x * 4 + p;
            touch_lines(rX, lds + SP_PF + wid * 256, W * 18, 18, lane);
            touch_lines(rQW, lds + SP_PF + wid * 256, W * 16, 16, lane);
            touch_lines(rW, lds + SP_PF + wid * 256, W * 41, 41, lane);
        }
        PSTEP(3, 0);
#pragma unroll 1
        for (int g = 1; g <= 5; ++g) { PSTEP(2, 0); iss_q(g + 3); }
#pragma unroll 1
        for (int g = 6; g <= 14; ++g) { PSTEP(2, 1); if (g == G_Z2) BAR(); iss_z(g + 3, g + 3 - G_Z2, sZ2); }
        PSTEP(2, 1); iss_z(18, 0, sZ1);
        PSTEP(2, 1); iss_z(19, 1, sZ1);
        PSTEP(2, 1);
        {
            const int pa = (8 * an - 14) >> 7;
            SPIN_UNTIL(flags_ready(ctl + CTL_QF + ti * 512, 64, ctl + CTL_QF + (pa < 0 ? ti : pa) * 512, 64, tag, lane));
            attn_issue(rQ, lds, an, p, lane, 0);
        }
        PSTEP(1, 2);
        BAR();
        attn_issue(rQ, lds, an, p, lane, 1);
        {
            const unsigned W = blockIdx.x * 4 + p;
            touch_lines(rS, lds + SP_PF + wid * 256, W * 64, 64, lane);
            touch_lines(mk_rsrc(a.wout, FH * D_ * 2), lds + SP_PF + wid * 256, W * 21, 21, lane);
            touch_lines(mk_rsrc(a.wo, D_ * D_ * 2), lds + SP_PF + wid * 256, W * 6, 6, lane);
        }
        ST(1);
        WAIT_V(3);
        __builtin_amdgcn_s_setprio(ATT_PRIO);
        attn_compute(lds, rO, an, p, lane);
        __builtin_amdgcn_s_setprio(0);
        ST(2);
        iss_z(20, 2, sZ1, true); iss_z(21, 3, sZ1, true);
        publish(ctl, CTL_OF + (an * 4 + p) * 8, tag, lane);
        ST(3);
#pragma unroll 1
        for (int g = 19; g <= 23; ++g) { PSTEP(2, 2); iss_z(g + 3, g + 3 - G_Z1, sZ1); }
        PSTEP(2, 2); PSTEP(1, 2); PSTEP(0, 2);
        SPIN_UNTIL(flags_ready(ctl + CTL_ZF + tj * 512, 64, ctl, 0, tag, lane));
        iss_m(27);
        PSTEP(0, 3);
        BAR();
        iss_m(28); iss_m(29); iss_m(30);
        if (fin) SPIN_UNTIL(flags_ready(ctl + CTL_OF + rb * 320, rb == 25 ? 24 : 40, ctl, 0, tag, lane));
#pragma unroll 1
        for (int g = 28; g <= 55; ++g) { PSTEP(2, 3); iss_m(g + 3); }
        if (!fin) { PSTEP(2, 3); PSTEP(1, 3); PSTEP(0, 3); BAR(); }
        else {
            PSTEP(2, 3);
            iss_e(59, true);
#pragma unroll 1
            for (int g = 57; g <= 60; ++g) { PSTEP(2, 4); iss_e(g + 3, true); }
            PSTEP(2, 4); PSTEP(1, 4); PSTEP(0, 4);
            {
                const int p0 = (rb * 80) >> 7, p1 = min(rb * 80 + 79, S_ - 1) >> 7;
                SPIN_UNTIL(flags_ready(ctl + CTL_HF + p0 * 512, 64, ctl + CTL_HF + p1 * 512, 64, tag, lane));
            }
            iss_e(64, false); iss_e(65, false);
            PSTEP(1, 5); iss_e(66, false); iss_e(67, false);
#pragma unroll 1
            for (int g = 65; g <= 78; ++g) { PSTEP(2, 5); iss_e(g + 3, false); }
            PSTEP(2, 5); PSTEP(1, 5); PSTEP(0, 5);
        }
        ST_END(1);
        asm volatile("fusedk_pf_end:");
        return;
    }

    const int m = wid, fr = lane & 15, g4 = lane >> 4;
    ST_DECL; ST(0);
    const rsrc_t rQs = mk_rsrc(a.ws + WS_QKV, S_ * QW * 2), rZs = mk_rsrc(a.ws + WS_Z2T, S_ * FH * 2), rHs = mk_rsrc(a.ws + WS_HF, S_ * FH * 2), rSt = mk_rsrc(a.ws + WS_ST, S_ * 16 * 16);
    LAS float* cst = (LAS float*)(lds + SP_CS);
    float xm[2], xr[2];
    {
        unsigned dummy = 0;
        if (wid == 0) {
            u64 base; unsigned sz;
            asm volatile("s_getpc_b64 %0\nfusedk_pf_begin:\n\ts_mov_b32 %1, fusedk_pf_end-fusedk_pf_begin" : "=s"(base), "=s"(sz));
            const unsigned off = (unsigned)(loc * 13 + lane) * 128u;
            if (lane < 13 && (int)off < (int)sz) asm volatile("global_load_dword %0, %1, off" : "=v"(dummy) : "v"(base + off));
        }
        asm volatile("s_waitcnt vmcnt(0)" : "+v"(dummy));
    }
    BAR();
    ST(1);
    {
        XWAcc<7, true> A; xw_zero(A);
        xw_gemm<7, true, true, 9>(A, lds, G_QKV, m, lane, NoHook());
        ST(2);
        share_colsum<7, true, true>(A, lds, m, lane);
#pragma unroll
        for (int t = 0; t < 2; ++t) {
            const int r3 = fr & 3;
            const float dq = r3 == 0 ? A.sq[t][0] : r3 == 1 ? A.sq[t][1] : r3 == 2 ? A.sq[t][2] : A.sq[t][3];
            const float sq = __shfl(dq, (fr >> 2) * 16 + fr);
            xm[t] = A.sx[t][0] * (1.0f / D_);
            xr[t] = __builtin_amdgcn_rsqf(fmaxf(sq * (1.0f / D_) - xm[t] * xm[t], 0.f) + 1e-5f);
        }
        const int c0 = (tj * 27) >> 2, dup = tj > 0 && c0 < (((tj - 1) * 27) >> 2) + 7;
#pragma unroll
        for (int t = 0; t < 2; ++t) {
            unsigned pk[7][2];
#pragma unroll
            for (int c = 0; c < 7; ++c) {
                const int tile = c + m >= 7 ? c + m - 7 : c + m;
                const f32x4 cs = *(LAS f32x4*)(cst + tile * 16 + 4 * g4);
                pk[c][0] = pack2((lo2(A.a[t][c]) - lo2(cs) * xm[t]) * xr[t]); pk[c][1] = pack2((hi2(A.a[t][c]) - hi2(cs) * xm[t]) * xr[t]);
            }
            unrotate<7>(pk, m);
            const unsigned ro = (unsigned)(ti * 128 + 32 * m + 16 * t + fr) * (QW * 2) + c0 * 32;
#pragma unroll
            for (int pp = 0; pp < 3; ++pp) st128_dev(rQs, ro + (1 + 2 * pp) * 32 + g4 * 16, gather8(pk[1 + 2 * pp][0], pk[1 + 2 * pp][1], pk[2 + 2 * pp][0], pk[2 + 2 * pp][1]));
            if (!dup) st64_dev(rQs, ro + g4 * 8, (u32x2){pk[0][0], pk[0][1]});
        }
    }
    ST(3);
    unsigned zp[2][9][2];
    {
        XWAcc<9, false> A; xw_zero(A);
        xw_gemm<9, false, false, 9>(A, lds, G_Z2, m, lane, [&](int s) { if (s == 4) publish(ctl, CTL_QF + ((ti * 16 + tj) * 4 + m) * 8, tag, lane); });
        ST(4);
        share_colsum<9, false, false>(A, lds, m, lane);
        f32x2 rm[2][2], rr[2][2];
#pragma unroll
        for (int t = 0; t < 2; ++t)
#pragma unroll
            for (int h = 0; h < 2; ++h) {
                rm[t][h] = (f32x2){__shfl(xm[t], 4 * g4 + 2 * h), __shfl(xm[t], 4 * g4 + 2 * h + 1)};
                rr[t][h] = (f32x2){__shfl(xr[t], 4 * g4 + 2 * h), __shfl(xr[t], 4 * g4 + 2 * h + 1)};
            }
        f32x2 s1[2][2], s2[2][2];
#pragma unroll
        for (int t = 0; t < 2; ++t)
#pragma unroll
            for (int h = 0; h < 2; ++h) { s1[t][h] = (f32x2){0.f, 0.f}; s2[t][h] = (f32x2){0.f, 0.f}; }
#pragma unroll
        for (int c = 0; c < 9; ++c) {
            const float cs = cst[(c + m >= 9 ? c + m - 9 : c + m) * 16 + fr];
#pragma unroll
            for (int t = 0; t < 2; ++t)
#pragma unroll
                for (int h = 0; h < 2; ++h) {
                    const f32x2 v = gelu2(((h ? hi2(A.a[t][c]) : lo2(A.a[t][c])) - rm[t][h] * cs) * rr[t][h]);
                    s1[t][h] += v; s2[t][h] += v * v;
                    zp[t][c][h] = pack2(v);
                    asm volatile("" : "+v"(zp[t][c][h]));
                }
        }
        float mm = 0.f, qq = 0.f;
        {
            const int idx = lane & 7;
            float a1[8], a2[8];
#pragma unroll
            for (int k = 0; k < 8; ++k) { a1[k] = s1[k >> 2][(k >> 1) & 1][k & 1]; a2[k] = s2[k >> 2][(k >> 1) & 1][k & 1]; }
#pragma unroll
            for (int k = 0; k < 8; ++k) { a1[k] = dpp_add<0x128>(a1[k]); a2[k] = dpp_add<0x128>(a2[k]); }
#pragma unroll
            for (int k = 0; k < 8; ++k) { a1[k] = dpp_add<0x124>(a1[k]); a2[k] = dpp_add<0x124>(a2[k]); }
#pragma unroll
            for (int k = 0; k < 8; ++k) { a1[k] = dpp_add<0x122>(a1[k]); a2[k] = dpp_add<0x122>(a2[k]); }
#pragma unroll
            for (int k = 0; k < 8; ++k) { a1[k] = dpp_add<0x121>(a1[k]); a2[k] = dpp_add<0x121>(a2[k]); }
            float u1 = a1[0], u2 = a2[0];
#pragma unroll
            for (int k = 1; k < 8; ++k) { u1 = idx == k ? a1[k] : u1; u2 = idx == k ? a2[k] : u2; }
            mm = u1 * (1.0f / 144.0f);
            qq = fmaxf(u2 - u1 * mm, 0.f);
            if (fr < 8) st128_dev(rSt, ((ti * 16 + tj) * 128 + 32 * m + 16 * (idx >> 2) + 4 * g4 + (idx & 3)) * 16,
                                  (u32x4){__float_as_uint(mm), (unsigned)tag, __float_as_uint(qq), (unsigned)(tag >> 32)});
        }
    }
    ST(5);
    unsigned z1p[2][9][2];
    {
        XWAcc<9, false> A; xw_zero(A);
        xw_gemm<9, true, false, 2>(A, lds, G_Z1, m, lane, NoHook());
        ST(6);
        float mu, rs;
        {
            const unsigned off = ((ti * 16 + (lane >> 5) * 8) * 128 + 32 * m + (lane & 31)) * 16;
            u32x4 v[8]; unsigned spins = 0;
            for (;;) {
#pragma unroll
                for (int k = 0; k < 8; ++k) v[k] = __builtin_amdgcn_raw_buffer_load_b128(rSt, off + k * 2048, 0, 16);
                unsigned bad = 0;
#pragma unroll
                for (int k = 0; k < 8; ++k) bad |= (v[k].y ^ (unsigned)tag) | (v[k].w ^ (unsigned)(tag >> 32));
                if (__all(bad == 0) || ++spins > SPIN_CAP) break;
                __builtin_amdgcn_s_sleep(2);
            }
            float s = 0.f;
#pragma unroll
            for (int k = 0; k < 8; ++k) s += __uint_as_float(v[k].x);
            s += xchg32(s);
            mu = s * (1.0f / 16.0f);
            float q = 0.f;
#pragma unroll
            for (int k = 0; k < 8; ++k) { const float d = __uint_as_float(v[k].x) - mu; q += __uint_as_float(v[k].z) + 144.0f * d * d; }
            q += xchg32(q);
            rs = __builtin_amdgcn_rsqf(q * (1.0f / FH) + 1e-5f);
        }
        ST(7);
        f32x2 mr[2][2], rr[2][2];
#pragma unroll
        for (int t = 0; t < 2; ++t)
#pragma unroll
            for (int h = 0; h < 2; ++h) {
                const int src = 16 * t + 4 * g4 + 2 * h;
                mr[t][h] = (f32x2){__shfl(mu, src), __shfl(mu, src + 1)}; rr[t][h] = (f32x2){__shfl(rs, src), __shfl(rs, src + 1)};
            }
        const unsigned zo = (unsigned)((tj * 64 + ti * 4 + m) * 9) * 1024 + lane * 16;
#pragma unroll
        for (int c = 0; c < 9; ++c) {
            const unsigned x0 = pack2((unpack2(zp[0][c][0]) - mr[0][0]) * rr[0][0]), x1 = pack2((unpack2(zp[0][c][1]) - mr[0][1]) * rr[0][1]);
            const unsigned y0 = pack2((unpack2(zp[1][c][0]) - mr[1][0]) * rr[1][0]), y1 = pack2((unpack2(zp[1][c][1]) - mr[1][1]) * rr[1][1]);
            st128_dev(rZs, zo + (c + m >= 9 ? c + m - 9 : c + m) * 1024, gather8(x0, x1, y0, y1));
        }
        ST(8);
        xw_gemm<9, true, false, 7>(A, lds, G_Z1 + 2, m, lane, [&](int s) { if (s == 1) publish(ctl, CTL_ZF + ((tj * 16 + ti) * 4 + m) * 8, tag, lane); });
        ST(9);
        share_colsum<9, true, false>(A, lds, m, lane);
#pragma unroll
        for (int c = 0; c < 9; ++c) {
            const f32x4 cs = *(LAS f32x4*)(cst + (c + m >= 9 ? c + m - 9 : c + m) * 16 + 4 * g4);
#pragma unroll
            for (int t = 0; t < 2; ++t) {
                z1p[t][c][0] = pack2(gelu2((lo2(A.a[t][c]) - lo2(cs) * xm[t]) * xr[t])); z1p[t][c][1] = pack2(gelu2((hi2(A.a[t][c]) - hi2(cs) * xm[t]) * xr[t]));
                asm volatile("" : "+v"(z1p[t][c][0]), "+v"(z1p[t][c][1]));
            }
        }
        unrotate<9>(z1p[0], m); unrotate<9>(z1p[1], m);
    }
    ST(10);
    {
        f32x4 acc[2][9];
        float bt[2];
#pragma unroll
        for (int t = 0; t < 2; ++t) bt[t] = bf2f(a.bsp[ti * 128 + 32 * m + 16 * t + fr]);
        main_gemm<32>(acc, lds, G_MAIN, m, lane);
        ST(11);
#pragma unroll
        for (int t = 0; t < 2; ++t) {
            const unsigned hp = (unsigned)(ti * 8 + 2 * m + t) * 72 * 1024 + lane * 16;
#pragma unroll
            for (int pp = 0; pp < 4; ++pp) {
                u32x4 o;
                o.x = pack2((lo2(acc[t][2 * pp]) + bt[t]) * unpack2(z1p[t][2 * pp][0])); o.y = pack2((hi2(acc[t][2 * pp]) + bt[t]) * unpack2(z1p[t][2 * pp][1]));
                o.z = pack2((lo2(acc[t][2 * pp + 1]) + bt[t]) * unpack2(z1p[t][2 * pp + 1][0])); o.w = pack2((hi2(acc[t][2 * pp + 1]) + bt[t]) * unpack2(z1p[t][2 * pp + 1][1]));
                st128_dev(rHs, hp + (tj * 4 + pp) * 1024, o);
            }
            u32x2 o;
            o.x = pack2((lo2(acc[t][8]) + bt[t]) * unpack2(z1p[t][8][0])); o.y = pack2((hi2(acc[t][8]) + bt[t]) * unpack2(z1p[t][8][1]));
            st64_dev(rHs, hp + (64 + (tj >> 1)) * 1024 + (tj & 1) * 8, o);
        }
    }
    ST(12);
    const size_t hflag = CTL_HF + ((ti * 16 + tj) * 4 + m) * 8;
    if (!fin) { publish(ctl, hflag, tag, lane); ST_END(0); return; }
    {
        f32x4 sa[5], ga[5];
        e_gemm<5, 18, true>(sa, lds, G_S, m, lane, [&](int s) { if (s == 0) publish(ctl, hflag, tag, lane); });
        ST(13);
        u32x2 xv[5];
#pragma unroll
        for (int t = 0; t < 5; ++t) xv[t] = *(const u32x2*)(a.x + (size_t)(min(rb * 5 + t, 127) * 16 + fr) * D_ + cb * 64 + m * 16 + g4 * 4);
        e_gemm<18, 72, false>(ga, lds, G_G, m, lane, NoHook());
        ST(14);
#pragma unroll
        for (int t = 0; t < 5; ++t) {
            if (rb * 5 + t < 128) {
                float o[4];
#pragma unroll
                for (int r = 0; r < 4; ++r) {
                    const float gv = ga[t][r], sg = gv * __builtin_amdgcn_rcpf(1.0f + __builtin_amdgcn_exp2f(gv * -1.4426950408889634f));
                    const float xx = (r & 1) ? bfhi(xv[t][r >> 1]) : bflo(xv[t][r >> 1]);
                    o[r] = xx + sa[t][r] * sg;
                }
                u32x2 ov; ov.x = pack_bf16(o[0], o[1]); ov.y = pack_bf16(o[2], o[3]);
                *(u32x2*)(a.out + (size_t)((rb * 5 + t) * 16 + fr) * D_ + cb * 64 + m * 16 + g4 * 4) = ov;
            }
        }
    }
    if (blockIdx.x == 0 && threadIdx.x == 0) __hip_atomic_store((GAS u64*)(ctl + CTL_EP), tag, __ATOMIC_RELAXED, __HIP_MEMORY_SCOPE_AGENT);
    ST(15);
    ST_END(0);
}

extern "C" void kernel_launch(void* const* d_in, const int* in_sizes, int n_in, void* d_out, int out_size, void* d_ws, size_t ws_size, hipStream_t stream) {
    Args a;
    a.x = (const u16*)d_in[0]; a.wqkv = (const u16*)d_in[1]; a.wo = (const u16*)d_in[2]; a.win = (const u16*)d_in[4]; a.wsp = (const u16*)d_in[5];
    a.bsp = (const u16*)d_in[6]; a.wout = (const u16*)d_in[7]; a.out = (u16*)d_out; a.ws = (char*)d_ws;
    (void)hipFuncSetAttribute((const void*)fused_kernel, hipFuncAttributeMaxDynamicSharedMemorySize, LDS_BYTES);
    fused_kernel<<<256, 512, LDS_BYTES, stream>>>(a);
}
```

```cpp
#include <hip/hip_runtime.h>
#include <hip/hip_bf16.h>
#include <stdint.h>

typedef unsigned short u16;
typedef unsigned long long u64;
typedef short bf16x8 __attribute__((ext_vector_type(8)));
typedef short s16x4 __attribute__((ext_vector_type(4)));
typedef float f32x4 __attribute__((ext_vector_type(4)));
typedef float f32x2 __attribute__((ext_vector_type(2)));
typedef unsigned u32x4 __attribute__((ext_vector_type(4)));
typedef unsigned u32x2 __attribute__((ext_vector_type(2)));
typedef __bf16 bf16x2_t __attribute__((ext_vector_type(2)));
#define LAS __attribute__((address_space(3)))
#define GAS __attribute__((address_space(1)))
typedef LAS void* lds_vp;
typedef __amdgpu_buffer_rsrc_t rsrc_t;

constexpr int S_ = 2048, D_ = 576, F_ = 4608, FH = 2304, QW = 1728;
constexpr int SLOT = 36864, SPARE = 4 * SLOT, LDS_BYTES = 163840;
constexpr int SP_CS = SPARE, SP_PF = SPARE + 1024, SP_AO = SPARE + 4096;
constexpr size_t WS_Z2T = 0;
constexpr size_t WS_QKV = WS_Z2T + (size_t)S_ * FH * 2;
constexpr size_t WS_HF = WS_QKV + (size_t)S_ * QW * 2;
constexpr size_t WS_OF = WS_HF + (size_t)S_ * FH * 2;
constexpr size_t WS_ST = WS_OF + (size_t)S_ * D_ * 2;
constexpr size_t WS_CTL = WS_ST + (size_t)S_ * 16 * 16;
constexpr size_t CTL_EP = 0, CTL_QF = 1024, CTL_ZF = CTL_QF + 8192, CTL_OF = CTL_ZF + 8192, CTL_HF = CTL_OF + 8192, CTL_END = CTL_HF + 8192;
constexpr int G_QKV = 0, G_Z2 = 9, G_Z1 = 18, G_MAIN = 27, G_G = 59, G_S = 77;
constexpr unsigned SPIN_CAP = 400000u;

__device__ __forceinline__ float bflo(unsigned v) { return __uint_as_float(v << 16); }
__device__ __forceinline__ float bfhi(unsigned v) { return __uint_as_float(v & 0xffff0000u); }
__device__ __forceinline__ float bf2f(u16 v) { return __uint_as_float(((unsigned)v) << 16); }
__device__ __forceinline__ unsigned pack_bf16(float lo, float hi) {
    f32x2 v = {lo, hi};
    return __builtin_bit_cast(unsigned, __builtin_convertvector(v, bf16x2_t));
}
#define BAR() do { asm volatile("" ::: "memory"); __builtin_amdgcn_s_barrier(); asm volatile("" ::: "memory"); } while (0)
#define WAIT_V(n) asm volatile("s_waitcnt vmcnt(%0)" ::"n"(n) : "memory")
#define WAIT_L0() asm volatile("s_waitcnt lgkmcnt(0)" ::: "memory")
#define SB0() __builtin_amdgcn_sched_barrier(0)
#define ST_DECL
#define ST(k)
#define ST_END(who)

__device__ __forceinline__ rsrc_t mk_rsrc(const void* p, unsigned bytes) { return __builtin_amdgcn_make_buffer_rsrc((void*)p, 0, bytes, 0x00020000); }
__device__ __forceinline__ void dma16(rsrc_t r, LAS char* lds, unsigned voff, unsigned soff) {
    __builtin_amdgcn_raw_ptr_buffer_load_lds(r, (lds_vp)lds, 16, voff, soff, 0, 0);
}
__device__ __forceinline__ void dma16_dev(rsrc_t r, LAS char* lds, unsigned voff, unsigned soff) {
    __builtin_amdgcn_raw_ptr_buffer_load_lds(r, (lds_vp)lds, 16, voff, soff, 0, 16);
}
#define ATT_PRIO 1
#define STAGE_ST 0
#define ST_AUX 16
__device__ __forceinline__ void touch_lines(rsrc_t r, LAS char* scratch, unsigned first, unsigned cnt, int lane) {
    __builtin_amdgcn_raw_ptr_buffer_load_lds(r, (lds_vp)scratch, 4, (unsigned)lane < cnt ? (first + lane) * 128u : 0x7ffffff0u, 0, 0, 0);
}
__device__ __forceinline__ void st128_dev(rsrc_t r, unsigned off, u32x4 v) { __builtin_amdgcn_raw_buffer_store_b128(v, r, off, 0, ST_AUX); }
__device__ __forceinline__ void st64_dev(rsrc_t r, unsigned off, u32x2 v) { __builtin_amdgcn_raw_buffer_store_b64(v, r, off, 0, ST_AUX); }
__device__ __forceinline__ bf16x8 lds_b128(LAS char* p) { return *(LAS bf16x8*)p; }
__device__ __forceinline__ bf16x8 lds_tr(LAS char* p, int off1) {
    s16x4 a = __builtin_amdgcn_ds_read_tr16_b64_v4i16((LAS s16x4*)p);
    s16x4 b = __builtin_amdgcn_ds_read_tr16_b64_v4i16((LAS s16x4*)(p + off1));
    return __builtin_shufflevector(a, b, 0, 1, 2, 3, 4, 5, 6, 7);
}
#define MFMA(a, b, c) __builtin_amdgcn_mfma_f32_16x16x32_bf16(a, b, c, 0, 0, 0)
template <int CTRL> __device__ __forceinline__ float dpp_add(float x) {
    return x + __builtin_bit_cast(float, __builtin_amdgcn_update_dpp(0, __builtin_bit_cast(int, x), CTRL, 0xf, 0xf, false));
}
__device__ __forceinline__ float row16_sum(float x) { x = dpp_add<0x128>(x); x = dpp_add<0x124>(x); x = dpp_add<0x122>(x); x = dpp_add<0x121>(x); return x; }
__device__ __forceinline__ float gelu_f(float x) {
    const float t = x * (-2.3022081498f - 0.1029432397f * x * x);
    return x * __builtin_amdgcn_rcpf(1.0f + __builtin_amdgcn_exp2f(t));
}
__device__ __forceinline__ f32x2 gelu2(f32x2 x) {
    const f32x2 t = x * (x * x * -0.1029432397f + -2.3022081498f);
    f32x2 e; e.x = __builtin_amdgcn_exp2f(t.x); e.y = __builtin_amdgcn_exp2f(t.y);
    const f32x2 d = e + 1.0f;
    f32x2 r; r.x = __builtin_amdgcn_rcpf(d.x); r.y = __builtin_amdgcn_rcpf(d.y);
    return x * r;
}
__device__ __forceinline__ f32x2 lo2(f32x4 v) { return (f32x2){v[0], v[1]}; }
__device__ __forceinline__ f32x2 hi2(f32x4 v) { return (f32x2){v[2], v[3]}; }
__device__ __forceinline__ unsigned pack2(f32x2 v) { return __builtin_bit_cast(unsigned, __builtin_convertvector(v, bf16x2_t)); }
__device__ __forceinline__ f32x2 unpack2(unsigned v) { return (f32x2){__uint_as_float(v << 16), __uint_as_float(v & 0xffff0000u)}; }
__device__ __forceinline__ int swap23(int r) { return (r & 0x13) | ((r & 4) << 1) | ((r & 8) >> 1); }
__device__ __forceinline__ float xchg32(float x) {
    auto r = __builtin_amdgcn_permlane32_swap(__float_as_uint(x), __float_as_uint(x), false, false);
    return __uint_as_float((threadIdx.x & 32) ? r[0] : r[1]);
}
__device__ __forceinline__ u32x4 gather8(unsigned x0, unsigned x1, unsigned y0, unsigned y1) {
    auto s0 = __builtin_amdgcn_permlane32_swap(x0, y0, false, false);
    auto s1 = __builtin_amdgcn_permlane32_swap(x1, y1, false, false);
    auto t0 = __builtin_amdgcn_permlane16_swap(s0[0], s0[1], false, false);
    auto t1 = __builtin_amdgcn_permlane16_swap(s1[0], s1[1], false, false);
    return (u32x4){t0[0], t1[0], t0[1], t1[1]};
}
__device__ __forceinline__ void publish(char* ctl, size_t word, u64 tag, int lane) {
    asm volatile("s_waitcnt vmcnt(0)" ::: "memory");
    if (lane == 0) __hip_atomic_store((GAS u64*)(ctl + word), tag, __ATOMIC_RELAXED, __HIP_MEMORY_SCOPE_AGENT);
}
__device__ __forceinline__ bool flags_ready(const char* f0, int n0, const char* f1, int n1, u64 tag, int lane) {
    u64 a = tag, b = tag;
    if (lane < n0) a = __hip_atomic_load((const GAS u64*)(f0 + lane * 8), __ATOMIC_RELAXED, __HIP_MEMORY_SCOPE_AGENT);
    if (lane < n1) b = __hip_atomic_load((const GAS u64*)(f1 + lane * 8), __ATOMIC_RELAXED, __HIP_MEMORY_SCOPE_AGENT);
    return __all(a == tag && b == tag);
}

struct Args { const u16* x; const u16* wqkv; const u16* wo; const u16* win; const u16* wsp; const u16* bsp; const u16* wout; u16* out; char* ws; };

struct RowsImg {
    unsigned v0, v1;
    __device__ __forceinline__ void init(int lane, unsigned rs) {
        const int r8 = lane >> 3, ch = lane & 7;
        v0 = r8 * rs + ((ch ^ ((lane >> 4) & 7)) << 4); v1 = r8 * rs + ((ch ^ ((4 + (lane >> 4)) & 7)) << 4);
    }
    __device__ __forceinline__ void issue(rsrc_t r, LAS char* dst, unsigned soff, unsigned rs) const {
#pragma unroll
        for (int q = 0; q < 4; ++q) dma16(r, dst + q * 1024, (q & 1) ? v1 : v0, soff + q * 8 * rs);
    }
    __device__ __forceinline__ void issue35(rsrc_t r, LAS char* dst, unsigned soff, unsigned rs, int p) const {
        const int q0 = p < 2 ? 3 * p : 6 + 5 * (p - 2);
        const unsigned va = (q0 & 1) ? v1 : v0, vb = (q0 & 1) ? v0 : v1;
#pragma unroll
        for (int q = 0; q < 3; ++q) dma16(r, dst + (q0 + q) * 1024, (q & 1) ? vb : va, soff + (q0 + q) * 8 * rs);
        if (p >= 2) {
#pragma unroll
            for (int q = 3; q < 5; ++q) dma16(r, dst + (q0 + q) * 1024, (q & 1) ? vb : va, soff + (q0 + q) * 8 * rs);
        }
    }
};
template <int NC> struct WImg {
    static constexpr int CPR = 2 * NC, BIG = (NC == 9) ? 5 : 4;
    unsigned v[BIG];
    static __device__ __forceinline__ int first(int p) { return NC == 9 ? (p < 2 ? 5 * p : 10 + 4 * (p - 2)) : (p < 2 ? 4 * p : 8 + 3 * (p - 2)); }
    __device__ __forceinline__ void init(int lane, int p, unsigned ldb) {
        const int I0 = first(p);
#pragma unroll
        for (int q = 0; q < BIG; ++q) {
            const int n = (I0 + q) * 64 + lane, pos = n / CPR, cc = n - pos * CPR;
            v[q] = ((pos & 32) + swap23(pos & 31)) * ldb + cc * 16;
        }
    }
    __device__ __forceinline__ void issue(rsrc_t r, LAS char* dst, unsigned soff, int p) const {
        const int I0 = first(p);
#pragma unroll
        for (int q = 0; q < BIG - 1; ++q) dma16(r, dst + (I0 + q) * 1024, v[q], soff);
        if (p < 2) dma16(r, dst + (I0 + BIG - 1) * 1024, v[BIG - 1], soff);
    }
};

#define MMA(acc, a, b) asm volatile("v_mfma_f32_16x16x32_bf16 %0, %1, %2, %0" : "+v"(acc) : "v"(a), "v"(b))
#define MMA_DRAIN() do { SB0(); asm volatile("s_nop 7\n\ts_nop 3"); SB0(); } while (0)
struct NoHook { __device__ __forceinline__ void operator()(int) const {} };
__device__ __forceinline__ s16x4 lds_tr1(LAS char* p) { return __builtin_amdgcn_ds_read_tr16_b64_v4i16((LAS s16x4*)p); }
__device__ __forceinline__ bf16x8 cat8(s16x4 a, s16x4 b) { return __builtin_shufflevector(a, b, 0, 1, 2, 3, 4, 5, 6, 7); }

template <int NC, bool XST> struct XWAcc { f32x4 a[2][NC]; f32x4 cs[3]; f32x4 sx[2], sq[2]; };
template <int NC> struct XF { bf16x8 x[2]; s16x4 w[NC][2]; };
template <int NC> struct XRot { int o[3]; };
template <int NC> __device__ __forceinline__ void xa_read1(XF<NC>& f, int i, LAS char* pX, LAS char* pW, const XRot<NC>& rot) {
    if (i < 2) f.x[i] = lds_b128(pX + i * 2048);
    else {
        const int c = (i - 2) >> 1;
        LAS char* q = c >= NC - 3 ? pW + rot.o[c >= NC - 3 ? c - (NC - 3) : 0] : pW;
        f.w[c][i & 1] = lds_tr1(q + c * 32 + (i & 1) * (8 * NC * 32));
    }
}
template <int NC, bool TR, bool XST, int DBL, bool LOAD>
__device__ __forceinline__ void xa_step(XWAcc<NC, XST>& A, const XF<NC>& f, XF<NC>& n, LAS char* pX, LAS char* pW, const XRot<NC>& rot, const bf16x8& ones) {
    constexpr int R = 2 + 2 * NC;
    int rd = 0, gap = 0;
#define XA_GAP() do { if (LOAD) { if (rd < R) { xa_read1<NC>(n, rd, pX, pW, rot); ++rd; } if (gap < DBL && rd < R) { xa_read1<NC>(n, rd, pX, pW, rot); ++rd; } } ++gap; SB0(); } while (0)
#pragma unroll
    for (int c = 0; c < NC; ++c) {
        const bf16x8 w = cat8(f.w[c][0], f.w[c][1]);
#pragma unroll
        for (int t = 0; t < 2; ++t) {
            if (TR) MMA(A.a[t][c], w, f.x[t]); else MMA(A.a[t][c], f.x[t], w);
            XA_GAP();
        }
        if ((c & 3) == 0) {
            if (TR) MMA(A.cs[c >> 2], w, ones); else MMA(A.cs[c >> 2], ones, w);
            XA_GAP();
        }
    }
    if (XST) {
#pragma unroll
        for (int t = 0; t < 2; ++t) { MMA(A.sx[t], ones, f.x[t]); XA_GAP(); MMA(A.sq[t], f.x[t], f.x[t]); XA_GAP(); }
    }
#pragma unroll
    for (int k = 0; k < 4; ++k) if (LOAD && rd < R) { xa_read1<NC>(n, rd, pX, pW, rot); ++rd; }
    SB0();
#undef XA_GAP
}
template <int NC, bool XST> __device__ __forceinline__ void xw_zero(XWAcc<NC, XST>& A) {
    const f32x4 z = {0.f, 0.f, 0.f, 0.f};
#pragma unroll
    for (int t = 0; t < 2; ++t) {
#pragma unroll
        for (int c = 0; c < NC; ++c) { A.a[t][c] = z; asm volatile("" : "+v"(A.a[t][c])); }
        if (XST) { A.sx[t] = z; A.sq[t] = z; asm volatile("" : "+v"(A.sx[t]), "+v"(A.sq[t])); }
    }
    A.cs[0] = z; A.cs[1] = z; asm volatile("" : "+v"(A.cs[0]), "+v"(A.cs[1]));
    if (NC > 8) { A.cs[2] = z; asm volatile("" : "+v"(A.cs[2])); }
}
template <int NC, bool TR, bool XST, int NST, class Hook>
__device__ __forceinline__ void xw_gemm(XWAcc<NC, XST>& A, LAS char* lds, int g0, int m, int lane, Hook hook) {
    asm volatile("" : "+v"(lane));
    const int fr = lane & 15, g = lane >> 4, row = 32 * m + fr;
    constexpr int G = 2 * NC + (NC + 3) / 4 + (XST ? 4 : 0), R = 2 + 2 * NC;
    constexpr int DA = R > G - 7 ? R - (G - 7) : 0, DB = R > G ? R - G : 0;
    LAS char* pX0 = lds + row * 128 + ((g ^ ((row >> 1) & 7)) << 4);
    LAS char* pX1 = lds + ((row * 128 + ((g ^ ((row >> 1) & 7)) << 4)) ^ 64);
    LAS char* pW0 = lds + 16384 + (16 * (g >> 1) + 4 * (g & 1) + (fr >> 2)) * (NC * 32) + (lane & 3) * 8 + m * 32;
    LAS char* pW1 = pW0 + 32 * NC * 32;
    XRot<NC> rot;
#pragma unroll
    for (int k = 0; k < 3; ++k) rot.o[k] = (NC - 3 + k + m >= NC) ? -NC * 32 : 0;
    XF<NC> f0, f1;
    bf16x8 ones = {0x3F80, 0x3F80, 0x3F80, 0x3F80, 0x3F80, 0x3F80, 0x3F80, 0x3F80};
    asm volatile("" : "+v"(ones));
    {
        const unsigned sb = (g0 & 3) * SLOT;
#pragma unroll
        for (int i = 0; i < R; ++i) xa_read1<NC>(f0, i, pX0 + sb, pW0 + sb, rot);
    }
    asm volatile("s_nop 1"); SB0();
#pragma unroll 1
    for (int s = 0; s < NST - 1; ++s) {
        const unsigned sb = ((g0 + s) & 3) * SLOT, sn = ((g0 + s + 1) & 3) * SLOT;
        LAS char* nX = pX0 + sn; LAS char* nW = pW0 + sn;
        xa_step<NC, TR, XST, DA, true>(A, f0, f1, pX1 + sb, pW1 + sb, rot, ones);
        asm volatile("" : "+v"(nX), "+v"(nW));
        WAIT_L0(); BAR(); SB0();
        hook(s);
        xa_step<NC, TR, XST, DB, true>(A, f1, f0, nX, nW, rot, ones);
    }
    const unsigned sb = ((g0 + NST - 1) & 3) * SLOT;
    xa_step<NC, TR, XST, DA, true>(A, f0, f1, pX1 + sb, pW1 + sb, rot, ones);
    WAIT_L0(); BAR(); SB0();
    xa_step<NC, TR, XST, 0, false>(A, f1, f0, pX0, pW0, rot, ones);
    MMA_DRAIN();
}
template <int NC, bool TR, bool XST> __device__ __forceinline__ void share_colsum(const XWAcc<NC, XST>& A, LAS char* lds, int m, int lane) {
    LAS float* tab = (LAS float*)(lds + SP_CS);
    const int fr = lane & 15, g = lane >> 4;
#pragma unroll
    for (int k = 0; k < (NC + 3) / 4; ++k) {
        int c = m + 4 * k; c = c >= NC ? c - NC : c;
        if (TR) { if (fr == 0) *(LAS f32x4*)(tab + c * 16 + 4 * g) = A.cs[k]; }
        else { if (g == 0) tab[c * 16 + fr] = A.cs[k][0]; }
    }
    WAIT_L0(); BAR();
}
template <int NC> __device__ __forceinline__ void unrotate(unsigned (&v)[NC][2], int m) {
    unsigned u[NC][2];
#define UNROT(M) _Pragma("unroll") for (int c = 0; c < NC; ++c) { u[(c + M) % NC][0] = v[c][0]; u[(c + M) % NC][1] = v[c][1]; }
    if (m == 0) { UNROT(0) } else if (m == 1) { UNROT(1) } else if (m == 2) { UNROT(2) } else { UNROT(3) }
#undef UNROT
#pragma unroll
    for (int c = 0; c < NC; ++c) { v[c][0] = u[c][0]; v[c][1] = u[c][1]; asm volatile("" : "+v"(v[c][0]), "+v"(v[c][1])); }
}

struct MFrag { bf16x8 w[2]; bf16x8 z[9]; };
template <bool LOAD> __device__ __forceinline__ void m_step(f32x4 (&acc)[2][9], const MFrag& f, MFrag& n, LAS char* pW, LAS char* pZ) {
#pragma unroll
    for (int i = 0; i < 18; ++i) {
        MMA(acc[i & 1][i >> 1], f.z[i >> 1], f.w[i & 1]);
        if (LOAD) { if (i < 2) n.w[i] = lds_b128(pW + i * 2048); else if (i < 11) n.z[i - 2] = lds_b128(pZ + (i - 2) * 1024); }
        SB0();
    }
}
__device__ __forceinline__ void o_read(MFrag& f, LAS char* pW, LAS char* pZ) {
#pragma unroll
    for (int t = 0; t < 2; ++t) f.w[t] = lds_b128(pW + t * 2048);
#pragma unroll
    for (int c = 0; c < 9; ++c) f.z[c] = lds_b128(pZ + c * 1024);
}
__device__ __forceinline__ void o_mma(f32x4 (&acc)[2][9], const MFrag& f) {
#pragma unroll
    for (int c = 0; c < 9; ++c)
#pragma unroll
        for (int t = 0; t < 2; ++t) acc[t][c] = MFMA(f.z[c], f.w[t], acc[t][c]);
}
__device__ __forceinline__ void o_sched() {
#pragma unroll
    for (int i = 0; i < 11; ++i) { __builtin_amdgcn_sched_group_barrier(0x008, 1, 0); __builtin_amdgcn_sched_group_barrier(0x100, 1, 0); }
    __builtin_amdgcn_sched_group_barrier(0x008, 7, 0);
    SB0();
}
template <int NST> __device__ __forceinline__ void main_gemm(f32x4 (&acc)[2][9], LAS char* lds, int g0, int m, int lane) {
    asm volatile("" : "+v"(lane));
    const int fr = lane & 15, g = lane >> 4, row = 32 * m + fr;
    LAS char* pW0 = lds + row * 128 + ((g ^ ((row >> 1) & 7)) << 4);
    LAS char* pW1 = lds + ((row * 128 + ((g ^ ((row >> 1) & 7)) << 4)) ^ 64);
    LAS char* pZ0 = lds + 16384 + lane * 16;
    LAS char* pZ1 = pZ0 + 9216;
#pragma unroll
    for (int t = 0; t < 2; ++t)
#pragma unroll
        for (int c = 0; c < 9; ++c) acc[t][c] = (f32x4){0.f, 0.f, 0.f, 0.f};
    MFrag f0, f1;
    o_read(f0, pW0 + (g0 & 3) * SLOT, pZ0 + (g0 & 3) * SLOT);
#pragma unroll 1
    for (int s = 0; s < NST - 1; ++s) {
        const unsigned sb = ((g0 + s) & 3) * SLOT, sn = ((g0 + s + 1) & 3) * SLOT;
        o_read(f1, pW1 + sb, pZ1 + sb);
        o_mma(acc, f0);
        o_sched();
        WAIT_L0(); BAR(); SB0();
        o_read(f0, pW0 + sn, pZ0 + sn);
        o_mma(acc, f1);
        o_sched();
    }
    const unsigned sb = ((g0 + NST - 1) & 3) * SLOT;
    o_read(f1, pW1 + sb, pZ1 + sb);
    o_mma(acc, f0);
    o_sched();
    WAIT_L0(); BAR(); SB0();
    o_mma(acc, f1);
}

struct EFrag { s16x4 w[2]; bf16x8 b[5]; };
template <bool LOAD, bool MM> __device__ __forceinline__ void e_step(f32x4 (&acc)[5], const EFrag& f, EFrag& n, LAS char* pW, LAS char* pB) {
    const bf16x8 w = cat8(f.w[0], f.w[1]);
#pragma unroll
    for (int t = 0; t < 5; ++t) {
        if (MM) MMA(acc[t], w, f.b[t]);
        if (LOAD) {
            if (t == 0) { n.w[0] = lds_tr1(pW); n.w[1] = lds_tr1(pW + 2048); }
            else if (t == 1) { n.b[0] = lds_b128(pB); n.b[1] = lds_b128(pB + 1024); }
            else n.b[t] = lds_b128(pB + t * 1024);
        }
        SB0();
    }
}
template <int NST, int NKS, bool MORE, class Hook> __device__ __forceinline__ void e_gemm(f32x4 (&acc)[5], LAS char* lds, int g0, int w, int lane, Hook hook) {
    asm volatile("" : "+v"(lane));
    const int g = lane >> 4, q = (lane & 15) >> 2;
    LAS char* pW = lds + (4 * g + q) * 128 + ((w ^ ((2 * g + (q >> 1)) & 3)) << 5) + (lane & 3) * 8;
    LAS char* pB = lds + 16384 + lane * 16;
#pragma unroll
    for (int t = 0; t < 5; ++t) { acc[t] = (f32x4){0.f, 0.f, 0.f, 0.f}; asm volatile("" : "+v"(acc[t])); }
    EFrag f0, f1;
    e_step<true, false>(acc, f1, f0, pW + (g0 & 3) * SLOT, pB + (g0 & 3) * SLOT);
    asm volatile("s_nop 1"); SB0();
#pragma unroll 1
    for (int s = 0; s < NST - 1; ++s) {
        const unsigned sb = ((g0 + s) & 3) * SLOT, sn = ((g0 + s + 1) & 3) * SLOT;
        e_step<true, true>(acc, f0, f1, pW + sb + 4096, pB + sb + 5120);
        e_step<true, true>(acc, f1, f0, pW + sb + 8192, pB + sb + 10240);
        e_step<true, true>(acc, f0, f1, pW + sb + 12288, pB + sb + 15360);
        WAIT_L0(); BAR(); SB0();
        hook(s);
        e_step<true, true>(acc, f1, f0, pW + sn, pB + sn);
    }
    const unsigned sb = ((g0 + NST - 1) & 3) * SLOT;
    constexpr int LAST = NKS - 4 * (NST - 1);
    e_step<true, true>(acc, f0, f1, pW + sb + 4096, pB + sb + 5120);
    if (LAST == 4) {
        e_step<true, true>(acc, f1, f0, pW + sb + 8192, pB + sb + 10240);
        e_step<true, true>(acc, f0, f1, pW + sb + 12288, pB + sb + 15360);
    }
    if (MORE) { WAIT_L0(); BAR(); SB0(); }
    e_step<false, true>(acc, f1, f0, pW, pB);
    MMA_DRAIN();
}

__device__ __forceinline__ float dot2(unsigned a, unsigned b, float c) { return __builtin_amdgcn_fdot2_f32_bf16(__builtin_bit_cast(bf16x2_t, a), __builtin_bit_cast(bf16x2_t, b), c, false); }
__device__ __forceinline__ unsigned at_region(int r, int p) { return (unsigned)(((r >> 1) ? 0 : 3 * SLOT) + ((r & 1) ? 16384 + p * 5120 : p * 4096)); }
__device__ __forceinline__ unsigned at_row(int rho, int p) { const int r = rho / 14; return at_region(r, p) + (unsigned)(rho - r * 14) * 288u; }
__device__ __forceinline__ void attn_issue(rsrc_t rQ, LAS char* lds, int n, int p, int lane, int half) {
#pragma unroll
    for (int rr = 0; rr < 2; ++rr)
#pragma unroll
        for (int q = 0; q < 4; ++q) {
            const int r = 2 * half + rr, c = q * 64 + lane, lr = c / 18, cc = c - lr * 18, rho = r * 14 + lr;
            int t, seg;
            if (rho < 22) { t = 8 * n - 14 + rho; seg = 1; } else if (rho < 44) { t = 8 * n - 14 + (rho - 22); seg = 2; } else { t = 8 * n + (rho - 44); seg = 0; }
            const unsigned voff = (t < 0 || rho >= 52 || lr >= 14) ? 0x7ffffff0u : (unsigned)(t * (QW * 2) + (seg * D_ + 144 * p) * 2 + cc * 16);
            dma16_dev(rQ, lds + at_region(r, p) + q * 1024, voff, 0);
        }
}
__device__ __forceinline__ void attn_compute(LAS char* lds, rsrc_t rO, int n, int p, int lane) {
    const int i = lane >> 3, hh = lane & 7, t = 8 * n + i;
    const unsigned hb = hh * 36, r0 = at_region(0, p) + hb, r1 = at_region(1, p) + hb - 14 * 288, r2 = at_region(2, p) + hb - 28 * 288, r3 = at_region(3, p) + hb - 42 * 288;
    unsigned qv[9];
    {
        LAS unsigned* q = (LAS unsigned*)(lds + r3 + (44 + i) * 288);
#pragma unroll
        for (int d = 0; d < 9; ++d) qv[d] = q[d];
    }
    float lg[8];
#pragma unroll
    for (int w = 0; w < 8; ++w) {
        const int rho = 14 + i - 2 * w;
        LAS unsigned* kp = (LAS unsigned*)(lds + (rho < 14 ? r0 : r1) + rho * 288);
        float s = 0.f;
#pragma unroll
        for (int d = 0; d < 9; ++d) s = dot2(qv[d], kp[d], s);
        lg[w] = (t - 2 * w >= 0) ? s * (0.23570226039551584f * 1.4426950408889634f) : -1e9f;
    }
    float mx = fmaxf(fmaxf(fmaxf(lg[0], lg[1]), fmaxf(lg[2], lg[3])), fmaxf(fmaxf(lg[4], lg[5]), fmaxf(lg[6], lg[7])));
    float sum = 0.f;
#pragma unroll
    for (int w = 0; w < 8; ++w) { lg[w] = __builtin_amdgcn_exp2f(lg[w] - mx); sum += lg[w]; }
    const float inv = __builtin_amdgcn_rcpf(sum);
    unsigned pp[4];
#pragma unroll
    for (int w = 0; w < 4; ++w) pp[w] = pack_bf16(lg[2 * w] * inv, lg[2 * w + 1] * inv);
    float acc[18];
#pragma unroll
    for (int d = 0; d < 18; ++d) acc[d] = 0.f;
#pragma unroll
    for (int w = 0; w < 4; ++w) {
        const int ra = 22 + 14 + i - 4 * w, rb = ra - 2;
        LAS unsigned* va = (LAS unsigned*)(lds + (ra < 28 ? r1 : ra < 42 ? r2 : r3) + ra * 288);
        LAS unsigned* vb = (LAS unsigned*)(lds + (rb < 28 ? r1 : rb < 42 ? r2 : r3) + rb * 288);
#pragma unroll
        for (int d = 0; d < 9; ++d) {
            const unsigned x = va[d], y = vb[d];
            acc[2 * d] = dot2(__builtin_amdgcn_perm(y, x, 0x05040100u), pp[w], acc[2 * d]);
            acc[2 * d + 1] = dot2(__builtin_amdgcn_perm(y, x, 0x07060302u), pp[w], acc[2 * d + 1]);
        }
    }
    LAS char* ob = lds + SP_AO + p * 2304;
#pragma unroll
    for (int d = 0; d < 9; ++d) *(LAS unsigned*)(ob + i * 288 + hh * 36 + d * 4) = pack_bf16(acc[2 * d], acc[2 * d + 1]);
    WAIT_L0();
    {
        const int r = lane & 7, g = (lane >> 3) & 3, tt = n >> 1, rl = (8 * n + r) & 15;
#pragma unroll
        for (int q = 0; q < 2; ++q) {
            const int a = (lane >> 5) + 2 * q;
            const u32x2 lo = *(LAS u32x2*)(ob + r * 288 + (32 * a + 4 * g) * 2), hi = *(LAS u32x2*)(ob + r * 288 + (32 * a + 16 + 4 * g) * 2);
            st128_dev(rO, ((tt * 18 + 4 * p + a) * 64 + rl + 16 * g) * 16, (u32x4){lo.x, lo.y, hi.x, hi.y});
        }
        const u32x2 v = *(LAS u32x2*)(ob + r * 288 + (128 + 4 * g) * 2);
        if (lane < 32) st64_dev(rO, ((tt * 18 + 16 + (p >> 1)) * 64 + rl + 16 * g) * 16 + (p & 1) * 8, v);
    }
}

__global__ __launch_bounds__(512, 2) void fused_kernel(Args a) {
    extern __shared__ __attribute__((aligned(16))) char smem[];
    LAS char* lds = (LAS char*)smem;
    const int wid = __builtin_amdgcn_readfirstlane(threadIdx.x >> 6), lane = threadIdx.x & 63;
    const int xcd = blockIdx.x & 7, loc = blockIdx.x >> 3;
    const int ti = (xcd & 1) * 8 + (loc & 7), tj = (xcd >> 1) * 4 + (loc >> 3);
    const int nt = xcd * 30 + loc;
    const bool fin = loc < 30 && nt < 234;
    const int rb = nt / 9, cb = nt - rb * 9;
    char* ctl = a.ws + WS_CTL;
    u64 tag;
    {
        const u64 e = __hip_atomic_load((const GAS u64*)(ctl + CTL_EP), __ATOMIC_RELAXED, __HIP_MEMORY_SCOPE_AGENT);
        const u64 tg = e * 0x9E3779B97F4A7C15ull + 0xD1B54A32D192ED03ull;
        tag = ((u64)(unsigned)__builtin_amdgcn_readfirstlane((unsigned)(tg >> 32)) << 32) | (unsigned)__builtin_amdgcn_readfirstlane((unsigned)tg);
    }
    const int an = ti * 16 + tj;

    if (wid >= 4) {
        const int p = wid - 4;
        ST_DECL; ST(0);
#define rX mk_rsrc(a.x, S_ * D_ * 2)
#define rQW mk_rsrc(a.wqkv, D_ * QW * 2)
#define rW mk_rsrc(a.win, D_ * F_ * 2)
#define rS mk_rsrc(a.wsp, S_ * S_ * 2)
#define rZ mk_rsrc(a.ws + WS_Z2T, S_ * FH * 2)
#define rQ mk_rsrc(a.ws + WS_QKV, S_ * QW * 2)
#define rO mk_rsrc(a.ws + WS_OF, S_ * D_ * 2)
        RowsImg xi; xi.init(lane, D_ * 2);
        RowsImg si; si.init(lane, S_ * 2);
        WImg<7> wq; wq.init(lane, p, QW * 2);
        WImg<9> wz; wz.init(lane, p, F_ * 2);
        const unsigned sX0 = (unsigned)(ti * 128) * (D_ * 2), sQ = (unsigned)((tj * 27) >> 2) * 32, sZ1 = (unsigned)(tj * 144) * 2, sZ2 = sZ1 + FH * 2;
        const unsigned sS = (unsigned)(ti * 128 + 32 * p) * (S_ * 2), sZ = (unsigned)tj * (64u * 9216u);
        const unsigned vl = lane * 16;
        const unsigned vE = (lane >> 3) * (D_ * 2) + ((((lane & 7) >> 1) ^ ((lane >> 4) & 3)) << 5) + (lane & 1) * 16;
        const int zI0 = WImg<9>::first(p);
        auto iss_q = [&](int g) {
            LAS char* slot = lds + (g & 3) * SLOT;
            xi.issue35(rX, slot, sX0 + g * 128, D_ * 2, p); wq.issue(rQW, slot + 16384, sQ + g * 64 * (QW * 2), p);
        };
        auto iss_z = [&](int g, int s, unsigned col) {
            LAS char* slot = lds + (g & 3) * SLOT;
            xi.issue35(rX, slot, sX0 + s * 128, D_ * 2, p);
            wz.issue(rW, slot + 16384, col + s * 64 * (F_ * 2), p);
        };
        auto iss_m = [&](int g) {
            LAS char* slot = lds + (g & 3) * SLOT; const int s = g - G_MAIN;
            si.issue(rS, slot + p * 4096, sS + s * 128, S_ * 2);
#pragma unroll
            for (int q = 0; q < 4; ++q) dma16_dev(rZ, slot + 16384 + (zI0 + q) * 1024, vl, sZ + s * 18432 + (zI0 + q) * 1024);
            if (p < 2) dma16_dev(rZ, slot + 16384 + (zI0 + 4) * 1024, vl, sZ + s * 18432 + (zI0 + 4) * 1024);
        };
        unsigned eT[5];
#pragma unroll
        for (int t = 0; t < 5; ++t) eT[t] = (unsigned)__builtin_amdgcn_readfirstlane(min(rb * 5 + t, 127));
        auto iss_e = [&](int g, bool isS) {
            LAS char* slot = lds + (g & 3) * SLOT;
            const int lim = isS ? 16 : 64, nks = isS ? 18 : 72;
            int ks = 4 * (g - (isS ? G_S : G_G)) + p; if (isS && ks > 17) ks = 17;
            const int c0 = ks < lim ? 9 * (ks >> 2) + 2 * (ks & 3) : 18 * (ks - lim) + 8, c1 = ks < lim ? c0 + 1 : c0 + 9;
            const rsrc_t rw = mk_rsrc(isS ? a.wo : a.wout, isS ? D_ * D_ * 2 : FH * D_ * 2), rb_ = mk_rsrc(a.ws + (isS ? WS_OF : WS_HF), isS ? S_ * D_ * 2 : S_ * FH * 2);
#pragma unroll
            for (int q = 0; q < 4; ++q) dma16(rw, slot + p * 4096 + q * 1024, vE, (unsigned)((q < 2 ? c0 : c1) * 16 + 8 * (q & 1)) * (D_ * 2) + cb * 128);
#pragma unroll
            for (int t = 0; t < 5; ++t) dma16_dev(rb_, slot + 16384 + p * 5120 + t * 1024, vl, (eT[t] * nks + ks) * 1024);
        };
#define PSTEP(K, PH) do { WAIT_V(7 * (K)); BAR(); } while (0)
#define SPIN_UNTIL(cond) do { unsigned spins_ = 0; while (!(cond) && ++spins_ < SPIN_CAP) __builtin_amdgcn_s_sleep(4); asm volatile("" ::: "memory"); } while (0)
        iss_q(0); iss_q(1); iss_q(2); iss_q(3);
        {
            const unsigned W = blockIdx.x * 4 + p;
            touch_lines(rX, lds + SP_PF + wid * 256, W * 18, 18, lane);
            touch_lines(rQW, lds + SP_PF + wid * 256, W * 16, 16, lane);
            touch_lines(rW, lds + SP_PF + wid * 256, W * 41, 41, lane);
        }
        PSTEP(3, 0);
#pragma unroll 1
        for (int g = 1; g <= 5; ++g) { PSTEP(2, 0); iss_q(g + 3); }
#pragma unroll 1
        for (int g = 6; g <= 14; ++g) { PSTEP(2, 1); if (g == G_Z2) BAR(); iss_z(g + 3, g + 3 - G_Z2, sZ2); }
        PSTEP(2, 1); iss_z(18, 0, sZ1);
        PSTEP(2, 1); iss_z(19, 1, sZ1);
        PSTEP(2, 1); iss_z(20, 2, sZ1);
        PSTEP(2, 2);
        BAR();
        iss_z(21, 3, sZ1);
        {
            const unsigned W = blockIdx.x * 4 + p;
            touch_lines(rS, lds + SP_PF + wid * 256, W * 64, 64, lane);
            touch_lines(mk_rsrc(a.wout, FH * D_ * 2), lds + SP_PF + wid * 256, W * 21, 21, lane);
            touch_lines(mk_rsrc(a.wo, D_ * D_ * 2), lds + SP_PF + wid * 256, W * 6, 6, lane);
        }
#pragma unroll 1
        for (int g = 19; g <= 23; ++g) { PSTEP(2, 2); iss_z(g + 3, g + 3 - G_Z1, sZ1); }
        PSTEP(2, 2); PSTEP(1, 2); PSTEP(0, 2);
        SPIN_UNTIL(flags_ready(ctl + CTL_ZF + tj * 512, 64, ctl, 0, tag, lane));
        iss_m(27);
        PSTEP(0, 3);
        BAR();
        iss_m(28); iss_m(29); iss_m(30);
        {
            const int pa = (8 * an - 14) >> 7;
            SPIN_UNTIL(flags_ready(ctl + CTL_QF + ti * 512, 64, ctl + CTL_QF + (pa < 0 ? ti : pa) * 512, 64, tag, lane));
        }
#pragma unroll 1
        for (int g = 28; g <= 55; ++g) { PSTEP(2, 3); iss_m(g + 3); }
        PSTEP(2, 3); attn_issue(rQ, lds, an, p, lane, 0);
        PSTEP(1, 3); attn_issue(rQ, lds, an, p, lane, 1);
        PSTEP(1, 3);
        BAR();
        ST(1);
        WAIT_V(0);
        attn_compute(lds, rO, an, p, lane);
        ST(2);
        if (!fin) publish(ctl, CTL_OF + (an * 4 + p) * 8, tag, lane);
        else {
            {
                const int p0 = (rb * 80) >> 7, p1 = min(rb * 80 + 79, S_ - 1) >> 7;
                WAIT_V(0);
                if (lane == 0) __hip_atomic_store((GAS u64*)(ctl + CTL_OF + (an * 4 + p) * 8), tag, __ATOMIC_RELAXED, __HIP_MEMORY_SCOPE_AGENT);
                SPIN_UNTIL(flags_ready(ctl + CTL_HF + p0 * 512, 64, ctl + CTL_HF + p1 * 512, 64, tag, lane));
            }
            ST(3);
            iss_e(59, false); iss_e(60, false);
            PSTEP(1, 5); iss_e(61, false); iss_e(62, false);
#pragma unroll 1
            for (int g = 60; g <= 73; ++g) {
                PSTEP(2, 5); iss_e(g + 3, false);
                if (g == 66) SPIN_UNTIL(flags_ready(ctl + CTL_OF + rb * 320, rb == 25 ? 24 : 40, ctl, 0, tag, lane));
            }
#pragma unroll 1
            for (int g = 74; g <= 78; ++g) { PSTEP(2, 4); iss_e(g + 3, true); }
            PSTEP(2, 4); PSTEP(1, 4); PSTEP(0, 4);
        }
        ST_END(1);
        asm volatile("fusedk_pf_end:");
        return;
    }

    const int m = wid, fr = lane & 15, g4 = lane >> 4;
    ST_DECL; ST(0);
    const rsrc_t rQs = mk_rsrc(a.ws + WS_QKV, S_ * QW * 2), rZs = mk_rsrc(a.ws + WS_Z2T, S_ * FH * 2), rHs = mk_rsrc(a.ws + WS_HF, S_ * FH * 2), rSt = mk_rsrc(a.ws + WS_ST, S_ * 16 * 16);
    LAS float* cst = (LAS float*)(lds + SP_CS);
    float xm[2], xr[2];
    {
        unsigned dummy = 0;
        if (wid == 0) {
            u64 base; unsigned sz;
            asm volatile("s_getpc_b64 %0\nfusedk_pf_begin:\n\ts_mov_b32 %1, fusedk_pf_end-fusedk_pf_begin" : "=s"(base), "=s"(sz));
            const unsigned off = (unsigned)(loc * 13 + lane) * 128u;
            if (lane < 13 && (int)off < (int)sz) asm volatile("global_load_dword %0, %1, off" : "=v"(dummy) : "v"(base + off));
        }
        asm volatile("s_waitcnt vmcnt(0)" : "+v"(dummy));
    }
    BAR();
    ST(1);
    {
        XWAcc<7, true> A; xw_zero(A);
        xw_gemm<7, true, true, 9>(A, lds, G_QKV, m, lane, NoHook());
        ST(2);
        share_colsum<7, true, true>(A, lds, m, lane);
#pragma unroll
        for (int t = 0; t < 2; ++t) {
            const int r3 = fr & 3;
            const float dq = r3 == 0 ? A.sq[t][0] : r3 == 1 ? A.sq[t][1] : r3 == 2 ? A.sq[t][2] : A.sq[t][3];
            const float sq = __shfl(dq, (fr >> 2) * 16 + fr);
            xm[t] = A.sx[t][0] * (1.0f / D_);
            xr[t] = __builtin_amdgcn_rsqf(fmaxf(sq * (1.0f / D_) - xm[t] * xm[t], 0.f) + 1e-5f);
        }
        const int c0 = (tj * 27) >> 2, dup = tj > 0 && c0 < (((tj - 1) * 27) >> 2) + 7;
#pragma unroll
        for (int t = 0; t < 2; ++t) {
            unsigned pk[7][2];
#pragma unroll
            for (int c = 0; c < 7; ++c) {
                const int tile = c + m >= 7 ? c + m - 7 : c + m;
                const f32x4 cs = *(LAS f32x4*)(cst + tile * 16 + 4 * g4);
                pk[c][0] = pack2((lo2(A.a[t][c]) - lo2(cs) * xm[t]) * xr[t]); pk[c][1] = pack2((hi2(A.a[t][c]) - hi2(cs) * xm[t]) * xr[t]);
            }
            unrotate<7>(pk, m);
            const unsigned ro = (unsigned)(ti * 128 + 32 * m + 16 * t + fr) * (QW * 2) + c0 * 32;
#pragma unroll
            for (int pp = 0; pp < 3; ++pp) st128_dev(rQs, ro + (1 + 2 * pp) * 32 + g4 * 16, gather8(pk[1 + 2 * pp][0], pk[1 + 2 * pp][1], pk[2 + 2 * pp][0], pk[2 + 2 * pp][1]));
            if (!dup) st64_dev(rQs, ro + g4 * 8, (u32x2){pk[0][0], pk[0][1]});
        }
    }
    ST(3);
    unsigned zp[2][9][2];
    {
        XWAcc<9, false> A; xw_zero(A);
        xw_gemm<9, false, false, 9>(A, lds, G_Z2, m, lane, [&](int s) { if (s == 4) publish(ctl, CTL_QF + ((ti * 16 + tj) * 4 + m) * 8, tag, lane); });
        ST(4);
        share_colsum<9, false, false>(A, lds, m, lane);
        f32x2 rm[2][2], rr[2][2];
#pragma unroll
        for (int t = 0; t < 2; ++t)
#pragma unroll
            for (int h = 0; h < 2; ++h) {
                rm[t][h] = (f32x2){__shfl(xm[t], 4 * g4 + 2 * h), __shfl(xm[t], 4 * g4 + 2 * h + 1)};
                rr[t][h] = (f32x2){__shfl(xr[t], 4 * g4 + 2 * h), __shfl(xr[t], 4 * g4 + 2 * h + 1)};
            }
        f32x2 s1[2][2], s2[2][2];
#pragma unroll
        for (int t = 0; t < 2; ++t)
#pragma unroll
            for (int h = 0; h < 2; ++h) { s1[t][h] = (f32x2){0.f, 0.f}; s2[t][h] = (f32x2){0.f, 0.f}; }
#pragma unroll
        for (int c = 0; c < 9; ++c) {
            const float cs = cst[(c + m >= 9 ? c + m - 9 : c + m) * 16 + fr];
#pragma unroll
            for (int t = 0; t < 2; ++t)
#pragma unroll
                for (int h = 0; h < 2; ++h) {
                    const f32x2 v = gelu2(((h ? hi2(A.a[t][c]) : lo2(A.a[t][c])) - rm[t][h] * cs) * rr[t][h]);
                    s1[t][h] += v; s2[t][h] += v * v;
                    zp[t][c][h] = pack2(v);
                    asm volatile("" : "+v"(zp[t][c][h]));
                }
        }
        float mm = 0.f, qq = 0.f;
        {
            const int idx = lane & 7;
            float a1[8], a2[8];
#pragma unroll
            for (int k = 0; k < 8; ++k) { a1[k] = s1[k >> 2][(k >> 1) & 1][k & 1]; a2[k] = s2[k >> 2][(k >> 1) & 1][k & 1]; }
#pragma unroll
            for (int k = 0; k < 8; ++k) { a1[k] = dpp_add<0x128>(a1[k]); a2[k] = dpp_add<0x128>(a2[k]); }
#pragma unroll
            for (int k = 0; k < 8; ++k) { a1[k] = dpp_add<0x124>(a1[k]); a2[k] = dpp_add<0x124>(a2[k]); }
#pragma unroll
            for (int k = 0; k < 8; ++k) { a1[k] = dpp_add<0x122>(a1[k]); a2[k] = dpp_add<0x122>(a2[k]); }
#pragma unroll
            for (int k = 0; k < 8; ++k) { a1[k] = dpp_add<0x121>(a1[k]); a2[k] = dpp_add<0x121>(a2[k]); }
            float u1 = a1[0], u2 = a2[0];
#pragma unroll
            for (int k = 1; k < 8; ++k) { u1 = idx == k ? a1[k] : u1; u2 = idx == k ? a2[k] : u2; }
            mm = u1 * (1.0f / 144.0f);
            qq = fmaxf(u2 - u1 * mm, 0.f);
            if (fr < 8) st128_dev(rSt, ((ti * 16 + tj) * 128 + 32 * m + 16 * (idx >> 2) + 4 * g4 + (idx & 3)) * 16,
                                  (u32x4){__float_as_uint(mm), (unsigned)tag, __float_as_uint(qq), (unsigned)(tag >> 32)});
        }
    }
    ST(5);
    unsigned z1p[2][9][2];
    {
        XWAcc<9, false> A; xw_zero(A);
        xw_gemm<9, true, false, 2>(A, lds, G_Z1, m, lane, NoHook());
        ST(6);
        float mu, rs;
        {
            const unsigned off = ((ti * 16 + (lane >> 5) * 8) * 128 + 32 * m + (lane & 31)) * 16;
            u32x4 v[8]; unsigned spins = 0;
            for (;;) {
#pragma unroll
                for (int k = 0; k < 8; ++k) v[k] = __builtin_amdgcn_raw_buffer_load_b128(rSt, off + k * 2048, 0, 16);
                unsigned bad = 0;
#pragma unroll
                for (int k = 0; k < 8; ++k) bad |= (v[k].y ^ (unsigned)tag) | (v[k].w ^ (unsigned)(tag >> 32));
                if (__all(bad == 0) || ++spins > SPIN_CAP) break;
                __builtin_amdgcn_s_sleep(2);
            }
            float s = 0.f;
#pragma unroll
            for (int k = 0; k < 8; ++k) s += __uint_as_float(v[k].x);
            s += xchg32(s);
            mu = s * (1.0f / 16.0f);
            float q = 0.f;
#pragma unroll
            for (int k = 0; k < 8; ++k) { const float d = __uint_as_float(v[k].x) - mu; q += __uint_as_float(v[k].z) + 144.0f * d * d; }
            q += xchg32(q);
            rs = __builtin_amdgcn_rsqf(q * (1.0f / FH) + 1e-5f);
        }
        ST(7);
        f32x2 mr[2][2], rr[2][2];
#pragma unroll
        for (int t = 0; t < 2; ++t)
#pragma unroll
            for (int h = 0; h < 2; ++h) {
                const int src = 16 * t + 4 * g4 + 2 * h;
                mr[t][h] = (f32x2){__shfl(mu, src), __shfl(mu, src + 1)}; rr[t][h] = (f32x2){__shfl(rs, src), __shfl(rs, src + 1)};
            }
        const unsigned zo = (unsigned)((tj * 64 + ti * 4 + m) * 9) * 1024 + lane * 16;
#pragma unroll
        for (int c = 0; c < 9; ++c) {
            const unsigned x0 = pack2((unpack2(zp[0][c][0]) - mr[0][0]) * rr[0][0]), x1 = pack2((unpack2(zp[0][c][1]) - mr[0][1]) * rr[0][1]);
            const unsigned y0 = pack2((unpack2(zp[1][c][0]) - mr[1][0]) * rr[1][0]), y1 = pack2((unpack2(zp[1][c][1]) - mr[1][1]) * rr[1][1]);
            st128_dev(rZs, zo + (c + m >= 9 ? c + m - 9 : c + m) * 1024, gather8(x0, x1, y0, y1));
        }
        ST(8);
        xw_gemm<9, true, false, 7>(A, lds, G_Z1 + 2, m, lane, [&](int s) { if (s == 1) publish(ctl, CTL_ZF + ((tj * 16 + ti) * 4 + m) * 8, tag, lane); });
        ST(9);
        share_colsum<9, true, false>(A, lds, m, lane);
#pragma unroll
        for (int c = 0; c < 9; ++c) {
            const f32x4 cs = *(LAS f32x4*)(cst + (c + m >= 9 ? c + m - 9 : c + m) * 16 + 4 * g4);
#pragma unroll
            for (int t = 0; t < 2; ++t) {
                z1p[t][c][0] = pack2(gelu2((lo2(A.a[t][c]) - lo2(cs) * xm[t]) * xr[t])); z1p[t][c][1] = pack2(gelu2((hi2(A.a[t][c]) - hi2(cs) * xm[t]) * xr[t]));
                asm volatile("" : "+v"(z1p[t][c][0]), "+v"(z1p[t][c][1]));
            }
        }
        unrotate<9>(z1p[0], m); unrotate<9>(z1p[1], m);
    }
    ST(10);
    {
        f32x4 acc[2][9];
        float bt[2];
#pragma unroll
        for (int t = 0; t < 2; ++t) bt[t] = bf2f(a.bsp[ti * 128 + 32 * m + 16 * t + fr]);
        main_gemm<32>(acc, lds, G_MAIN, m, lane);
        ST(11);
#pragma unroll
        for (int t = 0; t < 2; ++t) {
            const unsigned hp = (unsigned)(ti * 8 + 2 * m + t) * 72 * 1024 + lane * 16;
#pragma unroll
            for (int pp = 0; pp < 4; ++pp) {
                u32x4 o;
                o.x = pack2((lo2(acc[t][2 * pp]) + bt[t]) * unpack2(z1p[t][2 * pp][0])); o.y = pack2((hi2(acc[t][2 * pp]) + bt[t]) * unpack2(z1p[t][2 * pp][1]));
                o.z = pack2((lo2(acc[t][2 * pp + 1]) + bt[t]) * unpack2(z1p[t][2 * pp + 1][0])); o.w = pack2((hi2(acc[t][2 * pp + 1]) + bt[t]) * unpack2(z1p[t][2 * pp + 1][1]));
                st128_dev(rHs, hp + (tj * 4 + pp) * 1024, o);
            }
            u32x2 o;
            o.x = pack2((lo2(acc[t][8]) + bt[t]) * unpack2(z1p[t][8][0])); o.y = pack2((hi2(acc[t][8]) + bt[t]) * unpack2(z1p[t][8][1]));
            st64_dev(rHs, hp + (64 + (tj >> 1)) * 1024 + (tj & 1) * 8, o);
        }
    }
    ST(12);
    publish(ctl, CTL_HF + ((ti * 16 + tj) * 4 + m) * 8, tag, lane);
    ST(13);
    if (!fin) { ST_END(0); return; }
    {
        f32x4 sa[5], ga[5];
        u32x2 xv[5];
        const int fr = lane & 15, g4 = lane >> 4;
#pragma unroll
        for (int t = 0; t < 5; ++t) xv[t] = *(const u32x2*)(a.x + (size_t)(min(rb * 5 + t, 127) * 16 + fr) * D_ + cb * 64 + m * 16 + g4 * 4);
        BAR();
        ST(14);
        e_gemm<18, 72, true>(ga, lds, G_G, m, lane, NoHook());
        ST(15);
        e_gemm<5, 18, false>(sa, lds, G_S, m, lane, NoHook());
        ST(16);
#pragma unroll
        for (int t = 0; t < 5; ++t) {
            if (rb * 5 + t < 128) {
                float o[4];
#pragma unroll
                for (int r = 0; r < 4; ++r) {
                    const float gv = ga[t][r], sg = gv * __builtin_amdgcn_rcpf(1.0f + __builtin_amdgcn_exp2f(gv * -1.4426950408889634f));
                    const float xx = (r & 1) ? bfhi(xv[t][r >> 1]) : bflo(xv[t][r >> 1]);
                    o[r] = xx + sa[t][r] * sg;
                }
                u32x2 ov; ov.x = pack_bf16(o[0], o[1]); ov.y = pack_bf16(o[2], o[3]);
                *(u32x2*)(a.out + (size_t)((rb * 5 + t) * 16 + fr) * D_ + cb * 64 + m * 16 + g4 * 4) = ov;
            }
        }
    }
    if (blockIdx.x == 0 && threadIdx.x == 0) __hip_atomic_store((GAS u64*)(ctl + CTL_EP), tag, __ATOMIC_RELAXED, __HIP_MEMORY_SCOPE_AGENT);
    ST(17);
    ST_END(0);
}

extern "C" void kernel_launch(void* const* d_in, const int* in_sizes, int n_in, void* d_out, int out_size, void* d_ws, size_t ws_size, hipStream_t stream) {
    Args a;
    a.x = (const u16*)d_in[0]; a.wqkv = (const u16*)d_in[1]; a.wo = (const u16*)d_in[2]; a.win = (const u16*)d_in[4]; a.wsp = (const u16*)d_in[5];
    a.bsp = (const u16*)d_in[6]; a.wout = (const u16*)d_in[7]; a.out = (u16*)d_out; a.ws = (char*)d_ws;
    (void)hipFuncSetAttribute((const void*)fused_kernel, hipFuncAttributeMaxDynamicSharedMemorySize, LDS_BYTES);
    fused_kernel<<<256, 512, LDS_BYTES, stream>>>(a);
}
```

```cpp
#include <hip/hip_runtime.h>
#include <hip/hip_bf16.h>
#include <stdint.h>

typedef unsigned short u16;
typedef unsigned long long u64;
typedef short bf16x8 __attribute__((ext_vector_type(8)));
typedef short s16x4 __attribute__((ext_vector_type(4)));
typedef float f32x4 __attribute__((ext_vector_type(4)));
typedef float f32x2 __attribute__((ext_vector_type(2)));
typedef unsigned u32x4 __attribute__((ext_vector_type(4)));
typedef unsigned u32x2 __attribute__((ext_vector_type(2)));
typedef __bf16 bf16x2_t __attribute__((ext_vector_type(2)));
#define LAS __attribute__((address_space(3)))
#define GAS __attribute__((address_space(1)))
typedef LAS void* lds_vp;
typedef __amdgpu_buffer_rsrc_t rsrc_t;

constexpr int S_ = 2048, D_ = 576, F_ = 4608, FH = 2304, QW = 1728;
constexpr int SLOT = 36864, SPARE = 4 * SLOT, LDS_BYTES = 163840;
constexpr int SP_CS = SPARE, SP_PF = SPARE + 1024, SP_EP = SPARE + 3072, SP_AO = SPARE + 4096;
constexpr size_t WS_Z2T = 0;
constexpr size_t WS_QKV = WS_Z2T + (size_t)S_ * FH * 2;
constexpr size_t WS_HF = WS_QKV + (size_t)S_ * QW * 2;
constexpr size_t WS_OF = WS_HF + (size_t)S_ * FH * 2;
constexpr size_t WS_ST = WS_OF + (size_t)S_ * D_ * 2;
constexpr size_t WS_CTL = WS_ST + (size_t)S_ * 16 * 16;
constexpr size_t CTL_EP = 0, CTL_QF = 1024, CTL_ZF = CTL_QF + 8192, CTL_OF = CTL_ZF + 8192, CTL_HF = CTL_OF + 8192, CTL_END = CTL_HF + 8192;
constexpr int G_QKV = 0, G_Z2 = 9, G_Z1 = 18, G_MAIN = 27, G_G = 59, G_S = 77;
constexpr unsigned SPIN_CAP = 400000u;

__device__ __forceinline__ float bflo(unsigned v) { return __uint_as_float(v << 16); }
__device__ __forceinline__ float bfhi(unsigned v) { return __uint_as_float(v & 0xffff0000u); }
__device__ __forceinline__ float bf2f(u16 v) { return __uint_as_float(((unsigned)v) << 16); }
__device__ __forceinline__ unsigned pack_bf16(float lo, float hi) {
    f32x2 v = {lo, hi};
    return __builtin_bit_cast(unsigned, __builtin_convertvector(v, bf16x2_t));
}
#define BAR() do { asm volatile("" ::: "memory"); __builtin_amdgcn_s_barrier(); asm volatile("" ::: "memory"); } while (0)
#define WAIT_V(n) asm volatile("s_waitcnt vmcnt(%0)" ::"n"(n) : "memory")
#define WAIT_L0() asm volatile("s_waitcnt lgkmcnt(0)" ::: "memory")
#define SB0() __builtin_amdgcn_sched_barrier(0)
#define ST_DECL
#define ST(k)
#define ST_END(who)

__device__ __forceinline__ rsrc_t mk_rsrc(const void* p, unsigned bytes) { return __builtin_amdgcn_make_buffer_rsrc((void*)p, 0, bytes, 0x00020000); }
__device__ __forceinline__ void dma16(rsrc_t r, LAS char* lds, unsigned voff, unsigned soff) {
    __builtin_amdgcn_raw_ptr_buffer_load_lds(r, (lds_vp)lds, 16, voff, soff, 0, 0);
}
__device__ __forceinline__ void dma16_dev(rsrc_t r, LAS char* lds, unsigned voff, unsigned soff) {
    __builtin_amdgcn_raw_ptr_buffer_load_lds(r, (lds_vp)lds, 16, voff, soff, 0, 16);
}
#define ATT_PRIO 1
#define STAGE_ST 0
#define ST_AUX 16
__device__ __forceinline__ void touch_lines(rsrc_t r, LAS char* scratch, unsigned first, unsigned cnt, int lane) {
    __builtin_amdgcn_raw_ptr_buffer_load_lds(r, (lds_vp)scratch, 4, (unsigned)lane < cnt ? (first + lane) * 128u : 0x7ffffff0u, 0, 0, 0);
}
__device__ __forceinline__ void st128_dev(rsrc_t r, unsigned off, u32x4 v) { __builtin_amdgcn_raw_buffer_store_b128(v, r, off, 0, ST_AUX); }
__device__ __forceinline__ void st64_dev(rsrc_t r, unsigned off, u32x2 v) { __builtin_amdgcn_raw_buffer_store_b64(v, r, off, 0, ST_AUX); }
__device__ __forceinline__ bf16x8 lds_b128(LAS char* p) { return *(LAS bf16x8*)p; }
__device__ __forceinline__ bf16x8 lds_tr(LAS char* p, int off1) {
    s16x4 a = __builtin_amdgcn_ds_read_tr16_b64_v4i16((LAS s16x4*)p);
    s16x4 b = __builtin_amdgcn_ds_read_tr16_b64_v4i16((LAS s16x4*)(p + off1));
    return __builtin_shufflevector(a, b, 0, 1, 2, 3, 4, 5, 6, 7);
}
#define MFMA(a, b, c) __builtin_amdgcn_mfma_f32_16x16x32_bf16(a, b, c, 0, 0, 0)
template <int CTRL> __device__ __forceinline__ float dpp_add(float x) {
    return x + __builtin_bit_cast(float, __builtin_amdgcn_update_dpp(0, __builtin_bit_cast(int, x), CTRL, 0xf, 0xf, false));
}
__device__ __forceinline__ float row16_sum(float x) { x = dpp_add<0x128>(x); x = dpp_add<0x124>(x); x = dpp_add<0x122>(x); x = dpp_add<0x121>(x); return x; }
__device__ __forceinline__ float gelu_f(float x) {
    const float t = x * (-2.3022081498f - 0.1029432397f * x * x);
    return x * __builtin_amdgcn_rcpf(1.0f + __builtin_amdgcn_exp2f(t));
}
__device__ __forceinline__ f32x2 gelu2(f32x2 x) {
    const f32x2 t = x * (x * x * -0.1029432397f + -2.3022081498f);
    f32x2 e; e.x = __builtin_amdgcn_exp2f(t.x); e.y = __builtin_amdgcn_exp2f(t.y);
    const f32x2 d = e + 1.0f;
    f32x2 r; r.x = __builtin_amdgcn_rcpf(d.x); r.y = __builtin_amdgcn_rcpf(d.y);
    return x * r;
}
__device__ __forceinline__ f32x2 lo2(f32x4 v) { return (f32x2){v[0], v[1]}; }
__device__ __forceinline__ f32x2 hi2(f32x4 v) { return (f32x2){v[2], v[3]}; }
__device__ __forceinline__ unsigned pack2(f32x2 v) { return __builtin_bit_cast(unsigned, __builtin_convertvector(v, bf16x2_t)); }
__device__ __forceinline__ f32x2 unpack2(unsigned v) { return (f32x2){__uint_as_float(v << 16), __uint_as_float(v & 0xffff0000u)}; }
__device__ __forceinline__ int swap23(int r) { return (r & 0x13) | ((r & 4) << 1) | ((r & 8) >> 1); }
__device__ __forceinline__ float xchg32(float x) {
    auto r = __builtin_amdgcn_permlane32_swap(__float_as_uint(x), __float_as_uint(x), false, false);
    return __uint_as_float((threadIdx.x & 32) ? r[0] : r[1]);
}
__device__ __forceinline__ u32x4 gather8(unsigned x0, unsigned x1, unsigned y0, unsigned y1) {
    auto s0 = __builtin_amdgcn_permlane32_swap(x0, y0, false, false);
    auto s1 = __builtin_amdgcn_permlane32_swap(x1, y1, false, false);
    auto t0 = __builtin_amdgcn_permlane16_swap(s0[0], s0[1], false, false);
    auto t1 = __builtin_amdgcn_permlane16_swap(s1[0], s1[1], false, false);
    return (u32x4){t0[0], t1[0], t0[1], t1[1]};
}
__device__ __forceinline__ void publish(char* ctl, size_t word, u64 tag, int lane) {
    asm volatile("s_waitcnt vmcnt(0)" ::: "memory");
    if (lane == 0) __hip_atomic_store((GAS u64*)(ctl + word), tag, __ATOMIC_RELAXED, __HIP_MEMORY_SCOPE_AGENT);
}
__device__ __forceinline__ bool flags_ready(const char* f0, int n0, const char* f1, int n1, u64 tag, int lane) {
    u64 a = tag, b = tag;
    if (lane < n0) a = __hip_atomic_load((const GAS u64*)(f0 + lane * 8), __ATOMIC_RELAXED, __HIP_MEMORY_SCOPE_AGENT);
    if (lane < n1) b = __hip_atomic_load((const GAS u64*)(f1 + lane * 8), __ATOMIC_RELAXED, __HIP_MEMORY_SCOPE_AGENT);
    return __all(a == tag && b == tag);
}

struct Args { const u16* x; const u16* wqkv; const u16* wo; const u16* win; const u16* wsp; const u16* bsp; const u16* wout; u16* out; char* ws; };

struct RowsImg {
    unsigned v0, v1;
    __device__ __forceinline__ void init(int lane, unsigned rs) {
        const int r8 = lane >> 3, ch = lane & 7;
        v0 = r8 * rs + ((ch ^ ((lane >> 4) & 7)) << 4); v1 = r8 * rs + ((ch ^ ((4 + (lane >> 4)) & 7)) << 4);
    }
    __device__ __forceinline__ void issue(rsrc_t r, LAS char* dst, unsigned soff, unsigned rs) const {
#pragma unroll
        for (int q = 0; q < 4; ++q) dma16(r, dst + q * 1024, (q & 1) ? v1 : v0, soff + q * 8 * rs);
    }
    __device__ __forceinline__ void issue35(rsrc_t r, LAS char* dst, unsigned soff, unsigned rs, int p) const {
        const int q0 = p < 2 ? 3 * p : 6 + 5 * (p - 2);
        const unsigned va = (q0 & 1) ? v1 : v0, vb = (q0 & 1) ? v0 : v1;
#pragma unroll
        for (int q = 0; q < 3; ++q) dma16(r, dst + (q0 + q) * 1024, (q & 1) ? vb : va, soff + (q0 + q) * 8 * rs);
        if (p >= 2) {
#pragma unroll
            for (int q = 3; q < 5; ++q) dma16(r, dst + (q0 + q) * 1024, (q & 1) ? vb : va, soff + (q0 + q) * 8 * rs);
        }
    }
};
template <int NC> struct WImg {
    static constexpr int CPR = 2 * NC, BIG = (NC == 9) ? 5 : 4;
    unsigned v[BIG];
    static __device__ __forceinline__ int first(int p) { return NC == 9 ? (p < 2 ? 5 * p : 10 + 4 * (p - 2)) : (p < 2 ? 4 * p : 8 + 3 * (p - 2)); }
    __device__ __forceinline__ void init(int lane, int p, unsigned ldb) {
        const int I0 = first(p);
#pragma unroll
        for (int q = 0; q < BIG; ++q) {
            const int n = (I0 + q) * 64 + lane, pos = n / CPR, cc = n - pos * CPR;
            v[q] = ((pos & 32) + swap23(pos & 31)) * ldb + cc * 16;
        }
    }
    __device__ __forceinline__ void issue(rsrc_t r, LAS char* dst, unsigned soff, int p) const {
        const int I0 = first(p);
#pragma unroll
        for (int q = 0; q < BIG - 1; ++q) dma16(r, dst + (I0 + q) * 1024, v[q], soff);
        if (p < 2) dma16(r, dst + (I0 + BIG - 1) * 1024, v[BIG - 1], soff);
    }
};

#define MMA(acc, a, b) asm volatile("v_mfma_f32_16x16x32_bf16 %0, %1, %2, %0" : "+v"(acc) : "v"(a), "v"(b))
#define MMA_DRAIN() do { SB0(); asm volatile("s_nop 7\n\ts_nop 3"); SB0(); } while (0)
struct NoHook { __device__ __forceinline__ void operator()(int) const {} };
__device__ __forceinline__ s16x4 lds_tr1(LAS char* p) { return __builtin_amdgcn_ds_read_tr16_b64_v4i16((LAS s16x4*)p); }
__device__ __forceinline__ bf16x8 cat8(s16x4 a, s16x4 b) { return __builtin_shufflevector(a, b, 0, 1, 2, 3, 4, 5, 6, 7); }

template <int NC, bool XST> struct XWAcc { f32x4 a[2][NC]; f32x4 cs[3]; f32x4 sx[2], sq[2]; };
template <int NC> struct XF { bf16x8 x[2]; s16x4 w[NC][2]; };
template <int NC> struct XRot { int o[3]; };
template <int NC> __device__ __forceinline__ void xa_read1(XF<NC>& f, int i, LAS char* pX, LAS char* pW, const XRot<NC>& rot) {
    if (i < 2) f.x[i] = lds_b128(pX + i * 2048);
    else {
        const int c = (i - 2) >> 1;
        LAS char* q = c >= NC - 3 ? pW + rot.o[c >= NC - 3 ? c - (NC - 3) : 0] : pW;
        f.w[c][i & 1] = lds_tr1(q + c * 32 + (i & 1) * (8 * NC * 32));
    }
}
template <int NC, bool TR, bool XST, int DBL, bool LOAD>
__device__ __forceinline__ void xa_step(XWAcc<NC, XST>& A, const XF<NC>& f, XF<NC>& n, LAS char* pX, LAS char* pW, const XRot<NC>& rot, const bf16x8& ones) {
    constexpr int R = 2 + 2 * NC;
    int rd = 0, gap = 0;
#define XA_GAP() do { if (LOAD) { if (rd < R) { xa_read1<NC>(n, rd, pX, pW, rot); ++rd; } if (gap < DBL && rd < R) { xa_read1<NC>(n, rd, pX, pW, rot); ++rd; } } ++gap; SB0(); } while (0)
#pragma unroll
    for (int c = 0; c < NC; ++c) {
        const bf16x8 w = cat8(f.w[c][0], f.w[c][1]);
#pragma unroll
        for (int t = 0; t < 2; ++t) {
            if (TR) MMA(A.a[t][c], w, f.x[t]); else MMA(A.a[t][c], f.x[t], w);
            XA_GAP();
        }
        if ((c & 3) == 0) {
            if (TR) MMA(A.cs[c >> 2], w, ones); else MMA(A.cs[c >> 2], ones, w);
            XA_GAP();
        }
    }
    if (XST) {
#pragma unroll
        for (int t = 0; t < 2; ++t) { MMA(A.sx[t], ones, f.x[t]); XA_GAP(); MMA(A.sq[t], f.x[t], f.x[t]); XA_GAP(); }
    }
#pragma unroll
    for (int k = 0; k < 4; ++k) if (LOAD && rd < R) { xa_read1<NC>(n, rd, pX, pW, rot); ++rd; }
    SB0();
#undef XA_GAP
}
template <int NC, bool XST> __device__ __forceinline__ void xw_zero(XWAcc<NC, XST>& A) {
    const f32x4 z = {0.f, 0.f, 0.f, 0.f};
#pragma unroll
    for (int t = 0; t < 2; ++t) {
#pragma unroll
        for (int c = 0; c < NC; ++c) { A.a[t][c] = z; asm volatile("" : "+v"(A.a[t][c])); }
        if (XST) { A.sx[t] = z; A.sq[t] = z; asm volatile("" : "+v"(A.sx[t]), "+v"(A.sq[t])); }
    }
    A.cs[0] = z; A.cs[1] = z; asm volatile("" : "+v"(A.cs[0]), "+v"(A.cs[1]));
    if (NC > 8) { A.cs[2] = z; asm volatile("" : "+v"(A.cs[2])); }
}
template <int NC, bool TR, bool XST, int NST, class Hook>
__device__ __forceinline__ void xw_gemm(XWAcc<NC, XST>& A, LAS char* lds, int g0, int m, int lane, Hook hook) {
    asm volatile("" : "+v"(lane));
    const int fr = lane & 15, g = lane >> 4, row = 32 * m + fr;
    constexpr int G = 2 * NC + (NC + 3) / 4 + (XST ? 4 : 0), R = 2 + 2 * NC;
    constexpr int DA = R > G - 7 ? R - (G - 7) : 0, DB = R > G ? R - G : 0;
    LAS char* pX0 = lds + row * 128 + ((g ^ ((row >> 1) & 7)) << 4);
    LAS char* pX1 = lds + ((row * 128 + ((g ^ ((row >> 1) & 7)) << 4)) ^ 64);
    LAS char* pW0 = lds + 16384 + (16 * (g >> 1) + 4 * (g & 1) + (fr >> 2)) * (NC * 32) + (lane & 3) * 8 + m * 32;
    LAS char* pW1 = pW0 + 32 * NC * 32;
    XRot<NC> rot;
#pragma unroll
    for (int k = 0; k < 3; ++k) rot.o[k] = (NC - 3 + k + m >= NC) ? -NC * 32 : 0;
    XF<NC> f0, f1;
    bf16x8 ones = {0x3F80, 0x3F80, 0x3F80, 0x3F80, 0x3F80, 0x3F80, 0x3F80, 0x3F80};
    asm volatile("" : "+v"(ones));
    {
        const unsigned sb = (g0 & 3) * SLOT;
#pragma unroll
        for (int i = 0; i < R; ++i) xa_read1<NC>(f0, i, pX0 + sb, pW0 + sb, rot);
    }
    asm volatile("s_nop 1"); SB0();
#pragma unroll 1
    for (int s = 0; s < NST - 1; ++s) {
        const unsigned sb = ((g0 + s) & 3) * SLOT, sn = ((g0 + s + 1) & 3) * SLOT;
        LAS char* nX = pX0 + sn; LAS char* nW = pW0 + sn;
        xa_step<NC, TR, XST, DA, true>(A, f0, f1, pX1 + sb, pW1 + sb, rot, ones);
        asm volatile("" : "+v"(nX), "+v"(nW));
        WAIT_L0(); BAR(); SB0();
        hook(s);
        xa_step<NC, TR, XST, DB, true>(A, f1, f0, nX, nW, rot, ones);
    }
    const unsigned sb = ((g0 + NST - 1) & 3) * SLOT;
    xa_step<NC, TR, XST, DA, true>(A, f0, f1, pX1 + sb, pW1 + sb, rot, ones);
    WAIT_L0(); BAR(); SB0();
    xa_step<NC, TR, XST, 0, false>(A, f1, f0, pX0, pW0, rot, ones);
    MMA_DRAIN();
}
template <int NC, bool TR, bool XST> __device__ __forceinline__ void share_colsum(const XWAcc<NC, XST>& A, LAS char* lds, int m, int lane) {
    LAS float* tab = (LAS float*)(lds + SP_CS);
    const int fr = lane & 15, g = lane >> 4;
#pragma unroll
    for (int k = 0; k < (NC + 3) / 4; ++k) {
        int c = m + 4 * k; c = c >= NC ? c - NC : c;
        if (TR) { if (fr == 0) *(LAS f32x4*)(tab + c * 16 + 4 * g) = A.cs[k]; }
        else { if (g == 0) tab[c * 16 + fr] = A.cs[k][0]; }
    }
    WAIT_L0(); BAR();
}
template <int NC> __device__ __forceinline__ void unrotate(unsigned (&v)[NC][2], int m) {
    unsigned u[NC][2];
#define UNROT(M) _Pragma("unroll") for (int c = 0; c < NC; ++c) { u[(c + M) % NC][0] = v[c][0]; u[(c + M) % NC][1] = v[c][1]; }
    if (m == 0) { UNROT(0) } else if (m == 1) { UNROT(1) } else if (m == 2) { UNROT(2) } else { UNROT(3) }
#undef UNROT
#pragma unroll
    for (int c = 0; c < NC; ++c) { v[c][0] = u[c][0]; v[c][1] = u[c][1]; asm volatile("" : "+v"(v[c][0]), "+v"(v[c][1])); }
}

struct MFrag { bf16x8 w[2]; bf16x8 z[9]; };
template <bool LOAD> __device__ __forceinline__ void m_step(f32x4 (&acc)[2][9], const MFrag& f, MFrag& n, LAS char* pW, LAS char* pZ) {
#pragma unroll
    for (int i = 0; i < 18; ++i) {
        MMA(acc[i & 1][i >> 1], f.z[i >> 1], f.w[i & 1]);
        if (LOAD) { if (i < 2) n.w[i] = lds_b128(pW + i * 2048); else if (i < 11) n.z[i - 2] = lds_b128(pZ + (i - 2) * 1024); }
        SB0();
    }
}
__device__ __forceinline__ void o_read(MFrag& f, LAS char* pW, LAS char* pZ) {
#pragma unroll
    for (int t = 0; t < 2; ++t) f.w[t] = lds_b128(pW + t * 2048);
#pragma unroll
    for (int c = 0; c < 9; ++c) f.z[c] = lds_b128(pZ + c * 1024);
}
__device__ __forceinline__ void o_mma(f32x4 (&acc)[2][9], const MFrag& f) {
#pragma unroll
    for (int c = 0; c < 9; ++c)
#pragma unroll
        for (int t = 0; t < 2; ++t) acc[t][c] = MFMA(f.z[c], f.w[t], acc[t][c]);
}
__device__ __forceinline__ void o_sched() {
#pragma unroll
    for (int i = 0; i < 11; ++i) { __builtin_amdgcn_sched_group_barrier(0x008, 1, 0); __builtin_amdgcn_sched_group_barrier(0x100, 1, 0); }
    __builtin_amdgcn_sched_group_barrier(0x008, 7, 0);
    SB0();
}
template <int NST> __device__ __forceinline__ void main_gemm(f32x4 (&acc)[2][9], LAS char* lds, int g0, int m, int lane) {
    asm volatile("" : "+v"(lane));
    const int fr = lane & 15, g = lane >> 4, row = 32 * m + fr;
    LAS char* pW0 = lds + row * 128 + ((g ^ ((row >> 1) & 7)) << 4);
    LAS char* pW1 = lds + ((row * 128 + ((g ^ ((row >> 1) & 7)) << 4)) ^ 64);
    LAS char* pZ0 = lds + 16384 + lane * 16;
    LAS char* pZ1 = pZ0 + 9216;
#pragma unroll
    for (int t = 0; t < 2; ++t)
#pragma unroll
        for (int c = 0; c < 9; ++c) acc[t][c] = (f32x4){0.f, 0.f, 0.f, 0.f};
    MFrag f0, f1;
    o_read(f0, pW0 + (g0 & 3) * SLOT, pZ0 + (g0 & 3) * SLOT);
#pragma unroll 1
    for (int s = 0; s < NST - 1; ++s) {
        const unsigned sb = ((g0 + s) & 3) * SLOT, sn = ((g0 + s + 1) & 3) * SLOT;
        o_read(f1, pW1 + sb, pZ1 + sb);
        o_mma(acc, f0);
        o_sched();
        WAIT_L0(); BAR(); SB0();
        o_read(f0, pW0 + sn, pZ0 + sn);
        o_mma(acc, f1);
        o_sched();
    }
    const unsigned sb = ((g0 + NST - 1) & 3) * SLOT;
    o_read(f1, pW1 + sb, pZ1 + sb);
    o_mma(acc, f0);
    o_sched();
    WAIT_L0(); BAR(); SB0();
    o_mma(acc, f1);
}

struct EFrag { s16x4 w[2]; bf16x8 b[5]; };
template <bool LOAD, bool MM> __device__ __forceinline__ void e_step(f32x4 (&acc)[5], const EFrag& f, EFrag& n, LAS char* pW, LAS char* pB) {
    const bf16x8 w = cat8(f.w[0], f.w[1]);
#pragma unroll
    for (int t = 0; t < 5; ++t) {
        if (MM) MMA(acc[t], w, f.b[t]);
        if (LOAD) {
            if (t == 0) { n.w[0] = lds_tr1(pW); n.w[1] = lds_tr1(pW + 2048); }
            else if (t == 1) { n.b[0] = lds_b128(pB); n.b[1] = lds_b128(pB + 1024); }
            else n.b[t] = lds_b128(pB + t * 1024);
        }
        SB0();
    }
}
template <int NST, int NKS, bool MORE, class Hook> __device__ __forceinline__ void e_gemm(f32x4 (&acc)[5], LAS char* lds, int g0, int w, int lane, Hook hook) {
    asm volatile("" : "+v"(lane));
    const int g = lane >> 4, q = (lane & 15) >> 2;
    LAS char* pW = lds + (4 * g + q) * 128 + ((w ^ ((2 * g + (q >> 1)) & 3)) << 5) + (lane & 3) * 8;
    LAS char* pB = lds + 16384 + lane * 16;
#pragma unroll
    for (int t = 0; t < 5; ++t) { acc[t] = (f32x4){0.f, 0.f, 0.f, 0.f}; asm volatile("" : "+v"(acc[t])); }
    EFrag f0, f1;
    e_step<true, false>(acc, f1, f0, pW + (g0 & 3) * SLOT, pB + (g0 & 3) * SLOT);
    asm volatile("s_nop 1"); SB0();
#pragma unroll 1
    for (int s = 0; s < NST - 1; ++s) {
        const unsigned sb = ((g0 + s) & 3) * SLOT, sn = ((g0 + s + 1) & 3) * SLOT;
        e_step<true, true>(acc, f0, f1, pW + sb + 4096, pB + sb + 5120);
        e_step<true, true>(acc, f1, f0, pW + sb + 8192, pB + sb + 10240);
        e_step<true, true>(acc, f0, f1, pW + sb + 12288, pB + sb + 15360);
        WAIT_L0(); BAR(); SB0();
        hook(s);
        e_step<true, true>(acc, f1, f0, pW + sn, pB + sn);
    }
    const unsigned sb = ((g0 + NST - 1) & 3) * SLOT;
    constexpr int LAST = NKS - 4 * (NST - 1);
    e_step<true, true>(acc, f0, f1, pW + sb + 4096, pB + sb + 5120);
    if (LAST == 4) {
        e_step<true, true>(acc, f1, f0, pW + sb + 8192, pB + sb + 10240);
        e_step<true, true>(acc, f0, f1, pW + sb + 12288, pB + sb + 15360);
    }
    if (MORE) { WAIT_L0(); BAR(); SB0(); }
    e_step<false, true>(acc, f1, f0, pW, pB);
    MMA_DRAIN();
}

__device__ __forceinline__ float dot2(unsigned a, unsigned b, float c) { return __builtin_amdgcn_fdot2_f32_bf16(__builtin_bit_cast(bf16x2_t, a), __builtin_bit_cast(bf16x2_t, b), c, false); }
__device__ __forceinline__ unsigned at_region(int r, int p) { return (unsigned)(((r >> 1) ? 0 : 3 * SLOT) + ((r & 1) ? 16384 + p * 5120 : p * 4096)); }
__device__ __forceinline__ unsigned at_row(int rho, int p) { const int r = rho / 14; return at_region(r, p) + (unsigned)(rho - r * 14) * 288u; }
__device__ __forceinline__ void attn_issue(rsrc_t rQ, LAS char* lds, int n, int p, int lane, int half) {
#pragma unroll
    for (int rr = 0; rr < 2; ++rr)
#pragma unroll
        for (int q = 0; q < 4; ++q) {
            const int r = 2 * half + rr, c = q * 64 + lane, lr = c / 18, cc = c - lr * 18, rho = r * 14 + lr;
            int t, seg;
            if (rho < 22) { t = 8 * n - 14 + rho; seg = 1; } else if (rho < 44) { t = 8 * n - 14 + (rho - 22); seg = 2; } else { t = 8 * n + (rho - 44); seg = 0; }
            const unsigned voff = (t < 0 || rho >= 52 || lr >= 14) ? 0x7ffffff0u : (unsigned)(t * (QW * 2) + (seg * D_ + 144 * p) * 2 + cc * 16);
            dma16_dev(rQ, lds + at_region(r, p) + q * 1024, voff, 0);
        }
}
__device__ __forceinline__ void attn_compute(LAS char* lds, rsrc_t rO, int n, int p, int lane) {
    const int i = lane >> 3, hh = lane & 7, t = 8 * n + i;
    const unsigned hb = hh * 36, r0 = at_region(0, p) + hb, r1 = at_region(1, p) + hb - 14 * 288, r2 = at_region(2, p) + hb - 28 * 288, r3 = at_region(3, p) + hb - 42 * 288;
    unsigned qv[9];
    {
        LAS unsigned* q = (LAS unsigned*)(lds + r3 + (44 + i) * 288);
#pragma unroll
        for (int d = 0; d < 9; ++d) qv[d] = q[d];
    }
    float lg[8];
#pragma unroll
    for (int w = 0; w < 8; ++w) {
        const int rho = 14 + i - 2 * w;
        LAS unsigned* kp = (LAS unsigned*)(lds + (rho < 14 ? r0 : r1) + rho * 288);
        float s = 0.f;
#pragma unroll
        for (int d = 0; d < 9; ++d) s = dot2(qv[d], kp[d], s);
        lg[w] = (t - 2 * w >= 0) ? s * (0.23570226039551584f * 1.4426950408889634f) : -1e9f;
    }
    float mx = fmaxf(fmaxf(fmaxf(lg[0], lg[1]), fmaxf(lg[2], lg[3])), fmaxf(fmaxf(lg[4], lg[5]), fmaxf(lg[6], lg[7])));
    float sum = 0.f;
#pragma unroll
    for (int w = 0; w < 8; ++w) { lg[w] = __builtin_amdgcn_exp2f(lg[w] - mx); sum += lg[w]; }
    const float inv = __builtin_amdgcn_rcpf(sum);
    unsigned pp[4];
#pragma unroll
    for (int w = 0; w < 4; ++w) pp[w] = pack_bf16(lg[2 * w] * inv, lg[2 * w + 1] * inv);
    float acc[18];
#pragma unroll
    for (int d = 0; d < 18; ++d) acc[d] = 0.f;
#pragma unroll
    for (int w = 0; w < 4; ++w) {
        const int ra = 22 + 14 + i - 4 * w, rb = ra - 2;
        LAS unsigned* va = (LAS unsigned*)(lds + (ra < 28 ? r1 : ra < 42 ? r2 : r3) + ra * 288);
        LAS unsigned* vb = (LAS unsigned*)(lds + (rb < 28 ? r1 : rb < 42 ? r2 : r3) + rb * 288);
#pragma unroll
        for (int d = 0; d < 9; ++d) {
            const unsigned x = va[d], y = vb[d];
            acc[2 * d] = dot2(__builtin_amdgcn_perm(y, x, 0x05040100u), pp[w], acc[2 * d]);
            acc[2 * d + 1] = dot2(__builtin_amdgcn_perm(y, x, 0x07060302u), pp[w], acc[2 * d + 1]);
        }
    }
    LAS char* ob = lds + SP_AO + p * 2304;
#pragma unroll
    for (int d = 0; d < 9; ++d) *(LAS unsigned*)(ob + i * 288 + hh * 36 + d * 4) = pack_bf16(acc[2 * d], acc[2 * d + 1]);
    WAIT_L0();
    {
        const int r = lane & 7, g = (lane >> 3) & 3, tt = n >> 1, rl = (8 * n + r) & 15;
#pragma unroll
        for (int q = 0; q < 2; ++q) {
            const int a = (lane >> 5) + 2 * q;
            const u32x2 lo = *(LAS u32x2*)(ob + r * 288 + (32 * a + 4 * g) * 2), hi = *(LAS u32x2*)(ob + r * 288 + (32 * a + 16 + 4 * g) * 2);
            st128_dev(rO, ((tt * 18 + 4 * p + a) * 64 + rl + 16 * g) * 16, (u32x4){lo.x, lo.y, hi.x, hi.y});
        }
        const u32x2 v = *(LAS u32x2*)(ob + r * 288 + (128 + 4 * g) * 2);
        if (lane < 32) st64_dev(rO, ((tt * 18 + 16 + (p >> 1)) * 64 + rl + 16 * g) * 16 + (p & 1) * 8, v);
    }
}

__global__ __launch_bounds__(512, 2) void fused_kernel(Args a) {
    asm volatile("fusedk_pf_begin:");
    extern __shared__ __attribute__((aligned(16))) char smem[];
    LAS char* lds = (LAS char*)smem;
    const int wid = __builtin_amdgcn_readfirstlane(threadIdx.x >> 6), lane = threadIdx.x & 63;
    const int xcd = blockIdx.x & 7, loc = blockIdx.x >> 3;
    const int ti = (xcd & 1) * 8 + (loc & 7), tj = (xcd >> 1) * 4 + (loc >> 3);
    const int nt = xcd * 30 + loc;
    const bool fin = loc < 30 && nt < 234;
    const int rb = nt / 9, cb = nt - rb * 9;
    char* ctl = a.ws + WS_CTL;
    if (lane < 2) __builtin_amdgcn_raw_ptr_buffer_load_lds(mk_rsrc(ctl, 4096), (lds_vp)(lds + SP_EP + wid * 64), 4, lane * 4 + (unsigned)CTL_EP, 0, 0, 16);
#define TAG_FROM_EPOCH() \
    u64 ep_; asm volatile("ds_read_b64 %0, %1\n\ts_waitcnt lgkmcnt(0)" : "=&v"(ep_) : "v"((unsigned)(size_t)(lds + SP_EP + wid * 64)) : "memory"); \
    const u64 tg_ = ep_ * 0x9E3779B97F4A7C15ull + 0xD1B54A32D192ED03ull; \
    const u64 tag = ((u64)(unsigned)__builtin_amdgcn_readfirstlane((unsigned)(tg_ >> 32)) << 32) | (unsigned)__builtin_amdgcn_readfirstlane((unsigned)tg_)
    const int an = ti * 16 + tj;

    if (__builtin_expect(wid < 4, 0)) {
    const int m = wid, fr = lane & 15, g4 = lane >> 4;
    ST_DECL; ST(0);
    const rsrc_t rQs = mk_rsrc(a.ws + WS_QKV, S_ * QW * 2), rZs = mk_rsrc(a.ws + WS_Z2T, S_ * FH * 2), rHs = mk_rsrc(a.ws + WS_HF, S_ * FH * 2), rSt = mk_rsrc(a.ws + WS_ST, S_ * 16 * 16);
    LAS float* cst = (LAS float*)(lds + SP_CS);
    float xm[2], xr[2];
    {
        unsigned dummy = 0;
        if (wid == 0) {
            u64 base; unsigned sz;
            unsigned back;
            asm volatile("s_getpc_b64 %0\nfusedk_pf_here:\n\ts_mov_b32 %1, fusedk_pf_end-fusedk_pf_begin\n\ts_mov_b32 %2, fusedk_pf_here-fusedk_pf_begin" : "=s"(base), "=s"(sz), "=s"(back));
            base -= back;
            const unsigned off = (unsigned)(loc * 13 + lane) * 128u;
            if (lane < 13 && (int)off < (int)sz) asm volatile("global_load_dword %0, %1, off" : "=v"(dummy) : "v"(base + off));
        }
        asm volatile("s_waitcnt vmcnt(0)" : "+v"(dummy));
    }
    WAIT_V(0);
    TAG_FROM_EPOCH();
    BAR();
    ST(1);
    {
        XWAcc<7, true> A; xw_zero(A);
        xw_gemm<7, true, true, 9>(A, lds, G_QKV, m, lane, NoHook());
        ST(2);
        share_colsum<7, true, true>(A, lds, m, lane);
#pragma unroll
        for (int t = 0; t < 2; ++t) {
            const int r3 = fr & 3;
            const float dq = r3 == 0 ? A.sq[t][0] : r3 == 1 ? A.sq[t][1] : r3 == 2 ? A.sq[t][2] : A.sq[t][3];
            const float sq = __shfl(dq, (fr >> 2) * 16 + fr);
            xm[t] = A.sx[t][0] * (1.0f / D_);
            xr[t] = __builtin_amdgcn_rsqf(fmaxf(sq * (1.0f / D_) - xm[t] * xm[t], 0.f) + 1e-5f);
        }
        const int c0 = (tj * 27) >> 2, dup = tj > 0 && c0 < (((tj - 1) * 27) >> 2) + 7;
#pragma unroll
        for (int t = 0; t < 2; ++t) {
            unsigned pk[7][2];
#pragma unroll
            for (int c = 0; c < 7; ++c) {
                const int tile = c + m >= 7 ? c + m - 7 : c + m;
                const f32x4 cs = *(LAS f32x4*)(cst + tile * 16 + 4 * g4);
                pk[c][0] = pack2((lo2(A.a[t][c]) - lo2(cs) * xm[t]) * xr[t]); pk[c][1] = pack2((hi2(A.a[t][c]) - hi2(cs) * xm[t]) * xr[t]);
            }
            unrotate<7>(pk, m);
            const unsigned ro = (unsigned)(ti * 128 + 32 * m + 16 * t + fr) * (QW * 2) + c0 * 32;
#pragma unroll
            for (int pp = 0; pp < 3; ++pp) st128_dev(rQs, ro + (1 + 2 * pp) * 32 + g4 * 16, gather8(pk[1 + 2 * pp][0], pk[1 + 2 * pp][1], pk[2 + 2 * pp][0], pk[2 + 2 * pp][1]));
            if (!dup) st64_dev(rQs, ro + g4 * 8, (u32x2){pk[0][0], pk[0][1]});
        }
    }
    ST(3);
    unsigned zp[2][9][2];
    {
        XWAcc<9, false> A; xw_zero(A);
        xw_gemm<9, false, false, 9>(A, lds, G_Z2, m, lane, [&](int s) { if (s == 4) publish(ctl, CTL_QF + ((ti * 16 + tj) * 4 + m) * 8, tag, lane); });
        ST(4);
        share_colsum<9, false, false>(A, lds, m, lane);
        f32x2 rm[2][2], rr[2][2];
#pragma unroll
        for (int t = 0; t < 2; ++t)
#pragma unroll
            for (int h = 0; h < 2; ++h) {
                rm[t][h] = (f32x2){__shfl(xm[t], 4 * g4 + 2 * h), __shfl(xm[t], 4 * g4 + 2 * h + 1)};
                rr[t][h] = (f32x2){__shfl(xr[t], 4 * g4 + 2 * h), __shfl(xr[t], 4 * g4 + 2 * h + 1)};
            }
        f32x2 s1[2][2], s2[2][2];
#pragma unroll
        for (int t = 0; t < 2; ++t)
#pragma unroll
            for (int h = 0; h < 2; ++h) { s1[t][h] = (f32x2){0.f, 0.f}; s2[t][h] = (f32x2){0.f, 0.f}; }
#pragma unroll
        for (int c = 0; c < 9; ++c) {
            const float cs = cst[(c + m >= 9 ? c + m - 9 : c + m) * 16 + fr];
#pragma unroll
            for (int t = 0; t < 2; ++t)
#pragma unroll
                for (int h = 0; h < 2; ++h) {
                    const f32x2 v = gelu2(((h ? hi2(A.a[t][c]) : lo2(A.a[t][c])) - rm[t][h] * cs) * rr[t][h]);
                    s1[t][h] += v; s2[t][h] += v * v;
                    zp[t][c][h] = pack2(v);
                    asm volatile("" : "+v"(zp[t][c][h]));
                }
        }
        float mm = 0.f, qq = 0.f;
        {
            const int idx = lane & 7;
            float a1[8], a2[8];
#pragma unroll
            for (int k = 0; k < 8; ++k) { a1[k] = s1[k >> 2][(k >> 1) & 1][k & 1]; a2[k] = s2[k >> 2][(k >> 1) & 1][k & 1]; }
#pragma unroll
            for (int k = 0; k < 8; ++k) { a1[k] = dpp_add<0x128>(a1[k]); a2[k] = dpp_add<0x128>(a2[k]); }
#pragma unroll
            for (int k = 0; k < 8; ++k) { a1[k] = dpp_add<0x124>(a1[k]); a2[k] = dpp_add<0x124>(a2[k]); }
#pragma unroll
            for (int k = 0; k < 8; ++k) { a1[k] = dpp_add<0x122>(a1[k]); a2[k] = dpp_add<0x122>(a2[k]); }
#pragma unroll
            for (int k = 0; k < 8; ++k) { a1[k] = dpp_add<0x121>(a1[k]); a2[k] = dpp_add<0x121>(a2[k]); }
            float u1 = a1[0], u2 = a2[0];
#pragma unroll
            for (int k = 1; k < 8; ++k) { u1 = idx == k ? a1[k] : u1; u2 = idx == k ? a2[k] : u2; }
            mm = u1 * (1.0f / 144.0f);
            qq = fmaxf(u2 - u1 * mm, 0.f);
            if (fr < 8) st128_dev(rSt, ((ti * 16 + tj) * 128 + 32 * m + 16 * (idx >> 2) + 4 * g4 + (idx & 3)) * 16,
                                  (u32x4){__float_as_uint(mm), (unsigned)tag, __float_as_uint(qq), (unsigned)(tag >> 32)});
        }
    }
    ST(5);
    unsigned z1p[2][9][2];
    {
        XWAcc<9, false> A; xw_zero(A);
        xw_gemm<9, true, false, 2>(A, lds, G_Z1, m, lane, NoHook());
        ST(6);
        float mu, rs;
        {
            const unsigned off = ((ti * 16 + (lane >> 5) * 8) * 128 + 32 * m + (lane & 31)) * 16;
            u32x4 v[8]; unsigned spins = 0;
            for (;;) {
#pragma unroll
                for (int k = 0; k < 8; ++k) v[k] = __builtin_amdgcn_raw_buffer_load_b128(rSt, off + k * 2048, 0, 16);
                unsigned bad = 0;
#pragma unroll
                for (int k = 0; k < 8; ++k) bad |= (v[k].y ^ (unsigned)tag) | (v[k].w ^ (unsigned)(tag >> 32));
                if (__all(bad == 0) || ++spins > SPIN_CAP) break;
                __builtin_amdgcn_s_sleep(2);
            }
            float s = 0.f;
#pragma unroll
            for (int k = 0; k < 8; ++k) s += __uint_as_float(v[k].x);
            s += xchg32(s);
            mu = s * (1.0f / 16.0f);
            float q = 0.f;
#pragma unroll
            for (int k = 0; k < 8; ++k) { const float d = __uint_as_float(v[k].x) - mu; q += __uint_as_float(v[k].z) + 144.0f * d * d; }
            q += xchg32(q);
            rs = __builtin_amdgcn_rsqf(q * (1.0f / FH) + 1e-5f);
        }
        ST(7);
        f32x2 mr[2][2], rr[2][2];
#pragma unroll
        for (int t = 0; t < 2; ++t)
#pragma unroll
            for (int h = 0; h < 2; ++h) {
                const int src = 16 * t + 4 * g4 + 2 * h;
                mr[t][h] = (f32x2){__shfl(mu, src), __shfl(mu, src + 1)}; rr[t][h] = (f32x2){__shfl(rs, src), __shfl(rs, src + 1)};
            }
        const unsigned zo = (unsigned)((tj * 64 + ti * 4 + m) * 9) * 1024 + lane * 16;
#pragma unroll
        for (int c = 0; c < 9; ++c) {
            const unsigned x0 = pack2((unpack2(zp[0][c][0]) - mr[0][0]) * rr[0][0]), x1 = pack2((unpack2(zp[0][c][1]) - mr[0][1]) * rr[0][1]);
            const unsigned y0 = pack2((unpack2(zp[1][c][0]) - mr[1][0]) * rr[1][0]), y1 = pack2((unpack2(zp[1][c][1]) - mr[1][1]) * rr[1][1]);
            st128_dev(rZs, zo + (c + m >= 9 ? c + m - 9 : c + m) * 1024, gather8(x0, x1, y0, y1));
        }
        ST(8);
        xw_gemm<9, true, false, 7>(A, lds, G_Z1 + 2, m, lane, [&](int s) { if (s == 1) publish(ctl, CTL_ZF + ((tj * 16 + ti) * 4 + m) * 8, tag, lane); });
        ST(9);
        share_colsum<9, true, false>(A, lds, m, lane);
#pragma unroll
        for (int c = 0; c < 9; ++c) {
            const f32x4 cs = *(LAS f32x4*)(cst + (c + m >= 9 ? c + m - 9 : c + m) * 16 + 4 * g4);
#pragma unroll
            for (int t = 0; t < 2; ++t) {
                z1p[t][c][0] = pack2(gelu2((lo2(A.a[t][c]) - lo2(cs) * xm[t]) * xr[t])); z1p[t][c][1] = pack2(gelu2((hi2(A.a[t][c]) - hi2(cs) * xm[t]) * xr[t]));
                asm volatile("" : "+v"(z1p[t][c][0]), "+v"(z1p[t][c][1]));
            }
        }
        unrotate<9>(z1p[0], m); unrotate<9>(z1p[1], m);
    }
    ST(10);
    {
        f32x4 acc[2][9];
        float bt[2];
#pragma unroll
        for (int t = 0; t < 2; ++t) bt[t] = bf2f(a.bsp[ti * 128 + 32 * m + 16 * t + fr]);
        main_gemm<32>(acc, lds, G_MAIN, m, lane);
        ST(11);
#pragma unroll
        for (int t = 0; t < 2; ++t) {
            const unsigned hp = (unsigned)(ti * 8 + 2 * m + t) * 72 * 1024 + lane * 16;
#pragma unroll
            for (int pp = 0; pp < 4; ++pp) {
                u32x4 o;
                o.x = pack2((lo2(acc[t][2 * pp]) + bt[t]) * unpack2(z1p[t][2 * pp][0])); o.y = pack2((hi2(acc[t][2 * pp]) + bt[t]) * unpack2(z1p[t][2 * pp][1]));
                o.z = pack2((lo2(acc[t][2 * pp + 1]) + bt[t]) * unpack2(z1p[t][2 * pp + 1][0])); o.w = pack2((hi2(acc[t][2 * pp + 1]) + bt[t]) * unpack2(z1p[t][2 * pp + 1][1]));
                st128_dev(rHs, hp + (tj * 4 + pp) * 1024, o);
            }
            u32x2 o;
            o.x = pack2((lo2(acc[t][8]) + bt[t]) * unpack2(z1p[t][8][0])); o.y = pack2((hi2(acc[t][8]) + bt[t]) * unpack2(z1p[t][8][1]));
            st64_dev(rHs, hp + (64 + (tj >> 1)) * 1024 + (tj & 1) * 8, o);
        }
    }
    ST(12);
    publish(ctl, CTL_HF + ((ti * 16 + tj) * 4 + m) * 8, tag, lane);
    ST(13);
    if (!fin) { ST_END(0); return; }
    {
        f32x4 sa[5], ga[5];
        u32x2 xv[5];
        const int fr = lane & 15, g4 = lane >> 4;
#pragma unroll
        for (int t = 0; t < 5; ++t) xv[t] = *(const u32x2*)(a.x + (size_t)(min(rb * 5 + t, 127) * 16 + fr) * D_ + cb * 64 + m * 16 + g4 * 4);
        BAR();
        ST(14);
        e_gemm<18, 72, true>(ga, lds, G_G, m, lane, NoHook());
        ST(15);
        e_gemm<5, 18, false>(sa, lds, G_S, m, lane, NoHook());
        ST(16);
#pragma unroll
        for (int t = 0; t < 5; ++t) {
            if (rb * 5 + t < 128) {
                float o[4];
#pragma unroll
                for (int r = 0; r < 4; ++r) {
                    const float gv = ga[t][r], sg = gv * __builtin_amdgcn_rcpf(1.0f + __builtin_amdgcn_exp2f(gv * -1.4426950408889634f));
                    const float xx = (r & 1) ? bfhi(xv[t][r >> 1]) : bflo(xv[t][r >> 1]);
                    o[r] = xx + sa[t][r] * sg;
                }
                u32x2 ov; ov.x = pack_bf16(o[0], o[1]); ov.y = pack_bf16(o[2], o[3]);
                *(u32x2*)(a.out + (size_t)((rb * 5 + t) * 16 + fr) * D_ + cb * 64 + m * 16 + g4 * 4) = ov;
            }
        }
    }
    if (blockIdx.x == 0 && threadIdx.x == 0) __hip_atomic_store((GAS u64*)(ctl + CTL_EP), tag, __ATOMIC_RELAXED, __HIP_MEMORY_SCOPE_AGENT);
    ST(17);
    ST_END(0);
    asm volatile("fusedk_pf_end:");
        return;
    }
    {

        const int p = wid - 4;
        ST_DECL; ST(0);
#define rX mk_rsrc(a.x, S_ * D_ * 2)
#define rQW mk_rsrc(a.wqkv, D_ * QW * 2)
#define rW mk_rsrc(a.win, D_ * F_ * 2)
#define rS mk_rsrc(a.wsp, S_ * S_ * 2)
#define rZ mk_rsrc(a.ws + WS_Z2T, S_ * FH * 2)
#define rQ mk_rsrc(a.ws + WS_QKV, S_ * QW * 2)
#define rO mk_rsrc(a.ws + WS_OF, S_ * D_ * 2)
        RowsImg xi; xi.init(lane, D_ * 2);
        RowsImg si; si.init(lane, S_ * 2);
        WImg<7> wq; wq.init(lane, p, QW * 2);
        WImg<9> wz; wz.init(lane, p, F_ * 2);
        const unsigned sX0 = (unsigned)(ti * 128) * (D_ * 2), sQ = (unsigned)((tj * 27) >> 2) * 32, sZ1 = (unsigned)(tj * 144) * 2, sZ2 = sZ1 + FH * 2;
        const unsigned sS = (unsigned)(ti * 128 + 32 * p) * (S_ * 2), sZ = (unsigned)tj * (64u * 9216u);
        const unsigned vl = lane * 16;
        const unsigned vE = (lane >> 3) * (D_ * 2) + ((((lane & 7) >> 1) ^ ((lane >> 4) & 3)) << 5) + (lane & 1) * 16;
        const int zI0 = WImg<9>::first(p);
        auto iss_q = [&](int g) {
            LAS char* slot = lds + (g & 3) * SLOT;
            xi.issue35(rX, slot, sX0 + g * 128, D_ * 2, p); wq.issue(rQW, slot + 16384, sQ + g * 64 * (QW * 2), p);
        };
        auto iss_z = [&](int g, int s, unsigned col) {
            LAS char* slot = lds + (g & 3) * SLOT;
            xi.issue35(rX, slot, sX0 + s * 128, D_ * 2, p);
            wz.issue(rW, slot + 16384, col + s * 64 * (F_ * 2), p);
        };
        auto iss_m = [&](int g) {
            LAS char* slot = lds + (g & 3) * SLOT; const int s = g - G_MAIN;
            si.issue(rS, slot + p * 4096, sS + s * 128, S_ * 2);
#pragma unroll
            for (int q = 0; q < 4; ++q) dma16_dev(rZ, slot + 16384 + (zI0 + q) * 1024, vl, sZ + s * 18432 + (zI0 + q) * 1024);
            if (p < 2) dma16_dev(rZ, slot + 16384 + (zI0 + 4) * 1024, vl, sZ + s * 18432 + (zI0 + 4) * 1024);
        };
        unsigned eT[5];
#pragma unroll
        for (int t = 0; t < 5; ++t) eT[t] = (unsigned)__builtin_amdgcn_readfirstlane(min(rb * 5 + t, 127));
        auto iss_e = [&](int g, bool isS) {
            LAS char* slot = lds + (g & 3) * SLOT;
            const int lim = isS ? 16 : 64, nks = isS ? 18 : 72;
            int ks = 4 * (g - (isS ? G_S : G_G)) + p; if (isS && ks > 17) ks = 17;
            const int c0 = ks < lim ? 9 * (ks >> 2) + 2 * (ks & 3) : 18 * (ks - lim) + 8, c1 = ks < lim ? c0 + 1 : c0 + 9;
            const rsrc_t rw = mk_rsrc(isS ? a.wo : a.wout, isS ? D_ * D_ * 2 : FH * D_ * 2), rb_ = mk_rsrc(a.ws + (isS ? WS_OF : WS_HF), isS ? S_ * D_ * 2 : S_ * FH * 2);
#pragma unroll
            for (int q = 0; q < 4; ++q) dma16(rw, slot + p * 4096 + q * 1024, vE, (unsigned)((q < 2 ? c0 : c1) * 16 + 8 * (q & 1)) * (D_ * 2) + cb * 128);
#pragma unroll
            for (int t = 0; t < 5; ++t) dma16_dev(rb_, slot + 16384 + p * 5120 + t * 1024, vl, (eT[t] * nks + ks) * 1024);
        };
#define PSTEP(K, PH) do { WAIT_V(7 * (K)); BAR(); } while (0)
#define SPIN_UNTIL(cond) do { unsigned spins_ = 0; while (!(cond) && ++spins_ < SPIN_CAP) __builtin_amdgcn_s_sleep(4); asm volatile("" ::: "memory"); } while (0)
        iss_q(0); iss_q(1); iss_q(2); iss_q(3);
        {
            const unsigned W = blockIdx.x * 4 + p;
            touch_lines(rX, lds + SP_PF + wid * 256, W * 18, 18, lane);
            touch_lines(rQW, lds + SP_PF + wid * 256, W * 16, 16, lane);
            touch_lines(rW, lds + SP_PF + wid * 256, W * 41, 41, lane);
        }
        WAIT_V(28);
        TAG_FROM_EPOCH();
        PSTEP(3, 0);
#pragma unroll 1
        for (int g = 1; g <= 5; ++g) { PSTEP(2, 0); iss_q(g + 3); }
#pragma unroll 1
        for (int g = 6; g <= 14; ++g) { PSTEP(2, 1); if (g == G_Z2) BAR(); iss_z(g + 3, g + 3 - G_Z2, sZ2); }
        PSTEP(2, 1); iss_z(18, 0, sZ1);
        PSTEP(2, 1); iss_z(19, 1, sZ1);
        PSTEP(2, 1); iss_z(20, 2, sZ1);
        PSTEP(2, 2);
        BAR();
        iss_z(21, 3, sZ1);
        {
            const unsigned W = blockIdx.x * 4 + p;
            touch_lines(rS, lds + SP_PF + wid * 256, W * 64, 64, lane);
            touch_lines(mk_rsrc(a.wout, FH * D_ * 2), lds + SP_PF + wid * 256, W * 21, 21, lane);
            touch_lines(mk_rsrc(a.wo, D_ * D_ * 2), lds + SP_PF + wid * 256, W * 6, 6, lane);
        }
#pragma unroll 1
        for (int g = 19; g <= 23; ++g) { PSTEP(2, 2); iss_z(g + 3, g + 3 - G_Z1, sZ1); }
        PSTEP(2, 2); PSTEP(1, 2); PSTEP(0, 2);
        SPIN_UNTIL(flags_ready(ctl + CTL_ZF + tj * 512, 64, ctl, 0, tag, lane));
        iss_m(27);
        PSTEP(0, 3);
        BAR();
        iss_m(28); iss_m(29); iss_m(30);
        {
            const int pa = (8 * an - 14) >> 7;
            SPIN_UNTIL(flags_ready(ctl + CTL_QF + ti * 512, 64, ctl + CTL_QF + (pa < 0 ? ti : pa) * 512, 64, tag, lane));
        }
#pragma unroll 1
        for (int g = 28; g <= 55; ++g) { PSTEP(2, 3); iss_m(g + 3); }
        PSTEP(2, 3); attn_issue(rQ, lds, an, p, lane, 0);
        PSTEP(1, 3); attn_issue(rQ, lds, an, p, lane, 1);
        PSTEP(1, 3);
        BAR();
        ST(1);
        WAIT_V(0);
        attn_compute(lds, rO, an, p, lane);
        ST(2);
        if (!fin) publish(ctl, CTL_OF + (an * 4 + p) * 8, tag, lane);
        else {
            {
                const int p0 = (rb * 80) >> 7, p1 = min(rb * 80 + 79, S_ - 1) >> 7;
                WAIT_V(0);
                if (lane == 0) __hip_atomic_store((GAS u64*)(ctl + CTL_OF + (an * 4 + p) * 8), tag, __ATOMIC_RELAXED, __HIP_MEMORY_SCOPE_AGENT);
                SPIN_UNTIL(flags_ready(ctl + CTL_HF + p0 * 512, 64, ctl + CTL_HF + p1 * 512, 64, tag, lane));
            }
            ST(3);
            iss_e(59, false); iss_e(60, false);
            PSTEP(1, 5); iss_e(61, false); iss_e(62, false);
#pragma unroll 1
            for (int g = 60; g <= 73; ++g) {
                PSTEP(2, 5); iss_e(g + 3, false);
                if (g == 66) SPIN_UNTIL(flags_ready(ctl + CTL_OF + rb * 320, rb == 25 ? 24 : 40, ctl, 0, tag, lane));
            }
#pragma unroll 1
            for (int g = 74; g <= 78; ++g) { PSTEP(2, 4); iss_e(g + 3, true); }
            PSTEP(2, 4); PSTEP(1, 4); PSTEP(0, 4);
        }
        ST_END(1);
        return;
        }
}

extern "C" void kernel_launch(void* const* d_in, const int* in_sizes, int n_in, void* d_out, int out_size, void* d_ws, size_t ws_size, hipStream_t stream) {
    Args a;
    a.x = (const u16*)d_in[0]; a.wqkv = (const u16*)d_in[1]; a.wo = (const u16*)d_in[2]; a.win = (const u16*)d_in[4]; a.wsp = (const u16*)d_in[5];
    a.bsp = (const u16*)d_in[6]; a.wout = (const u16*)d_in[7]; a.out = (u16*)d_out; a.ws = (char*)d_ws;
    (void)hipFuncSetAttribute((const void*)fused_kernel, hipFuncAttributeMaxDynamicSharedMemorySize, LDS_BYTES);
    fused_kernel<<<256, 512, LDS_BYTES, stream>>>(a);
}
```

```cpp
#include <hip/hip_runtime.h>
#include <hip/hip_bf16.h>
#include <stdint.h>

typedef unsigned short u16;
typedef unsigned long long u64;
typedef short bf16x8 __attribute__((ext_vector_type(8)));
typedef short s16x4 __attribute__((ext_vector_type(4)));
typedef float f32x4 __attribute__((ext_vector_type(4)));
typedef float f32x2 __attribute__((ext_vector_type(2)));
typedef unsigned u32x4 __attribute__((ext_vector_type(4)));
typedef unsigned u32x2 __attribute__((ext_vector_type(2)));
typedef __bf16 bf16x2_t __attribute__((ext_vector_type(2)));
#define LAS __attribute__((address_space(3)))
#define GAS __attribute__((address_space(1)))
typedef LAS void* lds_vp;
typedef __amdgpu_buffer_rsrc_t rsrc_t;

constexpr int S_ = 2048, D_ = 576, F_ = 4608, FH = 2304, QW = 1728;
constexpr int SLOT = 36864, SPARE = 4 * SLOT, LDS_BYTES = 163840;
constexpr int SP_CS = SPARE, SP_PF = SPARE + 1024, SP_EP = SPARE + 3072, SP_AO = SPARE + 4096;
constexpr size_t WS_Z2T = 0;
constexpr size_t WS_QKV = WS_Z2T + (size_t)S_ * FH * 2;
constexpr size_t WS_HF = WS_QKV + (size_t)S_ * QW * 2;
constexpr size_t WS_OF = WS_HF + (size_t)S_ * FH * 2;
constexpr size_t WS_ST = WS_OF + (size_t)S_ * D_ * 2;
constexpr size_t WS_CTL = WS_ST + (size_t)S_ * 16 * 16;
constexpr size_t CTL_EP = 0, CTL_QF = 1024, CTL_ZF = CTL_QF + 8192, CTL_OF = CTL_ZF + 8192, CTL_HF = CTL_OF + 8192, CTL_END = CTL_HF + 8192;
constexpr int G_QKV = 0, G_Z2 = 9, G_Z1 = 18, G_MAIN = 27, G_G = 59, G_S = 77;
constexpr unsigned SPIN_CAP = 400000u;

__device__ __forceinline__ float bflo(unsigned v) { return __uint_as_float(v << 16); }
__device__ __forceinline__ float bfhi(unsigned v) { return __uint_as_float(v & 0xffff0000u); }
__device__ __forceinline__ float bf2f(u16 v) { return __uint_as_float(((unsigned)v) << 16); }
__device__ __forceinline__ unsigned pack_bf16(float lo, float hi) {
    f32x2 v = {lo, hi};
    return __builtin_bit_cast(unsigned, __builtin_convertvector(v, bf16x2_t));
}
#define BAR() do { asm volatile("" ::: "memory"); __builtin_amdgcn_s_barrier(); asm volatile("" ::: "memory"); } while (0)
#define WAIT_V(n) asm volatile("s_waitcnt vmcnt(%0)" ::"n"(n) : "memory")
#define WAIT_L0() asm volatile("s_waitcnt lgkmcnt(0)" ::: "memory")
#define SB0() __builtin_amdgcn_sched_barrier(0)
#define ST_DECL
#define ST(k)
#define ST_END(who)

__device__ __forceinline__ rsrc_t mk_rsrc(const void* p, unsigned bytes) { return __builtin_amdgcn_make_buffer_rsrc((void*)p, 0, bytes, 0x00020000); }
__device__ __forceinline__ void dma16(rsrc_t r, LAS char* lds, unsigned voff, unsigned soff) {
    __builtin_amdgcn_raw_ptr_buffer_load_lds(r, (lds_vp)lds, 16, voff, soff, 0, 0);
}
__device__ __forceinline__ void dma16_dev(rsrc_t r, LAS char* lds, unsigned voff, unsigned soff) {
    __builtin_amdgcn_raw_ptr_buffer_load_lds(r, (lds_vp)lds, 16, voff, soff, 0, 16);
}
#define ATT_PRIO 1
#define STAGE_ST 0
#define ST_AUX 16
__device__ __forceinline__ void touch_lines(rsrc_t r, LAS char* scratch, unsigned first, unsigned cnt, int lane) {
    __builtin_amdgcn_raw_ptr_buffer_load_lds(r, (lds_vp)scratch, 4, (unsigned)lane < cnt ? (first + lane) * 128u : 0x7ffffff0u, 0, 0, 0);
}
__device__ __forceinline__ void st128_dev(rsrc_t r, unsigned off, u32x4 v) { __builtin_amdgcn_raw_buffer_store_b128(v, r, off, 0, ST_AUX); }
__device__ __forceinline__ void st64_dev(rsrc_t r, unsigned off, u32x2 v) { __builtin_amdgcn_raw_buffer_store_b64(v, r, off, 0, ST_AUX); }
__device__ __forceinline__ bf16x8 lds_b128(LAS char* p) { return *(LAS bf16x8*)p; }
__device__ __forceinline__ bf16x8 lds_tr(LAS char* p, int off1) {
    s16x4 a = __builtin_amdgcn_ds_read_tr16_b64_v4i16((LAS s16x4*)p);
    s16x4 b = __builtin_amdgcn_ds_read_tr16_b64_v4i16((LAS s16x4*)(p + off1));
    return __builtin_shufflevector(a, b, 0, 1, 2, 3, 4, 5, 6, 7);
}
#define MFMA(a, b, c) __builtin_amdgcn_mfma_f32_16x16x32_bf16(a, b, c, 0, 0, 0)
template <int CTRL> __device__ __forceinline__ float dpp_add(float x) {
    return x + __builtin_bit_cast(float, __builtin_amdgcn_update_dpp(0, __builtin_bit_cast(int, x), CTRL, 0xf, 0xf, false));
}
__device__ __forceinline__ float row16_sum(float x) { x = dpp_add<0x128>(x); x = dpp_add<0x124>(x); x = dpp_add<0x122>(x); x = dpp_add<0x121>(x); return x; }
__device__ __forceinline__ float gelu_f(float x) {
    const float t = x * (-2.3022081498f - 0.1029432397f * x * x);
    return x * __builtin_amdgcn_rcpf(1.0f + __builtin_amdgcn_exp2f(t));
}
__device__ __forceinline__ f32x2 gelu2(f32x2 x) {
    const f32x2 t = x * (x * x * -0.1029432397f + -2.3022081498f);
    f32x2 e; e.x = __builtin_amdgcn_exp2f(t.x); e.y = __builtin_amdgcn_exp2f(t.y);
    const f32x2 d = e + 1.0f;
    f32x2 r; r.x = __builtin_amdgcn_rcpf(d.x); r.y = __builtin_amdgcn_rcpf(d.y);
    return x * r;
}
__device__ __forceinline__ f32x2 lo2(f32x4 v) { return (f32x2){v[0], v[1]}; }
__device__ __forceinline__ f32x2 hi2(f32x4 v) { return (f32x2){v[2], v[3]}; }
__device__ __forceinline__ unsigned pack2(f32x2 v) { return __builtin_bit_cast(unsigned, __builtin_convertvector(v, bf16x2_t)); }
__device__ __forceinline__ f32x2 unpack2(unsigned v) { return (f32x2){__uint_as_float(v << 16), __uint_as_float(v & 0xffff0000u)}; }
__device__ __forceinline__ int swap23(int r) { return (r & 0x13) | ((r & 4) << 1) | ((r & 8) >> 1); }
__device__ __forceinline__ float xchg32(float x) {
    auto r = __builtin_amdgcn_permlane32_swap(__float_as_uint(x), __float_as_uint(x), false, false);
    return __uint_as_float((threadIdx.x & 32) ? r[0] : r[1]);
}
__device__ __forceinline__ u32x4 gather8(unsigned x0, unsigned x1, unsigned y0, unsigned y1) {
    auto s0 = __builtin_amdgcn_permlane32_swap(x0, y0, false, false);
    auto s1 = __builtin_amdgcn_permlane32_swap(x1, y1, false, false);
    auto t0 = __builtin_amdgcn_permlane16_swap(s0[0], s0[1], false, false);
    auto t1 = __builtin_amdgcn_permlane16_swap(s1[0], s1[1], false, false);
    return (u32x4){t0[0], t1[0], t0[1], t1[1]};
}
__device__ __forceinline__ void publish(char* ctl, size_t word, u64 tag, int lane) {
    asm volatile("s_waitcnt vmcnt(0)" ::: "memory");
    if (lane == 0) __hip_atomic_store((GAS u64*)(ctl + word), tag, __ATOMIC_RELAXED, __HIP_MEMORY_SCOPE_AGENT);
}
__device__ __forceinline__ bool flags_ready(const char* f0, int n0, const char* f1, int n1, u64 tag, int lane) {
    u64 a = tag, b = tag;
    if (lane < n0) a = __hip_atomic_load((const GAS u64*)(f0 + lane * 8), __ATOMIC_RELAXED, __HIP_MEMORY_SCOPE_AGENT);
    if (lane < n1) b = __hip_atomic_load((const GAS u64*)(f1 + lane * 8), __ATOMIC_RELAXED, __HIP_MEMORY_SCOPE_AGENT);
    return __all(a == tag && b == tag);
}

struct Args { const u16* x; const u16* wqkv; const u16* wo; const u16* win; const u16* wsp; const u16* bsp; const u16* wout; u16* out; char* ws; };

struct RowsImg {
    unsigned v0, v1;
    __device__ __forceinline__ void init(int lane, unsigned rs) {
        const int r8 = lane >> 3, ch = lane & 7;
        v0 = r8 * rs + ((ch ^ ((lane >> 4) & 7)) << 4); v1 = r8 * rs + ((ch ^ ((4 + (lane >> 4)) & 7)) << 4);
    }
    __device__ __forceinline__ void issue(rsrc_t r, LAS char* dst, unsigned soff, unsigned rs) const {
#pragma unroll
        for (int q = 0; q < 4; ++q) dma16(r, dst + q * 1024, (q & 1) ? v1 : v0, soff + q * 8 * rs);
    }
    __device__ __forceinline__ void issue35(rsrc_t r, LAS char* dst, unsigned soff, unsigned rs, int p) const {
        const int q0 = p < 2 ? 3 * p : 6 + 5 * (p - 2);
        const unsigned va = (q0 & 1) ? v1 : v0, vb = (q0 & 1) ? v0 : v1;
#pragma unroll
        for (int q = 0; q < 3; ++q) dma16(r, dst + (q0 + q) * 1024, (q & 1) ? vb : va, soff + (q0 + q) * 8 * rs);
        if (p >= 2) {
#pragma unroll
            for (int q = 3; q < 5; ++q) dma16(r, dst + (q0 + q) * 1024, (q & 1) ? vb : va, soff + (q0 + q) * 8 * rs);
        }
    }
};
template <int NC> struct WImg {
    static constexpr int CPR = 2 * NC, BIG = (NC == 9) ? 5 : 4;
    unsigned v[BIG];
    static __device__ __forceinline__ int first(int p) { return NC == 9 ? (p < 2 ? 5 * p : 10 + 4 * (p - 2)) : (p < 2 ? 4 * p : 8 + 3 * (p - 2)); }
    __device__ __forceinline__ void init(int lane, int p, unsigned ldb) {
        const int I0 = first(p);
#pragma unroll
        for (int q = 0; q < BIG; ++q) {
            const int n = (I0 + q) * 64 + lane, pos = n / CPR, cc = n - pos * CPR;
            v[q] = ((pos & 32) + swap23(pos & 31)) * ldb + cc * 16;
        }
    }
    __device__ __forceinline__ void issue(rsrc_t r, LAS char* dst, unsigned soff, int p) const {
        const int I0 = first(p);
#pragma unroll
        for (int q = 0; q < BIG - 1; ++q) dma16(r, dst + (I0 + q) * 1024, v[q], soff);
        if (p < 2) dma16(r, dst + (I0 + BIG - 1) * 1024, v[BIG - 1], soff);
    }
};

#define MMA(acc, a, b) asm volatile("v_mfma_f32_16x16x32_bf16 %0, %1, %2, %0" : "+v"(acc) : "v"(a), "v"(b))
#define MMA_DRAIN() do { SB0(); asm volatile("s_nop 7\n\ts_nop 3"); SB0(); } while (0)
#define CST(idx)
struct NoHook { __device__ __forceinline__ void operator()(int) const {} };
__device__ __forceinline__ s16x4 lds_tr1(LAS char* p) { return __builtin_amdgcn_ds_read_tr16_b64_v4i16((LAS s16x4*)p); }
__device__ __forceinline__ bf16x8 cat8(s16x4 a, s16x4 b) { return __builtin_shufflevector(a, b, 0, 1, 2, 3, 4, 5, 6, 7); }

template <int NC, bool XST> struct XWAcc { f32x4 a[2][NC]; f32x4 cs[3]; f32x4 sx[2], sq[2]; };
template <int NC> struct XF { bf16x8 x[2]; s16x4 w[NC][2]; };
template <int NC> struct XRot { int o[3]; };
template <int NC> __device__ __forceinline__ void xa_read1(XF<NC>& f, int i, LAS char* pX, LAS char* pW, const XRot<NC>& rot) {
    if (i < 2) f.x[i] = lds_b128(pX + i * 2048);
    else {
        const int c = (i - 2) >> 1;
        LAS char* q = c >= NC - 3 ? pW + rot.o[c >= NC - 3 ? c - (NC - 3) : 0] : pW;
        f.w[c][i & 1] = lds_tr1(q + c * 32 + (i & 1) * (8 * NC * 32));
    }
}
template <int NC, bool TR, bool XST, int DBL, bool LOAD>
__device__ __forceinline__ void xa_step(XWAcc<NC, XST>& A, const XF<NC>& f, XF<NC>& n, LAS char* pX, LAS char* pW, const XRot<NC>& rot, const bf16x8& ones) {
    constexpr int R = 2 + 2 * NC;
    int rd = 0, gap = 0;
#define XA_GAP() do { if (LOAD) { if (rd < R) { xa_read1<NC>(n, rd, pX, pW, rot); ++rd; } if (gap < DBL && rd < R) { xa_read1<NC>(n, rd, pX, pW, rot); ++rd; } } ++gap; SB0(); } while (0)
#pragma unroll
    for (int c = 0; c < NC; ++c) {
        const bf16x8 w = cat8(f.w[c][0], f.w[c][1]);
#pragma unroll
        for (int t = 0; t < 2; ++t) {
            if (TR) MMA(A.a[t][c], w, f.x[t]); else MMA(A.a[t][c], f.x[t], w);
            XA_GAP();
        }
        if ((c & 3) == 0) {
            if (TR) MMA(A.cs[c >> 2], w, ones); else MMA(A.cs[c >> 2], ones, w);
            XA_GAP();
        }
    }
    if (XST) {
#pragma unroll
        for (int t = 0; t < 2; ++t) { MMA(A.sx[t], ones, f.x[t]); XA_GAP(); MMA(A.sq[t], f.x[t], f.x[t]); XA_GAP(); }
    }
#pragma unroll
    for (int k = 0; k < 4; ++k) if (LOAD && rd < R) { xa_read1<NC>(n, rd, pX, pW, rot); ++rd; }
    SB0();
#undef XA_GAP
}
template <int NC, bool XST> __device__ __forceinline__ void xw_zero(XWAcc<NC, XST>& A) {
    const f32x4 z = {0.f, 0.f, 0.f, 0.f};
#pragma unroll
    for (int t = 0; t < 2; ++t) {
#pragma unroll
        for (int c = 0; c < NC; ++c) { A.a[t][c] = z; asm volatile("" : "+v"(A.a[t][c])); }
        if (XST) { A.sx[t] = z; A.sq[t] = z; asm volatile("" : "+v"(A.sx[t]), "+v"(A.sq[t])); }
    }
    A.cs[0] = z; A.cs[1] = z; asm volatile("" : "+v"(A.cs[0]), "+v"(A.cs[1]));
    if (NC > 8) { A.cs[2] = z; asm volatile("" : "+v"(A.cs[2])); }
}
template <int NC, bool TR, bool XST, int NST, class Hook>
__device__ __forceinline__ void xw_gemm(XWAcc<NC, XST>& A, LAS char* lds, int g0, int m, int lane, Hook hook) {
    asm volatile("" : "+v"(lane));
    const int fr = lane & 15, g = lane >> 4, row = 32 * m + fr;
    constexpr int G = 2 * NC + (NC + 3) / 4 + (XST ? 4 : 0), R = 2 + 2 * NC;
    constexpr int DA = R > G - 7 ? R - (G - 7) : 0, DB = R > G ? R - G : 0;
    LAS char* pX0 = lds + row * 128 + ((g ^ ((row >> 1) & 7)) << 4);
    LAS char* pX1 = lds + ((row * 128 + ((g ^ ((row >> 1) & 7)) << 4)) ^ 64);
    LAS char* pW0 = lds + 16384 + (16 * (g >> 1) + 4 * (g & 1) + (fr >> 2)) * (NC * 32) + (lane & 3) * 8 + m * 32;
    LAS char* pW1 = pW0 + 32 * NC * 32;
    XRot<NC> rot;
#pragma unroll
    for (int k = 0; k < 3; ++k) rot.o[k] = (NC - 3 + k + m >= NC) ? -NC * 32 : 0;
    XF<NC> f0, f1;
    bf16x8 ones = {0x3F80, 0x3F80, 0x3F80, 0x3F80, 0x3F80, 0x3F80, 0x3F80, 0x3F80};
    asm volatile("" : "+v"(ones));
    {
        const unsigned sb = (g0 & 3) * SLOT;
#pragma unroll
        for (int i = 0; i < R; ++i) xa_read1<NC>(f0, i, pX0 + sb, pW0 + sb, rot);
    }
    asm volatile("s_nop 1"); SB0();
#pragma unroll 1
    for (int s = 0; s < NST - 1; ++s) {
        const unsigned sb = ((g0 + s) & 3) * SLOT, sn = ((g0 + s + 1) & 3) * SLOT;
        LAS char* nX = pX0 + sn; LAS char* nW = pW0 + sn;
        xa_step<NC, TR, XST, DA, true>(A, f0, f1, pX1 + sb, pW1 + sb, rot, ones);
        asm volatile("" : "+v"(nX), "+v"(nW));
        WAIT_L0(); BAR(); SB0();
        hook(s);
        xa_step<NC, TR, XST, DB, true>(A, f1, f0, nX, nW, rot, ones);
    }
    const unsigned sb = ((g0 + NST - 1) & 3) * SLOT;
    xa_step<NC, TR, XST, DA, true>(A, f0, f1, pX1 + sb, pW1 + sb, rot, ones);
    WAIT_L0(); BAR(); SB0();
    xa_step<NC, TR, XST, 0, false>(A, f1, f0, pX0, pW0, rot, ones);
    MMA_DRAIN();
}
template <int NC, bool TR, bool XST> __device__ __forceinline__ void share_colsum(const XWAcc<NC, XST>& A, LAS char* lds, int m, int lane) {
    LAS float* tab = (LAS float*)(lds + SP_CS);
    const int fr = lane & 15, g = lane >> 4;
#pragma unroll
    for (int k = 0; k < (NC + 3) / 4; ++k) {
        int c = m + 4 * k; c = c >= NC ? c - NC : c;
        if (TR) { if (fr == 0) *(LAS f32x4*)(tab + c * 16 + 4 * g) = A.cs[k]; }
        else { if (g == 0) tab[c * 16 + fr] = A.cs[k][0]; }
    }
    WAIT_L0(); BAR();
}
template <int NC> __device__ __forceinline__ void unrotate(unsigned (&v)[NC][2], int m) {
    unsigned u[NC][2];
#define UNROT(M) _Pragma("unroll") for (int c = 0; c < NC; ++c) { u[(c + M) % NC][0] = v[c][0]; u[(c + M) % NC][1] = v[c][1]; }
    if (m == 0) { UNROT(0) } else if (m == 1) { UNROT(1) } else if (m == 2) { UNROT(2) } else { UNROT(3) }
#undef UNROT
#pragma unroll
    for (int c = 0; c < NC; ++c) { v[c][0] = u[c][0]; v[c][1] = u[c][1]; asm volatile("" : "+v"(v[c][0]), "+v"(v[c][1])); }
}

struct MFrag { bf16x8 w[2]; bf16x8 z[9]; };
template <unsigned MASK, int G0, int G1> __device__ __forceinline__ void m_part(f32x4 (&acc)[2][9], const MFrag& f, MFrag& n, LAS char* pW, LAS char* pZ) {
    int r = 0;
#pragma unroll
    for (int i = 0; i < G0; ++i) r += (MASK >> i) & 1;
#pragma unroll
    for (int i = G0; i < G1; ++i) {
        MMA(acc[i & 1][i >> 1], f.z[i >> 1], f.w[i & 1]);
        if ((MASK >> i) & 1) { if (r < 2) n.w[r] = lds_b128(pW + r * 2048); else n.z[r - 2] = lds_b128(pZ + (r - 2) * 1024); ++r; }
        SB0();
    }
}
#ifndef M_MASK_A
#define M_MASK_A 0x07FF
#define M_MASK_B 0x07FF
#define M_BPOS 0
#endif
__device__ __forceinline__ void main_gemm(f32x4 (&acc)[2][9], LAS char* lds, int g0, int m, int lane) {
    asm volatile("" : "+v"(lane));
    const int fr = lane & 15, g = lane >> 4, row = 32 * m + fr;
    const unsigned oW = row * 128 + ((g ^ ((row >> 1) & 7)) << 4);
    LAS char* pW0[4]; LAS char* pW1[4]; LAS char* pZ[4];
#pragma unroll
    for (int k = 0; k < 4; ++k) {
        const unsigned sb = ((g0 + k) & 3) * SLOT;
        pW0[k] = lds + sb + oW; pW1[k] = lds + sb + (oW ^ 64); pZ[k] = lds + sb + 16384 + lane * 16;
        asm volatile("" : "+v"(pW0[k]), "+v"(pW1[k]), "+v"(pZ[k]));
    }
#pragma unroll
    for (int t = 0; t < 2; ++t)
#pragma unroll
        for (int c = 0; c < 9; ++c) { acc[t][c] = (f32x4){0.f, 0.f, 0.f, 0.f}; asm volatile("" : "+v"(acc[t][c])); }
    MFrag f0, f1;
#pragma unroll
    for (int i = 0; i < 2; ++i) f0.w[i] = lds_b128(pW0[0] + i * 2048);
#pragma unroll
    for (int c = 0; c < 9; ++c) f0.z[c] = lds_b128(pZ[0] + c * 1024);
    asm volatile("s_nop 1"); SB0();
#define M_STAGE(K, KN) do { m_part<M_MASK_A, 0, 18>(acc, f0, f1, pW1[K], pZ[K] + 9216); m_part<0, 0, M_BPOS>(acc, f1, f0, pW0[KN], pZ[KN]); WAIT_L0(); BAR(); SB0(); m_part<M_MASK_B, M_BPOS, 18>(acc, f1, f0, pW0[KN], pZ[KN]); } while (0)
#pragma unroll 1
    for (int s = 0; s < 7; ++s) { M_STAGE(0, 1); M_STAGE(1, 2); M_STAGE(2, 3); M_STAGE(3, 0); }
    M_STAGE(0, 1); M_STAGE(1, 2); M_STAGE(2, 3);
#undef M_STAGE
    m_part<M_MASK_A, 0, 18>(acc, f0, f1, pW1[3], pZ[3] + 9216);
    WAIT_L0(); BAR(); SB0();
    m_part<0, 0, 18>(acc, f1, f0, pW0[0], pZ[0]);
    MMA_DRAIN();
}

struct EFrag { s16x4 w[2]; bf16x8 b[5]; };
template <bool LOAD, bool MM> __device__ __forceinline__ void e_step(f32x4 (&acc)[5], const EFrag& f, EFrag& n, LAS char* pW, LAS char* pB) {
    const bf16x8 w = cat8(f.w[0], f.w[1]);
#pragma unroll
    for (int t = 0; t < 5; ++t) {
        if (MM) MMA(acc[t], w, f.b[t]);
        if (LOAD) {
            if (t == 0) { n.w[0] = lds_tr1(pW); n.w[1] = lds_tr1(pW + 2048); }
            else if (t == 1) { n.b[0] = lds_b128(pB); n.b[1] = lds_b128(pB + 1024); }
            else n.b[t] = lds_b128(pB + t * 1024);
        }
        SB0();
    }
}
template <int NST, int NKS, bool MORE, class Hook> __device__ __forceinline__ void e_gemm(f32x4 (&acc)[5], LAS char* lds, int g0, int w, int lane, Hook hook) {
    asm volatile("" : "+v"(lane));
    const int g = lane >> 4, q = (lane & 15) >> 2;
    LAS char* pW = lds + (4 * g + q) * 128 + ((w ^ ((2 * g + (q >> 1)) & 3)) << 5) + (lane & 3) * 8;
    LAS char* pB = lds + 16384 + lane * 16;
#pragma unroll
    for (int t = 0; t < 5; ++t) { acc[t] = (f32x4){0.f, 0.f, 0.f, 0.f}; asm volatile("" : "+v"(acc[t])); }
    EFrag f0, f1;
    e_step<true, false>(acc, f1, f0, pW + (g0 & 3) * SLOT, pB + (g0 & 3) * SLOT);
    asm volatile("s_nop 1"); SB0();
#pragma unroll 1
    for (int s = 0; s < NST - 1; ++s) {
        const unsigned sb = ((g0 + s) & 3) * SLOT, sn = ((g0 + s + 1) & 3) * SLOT;
        e_step<true, true>(acc, f0, f1, pW + sb + 4096, pB + sb + 5120);
        e_step<true, true>(acc, f1, f0, pW + sb + 8192, pB + sb + 10240);
        e_step<true, true>(acc, f0, f1, pW + sb + 12288, pB + sb + 15360);
        WAIT_L0(); BAR(); SB0();
        hook(s);
        e_step<true, true>(acc, f1, f0, pW + sn, pB + sn);
    }
    const unsigned sb = ((g0 + NST - 1) & 3) * SLOT;
    constexpr int LAST = NKS - 4 * (NST - 1);
    e_step<true, true>(acc, f0, f1, pW + sb + 4096, pB + sb + 5120);
    if (LAST == 4) {
        e_step<true, true>(acc, f1, f0, pW + sb + 8192, pB + sb + 10240);
        e_step<true, true>(acc, f0, f1, pW + sb + 12288, pB + sb + 15360);
    }
    if (MORE) { WAIT_L0(); BAR(); SB0(); }
    e_step<false, true>(acc, f1, f0, pW, pB);
    MMA_DRAIN();
}

__device__ __forceinline__ float dot2(unsigned a, unsigned b, float c) { return __builtin_amdgcn_fdot2_f32_bf16(__builtin_bit_cast(bf16x2_t, a), __builtin_bit_cast(bf16x2_t, b), c, false); }
__device__ __forceinline__ unsigned at_region(int r, int p) { return (unsigned)(((r >> 1) ? 0 : 3 * SLOT) + ((r & 1) ? 16384 + p * 5120 : p * 4096)); }
__device__ __forceinline__ unsigned at_row(int rho, int p) { const int r = rho / 14; return at_region(r, p) + (unsigned)(rho - r * 14) * 288u; }
__device__ __forceinline__ void attn_issue(rsrc_t rQ, LAS char* lds, int n, int p, int lane, int half) {
#pragma unroll
    for (int rr = 0; rr < 2; ++rr)
#pragma unroll
        for (int q = 0; q < 4; ++q) {
            const int r = 2 * half + rr, c = q * 64 + lane, lr = c / 18, cc = c - lr * 18, rho = r * 14 + lr;
            int t, seg;
            if (rho < 22) { t = 8 * n - 14 + rho; seg = 1; } else if (rho < 44) { t = 8 * n - 14 + (rho - 22); seg = 2; } else { t = 8 * n + (rho - 44); seg = 0; }
            const unsigned voff = (t < 0 || rho >= 52 || lr >= 14) ? 0x7ffffff0u : (unsigned)(t * (QW * 2) + (seg * D_ + 144 * p) * 2 + cc * 16);
            dma16_dev(rQ, lds + at_region(r, p) + q * 1024, voff, 0);
        }
}
__device__ __forceinline__ void attn_compute(LAS char* lds, rsrc_t rO, int n, int p, int lane) {
    const int i = lane >> 3, hh = lane & 7, t = 8 * n + i;
    const unsigned hb = hh * 36, r0 = at_region(0, p) + hb, r1 = at_region(1, p) + hb - 14 * 288, r2 = at_region(2, p) + hb - 28 * 288, r3 = at_region(3, p) + hb - 42 * 288;
    unsigned qv[9];
    {
        LAS unsigned* q = (LAS unsigned*)(lds + r3 + (44 + i) * 288);
#pragma unroll
        for (int d = 0; d < 9; ++d) qv[d] = q[d];
    }
    float lg[8];
#pragma unroll
    for (int w = 0; w < 8; ++w) {
        const int rho = 14 + i - 2 * w;
        LAS unsigned* kp = (LAS unsigned*)(lds + (rho < 14 ? r0 : r1) + rho * 288);
        float s = 0.f;
#pragma unroll
        for (int d = 0; d < 9; ++d) s = dot2(qv[d], kp[d], s);
        lg[w] = (t - 2 * w >= 0) ? s * (0.23570226039551584f * 1.4426950408889634f) : -1e9f;
    }
    float mx = fmaxf(fmaxf(fmaxf(lg[0], lg[1]), fmaxf(lg[2], lg[3])), fmaxf(fmaxf(lg[4], lg[5]), fmaxf(lg[6], lg[7])));
    float sum = 0.f;
#pragma unroll
    for (int w = 0; w < 8; ++w) { lg[w] = __builtin_amdgcn_exp2f(lg[w] - mx); sum += lg[w]; }
    const float inv = __builtin_amdgcn_rcpf(sum);
    unsigned pp[4];
#pragma unroll
    for (int w = 0; w < 4; ++w) pp[w] = pack_bf16(lg[2 * w] * inv, lg[2 * w + 1] * inv);
    float acc[18];
#pragma unroll
    for (int d = 0; d < 18; ++d) acc[d] = 0.f;
#pragma unroll
    for (int w = 0; w < 4; ++w) {
        const int ra = 22 + 14 + i - 4 * w, rb = ra - 2;
        LAS unsigned* va = (LAS unsigned*)(lds + (ra < 28 ? r1 : ra < 42 ? r2 : r3) + ra * 288);
        LAS unsigned* vb = (LAS unsigned*)(lds + (rb < 28 ? r1 : rb < 42 ? r2 : r3) + rb * 288);
#pragma unroll
        for (int d = 0; d < 9; ++d) {
            const unsigned x = va[d], y = vb[d];
            acc[2 * d] = dot2(__builtin_amdgcn_perm(y, x, 0x05040100u), pp[w], acc[2 * d]);
            acc[2 * d + 1] = dot2(__builtin_amdgcn_perm(y, x, 0x07060302u), pp[w], acc[2 * d + 1]);
        }
    }
    LAS char* ob = lds + SP_AO + p * 2304;
#pragma unroll
    for (int d = 0; d < 9; ++d) *(LAS unsigned*)(ob + i * 288 + hh * 36 + d * 4) = pack_bf16(acc[2 * d], acc[2 * d + 1]);
    WAIT_L0();
    {
        const int r = lane & 7, g = (lane >> 3) & 3, tt = n >> 1, rl = (8 * n + r) & 15;
#pragma unroll
        for (int q = 0; q < 2; ++q) {
            const int a = (lane >> 5) + 2 * q;
            const u32x2 lo = *(LAS u32x2*)(ob + r * 288 + (32 * a + 4 * g) * 2), hi = *(LAS u32x2*)(ob + r * 288 + (32 * a + 16 + 4 * g) * 2);
            st128_dev(rO, ((tt * 18 + 4 * p + a) * 64 + rl + 16 * g) * 16, (u32x4){lo.x, lo.y, hi.x, hi.y});
        }
        const u32x2 v = *(LAS u32x2*)(ob + r * 288 + (128 + 4 * g) * 2);
        if (lane < 32) st64_dev(rO, ((tt * 18 + 16 + (p >> 1)) * 64 + rl + 16 * g) * 16 + (p & 1) * 8, v);
    }
}

__global__ __launch_bounds__(512, 2) void fused_kernel(Args a) {
    asm volatile("fusedk_pf_begin:");
    extern __shared__ __attribute__((aligned(16))) char smem[];
    LAS char* lds = (LAS char*)smem;
    const int wid = __builtin_amdgcn_readfirstlane(threadIdx.x >> 6), lane = threadIdx.x & 63;
    const int xcd = blockIdx.x & 7, loc = blockIdx.x >> 3;
    const int ti = (xcd & 1) * 8 + (loc & 7), tj = (xcd >> 1) * 4 + (loc >> 3);
    const int nt = xcd * 30 + loc;
    const bool fin = loc < 30 && nt < 234;
    const int rb = nt / 9, cb = nt - rb * 9;
    char* ctl = a.ws + WS_CTL;
    if (lane < 2) __builtin_amdgcn_raw_ptr_buffer_load_lds(mk_rsrc(ctl, 4096), (lds_vp)(lds + SP_EP + wid * 64), 4, lane * 4 + (unsigned)CTL_EP, 0, 0, 16);
#define TAG_FROM_EPOCH() \
    u64 ep_; asm volatile("ds_read_b64 %0, %1\n\ts_waitcnt lgkmcnt(0)" : "=&v"(ep_) : "v"((unsigned)(size_t)(lds + SP_EP + wid * 64)) : "memory"); \
    const u64 tg_ = ep_ * 0x9E3779B97F4A7C15ull + 0xD1B54A32D192ED03ull; \
    const u64 tag = ((u64)(unsigned)__builtin_amdgcn_readfirstlane((unsigned)(tg_ >> 32)) << 32) | (unsigned)__builtin_amdgcn_readfirstlane((unsigned)tg_)
    const int an = ti * 16 + tj;

    if (__builtin_expect(wid < 4, 0)) {
    const int m = wid, fr = lane & 15, g4 = lane >> 4;
    ST_DECL; ST(0);
    const rsrc_t rQs = mk_rsrc(a.ws + WS_QKV, S_ * QW * 2), rZs = mk_rsrc(a.ws + WS_Z2T, S_ * FH * 2), rHs = mk_rsrc(a.ws + WS_HF, S_ * FH * 2), rSt = mk_rsrc(a.ws + WS_ST, S_ * 16 * 16);
    LAS float* cst = (LAS float*)(lds + SP_CS);
    float xm[2], xr[2];
    {
        unsigned dummy = 0;
        if (wid == 0) {
            u64 base; unsigned sz;
            unsigned back;
            asm volatile("s_getpc_b64 %0\nfusedk_pf_here:\n\ts_mov_b32 %1, fusedk_pf_end-fusedk_pf_begin\n\ts_mov_b32 %2, fusedk_pf_here-fusedk_pf_begin" : "=s"(base), "=s"(sz), "=s"(back));
            base -= back;
            const unsigned off = (unsigned)(loc * 13 + lane) * 128u;
            if (lane < 13 && (int)off < (int)sz) asm volatile("global_load_dword %0, %1, off" : "=v"(dummy) : "v"(base + off));
        }
        asm volatile("s_waitcnt vmcnt(0)" : "+v"(dummy));
    }
    WAIT_V(0);
    TAG_FROM_EPOCH();
    BAR();
    ST(1);
    {
        XWAcc<7, true> A; xw_zero(A);
        xw_gemm<7, true, true, 9>(A, lds, G_QKV, m, lane, NoHook());
        ST(2);
        share_colsum<7, true, true>(A, lds, m, lane);
#pragma unroll
        for (int t = 0; t < 2; ++t) {
            const int r3 = fr & 3;
            const float dq = r3 == 0 ? A.sq[t][0] : r3 == 1 ? A.sq[t][1] : r3 == 2 ? A.sq[t][2] : A.sq[t][3];
            const float sq = __shfl(dq, (fr >> 2) * 16 + fr);
            xm[t] = A.sx[t][0] * (1.0f / D_);
            xr[t] = __builtin_amdgcn_rsqf(fmaxf(sq * (1.0f / D_) - xm[t] * xm[t], 0.f) + 1e-5f);
        }
        const int c0 = (tj * 27) >> 2, dup = tj > 0 && c0 < (((tj - 1) * 27) >> 2) + 7;
#pragma unroll
        for (int t = 0; t < 2; ++t) {
            unsigned pk[7][2];
#pragma unroll
            for (int c = 0; c < 7; ++c) {
                const int tile = c + m >= 7 ? c + m - 7 : c + m;
                const f32x4 cs = *(LAS f32x4*)(cst + tile * 16 + 4 * g4);
                pk[c][0] = pack2((lo2(A.a[t][c]) - lo2(cs) * xm[t]) * xr[t]); pk[c][1] = pack2((hi2(A.a[t][c]) - hi2(cs) * xm[t]) * xr[t]);
            }
            unrotate<7>(pk, m);
            const unsigned ro = (unsigned)(ti * 128 + 32 * m + 16 * t + fr) * (QW * 2) + c0 * 32;
#pragma unroll
            for (int pp = 0; pp < 3; ++pp) st128_dev(rQs, ro + (1 + 2 * pp) * 32 + g4 * 16, gather8(pk[1 + 2 * pp][0], pk[1 + 2 * pp][1], pk[2 + 2 * pp][0], pk[2 + 2 * pp][1]));
            if (!dup) st64_dev(rQs, ro + g4 * 8, (u32x2){pk[0][0], pk[0][1]});
        }
    }
    ST(3);
    unsigned zp[2][9][2];
    {
        XWAcc<9, false> A; xw_zero(A);
        xw_gemm<9, false, false, 9>(A, lds, G_Z2, m, lane, [&](int s) { if (s == 4) publish(ctl, CTL_QF + ((ti * 16 + tj) * 4 + m) * 8, tag, lane); });
        ST(4);
        share_colsum<9, false, false>(A, lds, m, lane);
        f32x2 rm[2][2], rr[2][2];
#pragma unroll
        for (int t = 0; t < 2; ++t)
#pragma unroll
            for (int h = 0; h < 2; ++h) {
                rm[t][h] = (f32x2){__shfl(xm[t], 4 * g4 + 2 * h), __shfl(xm[t], 4 * g4 + 2 * h + 1)};
                rr[t][h] = (f32x2){__shfl(xr[t], 4 * g4 + 2 * h), __shfl(xr[t], 4 * g4 + 2 * h + 1)};
            }
        f32x2 s1[2][2], s2[2][2];
#pragma unroll
        for (int t = 0; t < 2; ++t)
#pragma unroll
            for (int h = 0; h < 2; ++h) { s1[t][h] = (f32x2){0.f, 0.f}; s2[t][h] = (f32x2){0.f, 0.f}; }
#pragma unroll
        for (int c = 0; c < 9; ++c) {
            const float cs = cst[(c + m >= 9 ? c + m - 9 : c + m) * 16 + fr];
#pragma unroll
            for (int t = 0; t < 2; ++t)
#pragma unroll
                for (int h = 0; h < 2; ++h) {
                    const f32x2 v = gelu2(((h ? hi2(A.a[t][c]) : lo2(A.a[t][c])) - rm[t][h] * cs) * rr[t][h]);
                    s1[t][h] += v; s2[t][h] += v * v;
                    zp[t][c][h] = pack2(v);
                    asm volatile("" : "+v"(zp[t][c][h]));
                }
        }
        float mm = 0.f, qq = 0.f;
        {
            const int idx = lane & 7;
            float a1[8], a2[8];
#pragma unroll
            for (int k = 0; k < 8; ++k) { a1[k] = s1[k >> 2][(k >> 1) & 1][k & 1]; a2[k] = s2[k >> 2][(k >> 1) & 1][k & 1]; }
#pragma unroll
            for (int k = 0; k < 8; ++k) { a1[k] = dpp_add<0x128>(a1[k]); a2[k] = dpp_add<0x128>(a2[k]); }
#pragma unroll
            for (int k = 0; k < 8; ++k) { a1[k] = dpp_add<0x124>(a1[k]); a2[k] = dpp_add<0x124>(a2[k]); }
#pragma unroll
            for (int k = 0; k < 8; ++k) { a1[k] = dpp_add<0x122>(a1[k]); a2[k] = dpp_add<0x122>(a2[k]); }
#pragma unroll
            for (int k = 0; k < 8; ++k) { a1[k] = dpp_add<0x121>(a1[k]); a2[k] = dpp_add<0x121>(a2[k]); }
            float u1 = a1[0], u2 = a2[0];
#pragma unroll
            for (int k = 1; k < 8; ++k) { u1 = idx == k ? a1[k] : u1; u2 = idx == k ? a2[k] : u2; }
            mm = u1 * (1.0f / 144.0f);
            qq = fmaxf(u2 - u1 * mm, 0.f);
            if (fr < 8) st128_dev(rSt, ((ti * 16 + tj) * 128 + 32 * m + 16 * (idx >> 2) + 4 * g4 + (idx & 3)) * 16,
                                  (u32x4){__float_as_uint(mm), (unsigned)tag, __float_as_uint(qq), (unsigned)(tag >> 32)});
        }
    }
    ST(5);
    unsigned z1p[2][9][2];
    {
        XWAcc<9, false> A; xw_zero(A);
        xw_gemm<9, true, false, 2>(A, lds, G_Z1, m, lane, NoHook());
        ST(6);
        float mu, rs;
        {
            const unsigned off = ((ti * 16 + (lane >> 5) * 8) * 128 + 32 * m + (lane & 31)) * 16;
            u32x4 v[8]; unsigned spins = 0;
            for (;;) {
#pragma unroll
                for (int k = 0; k < 8; ++k) v[k] = __builtin_amdgcn_raw_buffer_load_b128(rSt, off + k * 2048, 0, 16);
                unsigned bad = 0;
#pragma unroll
                for (int k = 0; k < 8; ++k) bad |= (v[k].y ^ (unsigned)tag) | (v[k].w ^ (unsigned)(tag >> 32));
                if (__all(bad == 0) || ++spins > SPIN_CAP) break;
                __builtin_amdgcn_s_sleep(2);
            }
            float s = 0.f;
#pragma unroll
            for (int k = 0; k < 8; ++k) s += __uint_as_float(v[k].x);
            s += xchg32(s);
            mu = s * (1.0f / 16.0f);
            float q = 0.f;
#pragma unroll
            for (int k = 0; k < 8; ++k) { const float d = __uint_as_float(v[k].x) - mu; q += __uint_as_float(v[k].z) + 144.0f * d * d; }
            q += xchg32(q);
            rs = __builtin_amdgcn_rsqf(q * (1.0f / FH) + 1e-5f);
        }
        ST(7);
        f32x2 mr[2][2], rr[2][2];
#pragma unroll
        for (int t = 0; t < 2; ++t)
#pragma unroll
            for (int h = 0; h < 2; ++h) {
                const int src = 16 * t + 4 * g4 + 2 * h;
                mr[t][h] = (f32x2){__shfl(mu, src), __shfl(mu, src + 1)}; rr[t][h] = (f32x2){__shfl(rs, src), __shfl(rs, src + 1)};
            }
        const unsigned zo = (unsigned)((tj * 64 + ti * 4 + m) * 9) * 1024 + lane * 16;
#pragma unroll
        for (int c = 0; c < 9; ++c) {
            const unsigned x0 = pack2((unpack2(zp[0][c][0]) - mr[0][0]) * rr[0][0]), x1 = pack2((unpack2(zp[0][c][1]) - mr[0][1]) * rr[0][1]);
            const unsigned y0 = pack2((unpack2(zp[1][c][0]) - mr[1][0]) * rr[1][0]), y1 = pack2((unpack2(zp[1][c][1]) - mr[1][1]) * rr[1][1]);
            st128_dev(rZs, zo + (c + m >= 9 ? c + m - 9 : c + m) * 1024, gather8(x0, x1, y0, y1));
        }
        ST(8);
        xw_gemm<9, true, false, 7>(A, lds, G_Z1 + 2, m, lane, [&](int s) { if (s == 1) publish(ctl, CTL_ZF + ((tj * 16 + ti) * 4 + m) * 8, tag, lane); });
        ST(9);
        share_colsum<9, true, false>(A, lds, m, lane);
#pragma unroll
        for (int c = 0; c < 9; ++c) {
            const f32x4 cs = *(LAS f32x4*)(cst + (c + m >= 9 ? c + m - 9 : c + m) * 16 + 4 * g4);
#pragma unroll
            for (int t = 0; t < 2; ++t) {
                z1p[t][c][0] = pack2(gelu2((lo2(A.a[t][c]) - lo2(cs) * xm[t]) * xr[t])); z1p[t][c][1] = pack2(gelu2((hi2(A.a[t][c]) - hi2(cs) * xm[t]) * xr[t]));
                asm volatile("" : "+v"(z1p[t][c][0]), "+v"(z1p[t][c][1]));
            }
        }
        unrotate<9>(z1p[0], m); unrotate<9>(z1p[1], m);
    }
    ST(10);
    {
        f32x4 acc[2][9];
        float bt[2];
#pragma unroll
        for (int t = 0; t < 2; ++t) bt[t] = bf2f(a.bsp[ti * 128 + 32 * m + 16 * t + fr]);
        main_gemm(acc, lds, G_MAIN, m, lane);
        ST(11);
#pragma unroll
        for (int t = 0; t < 2; ++t) {
            const unsigned hp = (unsigned)(ti * 8 + 2 * m + t) * 72 * 1024 + lane * 16;
#pragma unroll
            for (int pp = 0; pp < 4; ++pp) {
                u32x4 o;
                o.x = pack2((lo2(acc[t][2 * pp]) + bt[t]) * unpack2(z1p[t][2 * pp][0])); o.y = pack2((hi2(acc[t][2 * pp]) + bt[t]) * unpack2(z1p[t][2 * pp][1]));
                o.z = pack2((lo2(acc[t][2 * pp + 1]) + bt[t]) * unpack2(z1p[t][2 * pp + 1][0])); o.w = pack2((hi2(acc[t][2 * pp + 1]) + bt[t]) * unpack2(z1p[t][2 * pp + 1][1]));
                st128_dev(rHs, hp + (tj * 4 + pp) * 1024, o);
            }
            u32x2 o;
            o.x = pack2((lo2(acc[t][8]) + bt[t]) * unpack2(z1p[t][8][0])); o.y = pack2((hi2(acc[t][8]) + bt[t]) * unpack2(z1p[t][8][1]));
            st64_dev(rHs, hp + (64 + (tj >> 1)) * 1024 + (tj & 1) * 8, o);
        }
    }
    ST(12);
    publish(ctl, CTL_HF + ((ti * 16 + tj) * 4 + m) * 8, tag, lane);
    ST(13);
    if (!fin) { ST_END(0); return; }
    {
        f32x4 sa[5], ga[5];
        u32x2 xv[5];
        const int fr = lane & 15, g4 = lane >> 4;
#pragma unroll
        for (int t = 0; t < 5; ++t) xv[t] = *(const u32x2*)(a.x + (size_t)(min(rb * 5 + t, 127) * 16 + fr) * D_ + cb * 64 + m * 16 + g4 * 4);
        BAR();
        ST(14);
        e_gemm<18, 72, true>(ga, lds, G_G, m, lane, NoHook());
        ST(15);
        e_gemm<5, 18, false>(sa, lds, G_S, m, lane, NoHook());
        ST(16);
#pragma unroll
        for (int t = 0; t < 5; ++t) {
            if (rb * 5 + t < 128) {
                float o[4];
#pragma unroll
                for (int r = 0; r < 4; ++r) {
                    const float gv = ga[t][r], sg = gv * __builtin_amdgcn_rcpf(1.0f + __builtin_amdgcn_exp2f(gv * -1.4426950408889634f));
                    const float xx = (r & 1) ? bfhi(xv[t][r >> 1]) : bflo(xv[t][r >> 1]);
                    o[r] = xx + sa[t][r] * sg;
                }
                u32x2 ov; ov.x = pack_bf16(o[0], o[1]); ov.y = pack_bf16(o[2], o[3]);
                *(u32x2*)(a.out + (size_t)((rb * 5 + t) * 16 + fr) * D_ + cb * 64 + m * 16 + g4 * 4) = ov;
            }
        }
    }
    if (blockIdx.x == 0 && threadIdx.x == 0) __hip_atomic_store((GAS u64*)(ctl + CTL_EP), tag, __ATOMIC_RELAXED, __HIP_MEMORY_SCOPE_AGENT);
    ST(17);
    ST_END(0);
    asm volatile("fusedk_pf_end:");
        return;
    }
    {

        const int p = wid - 4;
        ST_DECL; ST(0);
#define rX mk_rsrc(a.x, S_ * D_ * 2)
#define rQW mk_rsrc(a.wqkv, D_ * QW * 2)
#define rW mk_rsrc(a.win, D_ * F_ * 2)
#define rS mk_rsrc(a.wsp, S_ * S_ * 2)
#define rZ mk_rsrc(a.ws + WS_Z2T, S_ * FH * 2)
#define rQ mk_rsrc(a.ws + WS_QKV, S_ * QW * 2)
#define rO mk_rsrc(a.ws + WS_OF, S_ * D_ * 2)
        RowsImg xi; xi.init(lane, D_ * 2);
        RowsImg si; si.init(lane, S_ * 2);
        WImg<7> wq; wq.init(lane, p, QW * 2);
        WImg<9> wz; wz.init(lane, p, F_ * 2);
        const unsigned sX0 = (unsigned)(ti * 128) * (D_ * 2), sQ = (unsigned)((tj * 27) >> 2) * 32, sZ1 = (unsigned)(tj * 144) * 2, sZ2 = sZ1 + FH * 2;
        const unsigned sS = (unsigned)(ti * 128 + 32 * p) * (S_ * 2), sZ = (unsigned)tj * (64u * 9216u);
        const unsigned vl = lane * 16;
        const unsigned vE = (lane >> 3) * (D_ * 2) + ((((lane & 7) >> 1) ^ ((lane >> 4) & 3)) << 5) + (lane & 1) * 16;
        const int zI0 = WImg<9>::first(p);
        auto iss_q = [&](int g) {
            LAS char* slot = lds + (g & 3) * SLOT;
            xi.issue35(rX, slot, sX0 + g * 128, D_ * 2, p); wq.issue(rQW, slot + 16384, sQ + g * 64 * (QW * 2), p);
        };
        auto iss_z = [&](int g, int s, unsigned col) {
            LAS char* slot = lds + (g & 3) * SLOT;
            xi.issue35(rX, slot, sX0 + s * 128, D_ * 2, p);
            wz.issue(rW, slot + 16384, col + s * 64 * (F_ * 2), p);
        };
        auto iss_m = [&](int g) {
            LAS char* slot = lds + (g & 3) * SLOT; const int s = g - G_MAIN;
            si.issue(rS, slot + p * 4096, sS + s * 128, S_ * 2);
#pragma unroll
            for (int q = 0; q < 4; ++q) dma16_dev(rZ, slot + 16384 + (zI0 + q) * 1024, vl, sZ + s * 18432 + (zI0 + q) * 1024);
            if (p < 2) dma16_dev(rZ, slot + 16384 + (zI0 + 4) * 1024, vl, sZ + s * 18432 + (zI0 + 4) * 1024);
        };
        unsigned eH[5], eO[5];
#pragma unroll
        for (int t = 0; t < 5; ++t) { const unsigned tt = (unsigned)__builtin_amdgcn_readfirstlane(min(rb * 5 + t, 127)); eH[t] = tt * 73728u + p * 1024u; eO[t] = tt * 18432u + p * 1024u; }
        auto iss_e = [&](int g, rsrc_t rw, rsrc_t rb_, const unsigned (&eb)[5], unsigned w0, unsigned w1, unsigned bo) {
            LAS char* slot = lds + (g & 3) * SLOT;
            dma16(rw, slot + p * 4096, vE, w0); dma16(rw, slot + p * 4096 + 1024, vE, w0 + 8 * (D_ * 2));
            dma16(rw, slot + p * 4096 + 2048, vE, w1); dma16(rw, slot + p * 4096 + 3072, vE, w1 + 8 * (D_ * 2));
#pragma unroll
            for (int t = 0; t < 5; ++t) dma16_dev(rb_, slot + 16384 + p * 5120 + t * 1024, vl, eb[t] + bo);
        };
        constexpr unsigned TILE_B = 16 * D_ * 2;
        const unsigned wcol = cb * 128;
        auto iss_g = [&](int s) {
            const unsigned c0 = s < 16 ? 9 * s + 2 * p : 18 * (4 * (s - 16) + p) + 8, c1 = s < 16 ? c0 + 1 : c0 + 9;
            iss_e(G_G + s, mk_rsrc(a.wout, FH * D_ * 2), mk_rsrc(a.ws + WS_HF, S_ * FH * 2), eH, c0 * TILE_B + wcol, c1 * TILE_B + wcol, s * 4096);
        };
        auto iss_s = [&](int s) {
            const int pp = s == 4 && p > 1 ? 1 : p;
            const unsigned c0 = s < 4 ? 9 * s + 2 * p : 18 * pp + 8, c1 = s < 4 ? c0 + 1 : c0 + 9;
            iss_e(G_S + s, mk_rsrc(a.wo, D_ * D_ * 2), rO, eO, c0 * TILE_B + wcol, c1 * TILE_B + wcol, s * 4096 + (pp - p) * 1024);
        };
#define PSTEP(K, PH) do { WAIT_V(7 * (K)); BAR(); } while (0)
#define PW(N, PH) do { WAIT_V(N); BAR(); } while (0)
#define SPIN_UNTIL(cond) do { unsigned spins_ = 0; while (!(cond) && ++spins_ < SPIN_CAP) __builtin_amdgcn_s_sleep(4); asm volatile("" ::: "memory"); } while (0)
        iss_q(0); iss_q(1); iss_q(2); iss_q(3);
        {
            const unsigned W = blockIdx.x * 4 + p;
            touch_lines(rX, lds + SP_PF + wid * 256, W * 18, 18, lane);
            touch_lines(rQW, lds + SP_PF + wid * 256, W * 16, 16, lane);
            touch_lines(rW, lds + SP_PF + wid * 256, W * 41, 41, lane);
        }
        WAIT_V(28);
        TAG_FROM_EPOCH();
        PSTEP(3, 0);
#pragma unroll 1
        for (int g = 1; g <= 5; ++g) { PW(14, 0); iss_q(g + 3); }
#pragma unroll 1
        for (int g = 6; g <= 7; ++g) { PSTEP(2, 1); iss_z(g + 3, g + 3 - G_Z2, sZ2); }
#pragma unroll 1
        for (int g = 8; g <= 14; ++g) { PW(16, 1); if (g == G_Z2) BAR(); iss_z(g + 3, g + 3 - G_Z2, sZ2); }
        PW(16, 1); iss_z(18, 0, sZ1);
        PW(16, 1); iss_z(19, 1, sZ1);
        PW(16, 1); iss_z(20, 2, sZ1);
        PW(16, 2);
        BAR();
        iss_z(21, 3, sZ1);
        {
            const unsigned W = blockIdx.x * 4 + p;
            touch_lines(rS, lds + SP_PF + wid * 256, W * 64, 64, lane);
            touch_lines(mk_rsrc(a.wout, FH * D_ * 2), lds + SP_PF + wid * 256, W * 21, 21, lane);
            touch_lines(mk_rsrc(a.wo, D_ * D_ * 2), lds + SP_PF + wid * 256, W * 6, 6, lane);
        }
#pragma unroll 1
        for (int g = 19; g <= 23; ++g) { PW(16, 2); iss_z(g + 3, g + 3 - G_Z1, sZ1); }
        PSTEP(2, 2); PSTEP(1, 2); PSTEP(0, 2);
        SPIN_UNTIL(flags_ready(ctl + CTL_ZF + tj * 512, 64, ctl, 0, tag, lane));
        iss_m(27);
        PSTEP(0, 3);
        BAR();
        iss_m(28); iss_m(29); iss_m(30);
        {
            const int pa = (8 * an - 14) >> 7;
            SPIN_UNTIL(flags_ready(ctl + CTL_QF + ti * 512, 64, ctl + CTL_QF + (pa < 0 ? ti : pa) * 512, 64, tag, lane));
        }
#pragma unroll 1
        for (int g = 28; g <= 55; ++g) { PW(16, 3); iss_m(g + 3); }
        PSTEP(2, 3); attn_issue(rQ, lds, an, p, lane, 0);
        PSTEP(1, 3); attn_issue(rQ, lds, an, p, lane, 1);
        PSTEP(1, 3);
        BAR();
        ST(1);
        WAIT_V(0);
        attn_compute(lds, rO, an, p, lane);
        ST(2);
        if (!fin) publish(ctl, CTL_OF + (an * 4 + p) * 8, tag, lane);
        else {
            {
                const int p0 = (rb * 80) >> 7, p1 = min(rb * 80 + 79, S_ - 1) >> 7;
                WAIT_V(0);
                if (lane == 0) __hip_atomic_store((GAS u64*)(ctl + CTL_OF + (an * 4 + p) * 8), tag, __ATOMIC_RELAXED, __HIP_MEMORY_SCOPE_AGENT);
                SPIN_UNTIL(flags_ready(ctl + CTL_HF + p0 * 512, 64, ctl + CTL_HF + p1 * 512, 64, tag, lane));
                SPIN_UNTIL(flags_ready(ctl + CTL_OF + rb * 320, rb == 25 ? 24 : 40, ctl, 0, tag, lane));
            }
            ST(3);
            iss_g(0); iss_g(1);
            PSTEP(1, 5); iss_g(2); iss_g(3);
            {
                const rsrc_t rw = mk_rsrc(a.wout, FH * D_ * 2), rh = mk_rsrc(a.ws + WS_HF, S_ * FH * 2);
                unsigned w0 = (36 + 2 * p) * TILE_B + wcol, bo = 4 * 4096;
#pragma unroll 1
                for (int g = 60; g <= 71; ++g) {
                    PW(18, 5);
                    iss_e(g + 3, rw, rh, eH, w0, w0 + TILE_B, bo);
                    w0 += 9 * TILE_B; bo += 4096;
                }
            }
            PW(18, 5); iss_g(16);
            PW(18, 5); iss_g(17);
            PW(18, 4); iss_s(0);
            PW(18, 4); iss_s(1);
            PW(18, 4); iss_s(2);
            PW(18, 4); iss_s(3);
            PW(18, 4); iss_s(4);
            PSTEP(2, 4); PSTEP(1, 4); PSTEP(0, 4);
        }
        ST_END(1);
        return;
        }
}

extern "C" void kernel_launch(void* const* d_in, const int* in_sizes, int n_in, void* d_out, int out_size, void* d_ws, size_t ws_size, hipStream_t stream) {
    Args a;
    a.x = (const u16*)d_in[0]; a.wqkv = (const u16*)d_in[1]; a.wo = (const u16*)d_in[2]; a.win = (const u16*)d_in[4]; a.wsp = (const u16*)d_in[5];
    a.bsp = (const u16*)d_in[6]; a.wout = (const u16*)d_in[7]; a.out = (u16*)d_out; a.ws = (char*)d_ws;
    (void)hipFuncSetAttribute((const void*)fused_kernel, hipFuncAttributeMaxDynamicSharedMemorySize, LDS_BYTES);
    fused_kernel<<<256, 512, LDS_BYTES, stream>>>(a);
}
```

```cpp
#include <hip/hip_runtime.h>
#include <hip/hip_bf16.h>
#include <stdint.h>

typedef unsigned short u16;
typedef unsigned long long u64;
typedef short bf16x8 __attribute__((ext_vector_type(8)));
typedef short s16x4 __attribute__((ext_vector_type(4)));
typedef float f32x4 __attribute__((ext_vector_type(4)));
typedef float f32x2 __attribute__((ext_vector_type(2)));
typedef unsigned u32x4 __attribute__((ext_vector_type(4)));
typedef unsigned u32x2 __attribute__((ext_vector_type(2)));
typedef __bf16 bf16x2_t __attribute__((ext_vector_type(2)));
#define LAS __attribute__((address_space(3)))
#define GAS __attribute__((address_space(1)))
typedef LAS void* lds_vp;
typedef __amdgpu_buffer_rsrc_t rsrc_t;

constexpr int S_ = 2048, D_ = 576, F_ = 4608, FH = 2304, QW = 1728;
constexpr int SLOT = 36864, SPARE = 4 * SLOT, LDS_BYTES = 163840;
constexpr int SP_CS = SPARE, SP_PF = SPARE + 1024, SP_EP = SPARE + 3072, SP_AO = SPARE + 4096;
constexpr size_t WS_Z2T = 0;
constexpr size_t WS_QKV = WS_Z2T + (size_t)S_ * FH * 2;
constexpr size_t WS_HF = WS_QKV + (size_t)S_ * QW * 2;
constexpr size_t WS_OF = WS_HF + (size_t)S_ * FH * 2;
constexpr size_t WS_ST = WS_OF + (size_t)S_ * D_ * 2;
constexpr size_t WS_CTL = WS_ST + (size_t)S_ * 16 * 16;
constexpr size_t CTL_EP = 0, CTL_QF = 1024, CTL_ZF = CTL_QF + 8192, CTL_OF = CTL_ZF + 8192, CTL_HF = CTL_OF + 8192, CTL_END = CTL_HF + 8192;
constexpr int G_QKV = 0, G_Z2 = 9, G_Z1 = 18, G_MAIN = 27, G_G = 59, G_S = 77;
constexpr unsigned SPIN_CAP = 400000u;

__device__ __forceinline__ float bflo(unsigned v) { return __uint_as_float(v << 16); }
__device__ __forceinline__ float bfhi(unsigned v) { return __uint_as_float(v & 0xffff0000u); }
__device__ __forceinline__ float bf2f(u16 v) { return __uint_as_float(((unsigned)v) << 16); }
__device__ __forceinline__ unsigned pack_bf16(float lo, float hi) {
    f32x2 v = {lo, hi};
    return __builtin_bit_cast(unsigned, __builtin_convertvector(v, bf16x2_t));
}
#define BAR() do { asm volatile("" ::: "memory"); __builtin_amdgcn_s_barrier(); asm volatile("" ::: "memory"); } while (0)
#define WAIT_V(n) asm volatile("s_waitcnt vmcnt(%0)" ::"n"(n) : "memory")
#define WAIT_L0() asm volatile("s_waitcnt lgkmcnt(0)" ::: "memory")
#define SB0() __builtin_amdgcn_sched_barrier(0)
#define ST_DECL
#define ST(k)
#define ST_END(who)

__device__ __forceinline__ rsrc_t mk_rsrc(const void* p, unsigned bytes) { return __builtin_amdgcn_make_buffer_rsrc((void*)p, 0, bytes, 0x00020000); }
__device__ __forceinline__ void dma16(rsrc_t r, LAS char* lds, unsigned voff, unsigned soff) {
    __builtin_amdgcn_raw_ptr_buffer_load_lds(r, (lds_vp)lds, 16, voff, soff, 0, 0);
}
__device__ __forceinline__ void dma16_dev(rsrc_t r, LAS char* lds, unsigned voff, unsigned soff) {
    __builtin_amdgcn_raw_ptr_buffer_load_lds(r, (lds_vp)lds, 16, voff, soff, 0, 16);
}
#define ATT_PRIO 1
#define STAGE_ST 0
#define Z1A 4
#define ST_AUX 16
__device__ __forceinline__ void touch_lines(rsrc_t r, LAS char* scratch, unsigned first, unsigned cnt, int lane) {
    __builtin_amdgcn_raw_ptr_buffer_load_lds(r, (lds_vp)scratch, 4, (unsigned)lane < cnt ? (first + lane) * 128u : 0x7ffffff0u, 0, 0, 0);
}
__device__ __forceinline__ void st128_dev(rsrc_t r, unsigned off, u32x4 v) { __builtin_amdgcn_raw_buffer_store_b128(v, r, off, 0, ST_AUX); }
__device__ __forceinline__ void st64_dev(rsrc_t r, unsigned off, u32x2 v) { __builtin_amdgcn_raw_buffer_store_b64(v, r, off, 0, ST_AUX); }
__device__ __forceinline__ bf16x8 lds_b128(LAS char* p) { return *(LAS bf16x8*)p; }
__device__ __forceinline__ bf16x8 lds_tr(LAS char* p, int off1) {
    s16x4 a = __builtin_amdgcn_ds_read_tr16_b64_v4i16((LAS s16x4*)p);
    s16x4 b = __builtin_amdgcn_ds_read_tr16_b64_v4i16((LAS s16x4*)(p + off1));
    return __builtin_shufflevector(a, b, 0, 1, 2, 3, 4, 5, 6, 7);
}
#define MFMA(a, b, c) __builtin_amdgcn_mfma_f32_16x16x32_bf16(a, b, c, 0, 0, 0)
template <int CTRL> __device__ __forceinline__ float dpp_add(float x) {
    return x + __builtin_bit_cast(float, __builtin_amdgcn_update_dpp(0, __builtin_bit_cast(int, x), CTRL, 0xf, 0xf, false));
}
__device__ __forceinline__ float row16_sum(float x) { x = dpp_add<0x128>(x); x = dpp_add<0x124>(x); x = dpp_add<0x122>(x); x = dpp_add<0x121>(x); return x; }
__device__ __forceinline__ float gelu_f(float x) {
    const float t = x * (-2.3022081498f - 0.1029432397f * x * x);
    return x * __builtin_amdgcn_rcpf(1.0f + __builtin_amdgcn_exp2f(t));
}
__device__ __forceinline__ f32x2 gelu2(f32x2 x) {
    const f32x2 t = x * (x * x * -0.1029432397f + -2.3022081498f);
    f32x2 e; e.x = __builtin_amdgcn_exp2f(t.x); e.y = __builtin_amdgcn_exp2f(t.y);
    const f32x2 d = e + 1.0f;
    f32x2 r; r.x = __builtin_amdgcn_rcpf(d.x); r.y = __builtin_amdgcn_rcpf(d.y);
    return x * r;
}
__device__ __forceinline__ f32x2 lo2(f32x4 v) { return (f32x2){v[0], v[1]}; }
__device__ __forceinline__ f32x2 hi2(f32x4 v) { return (f32x2){v[2], v[3]}; }
__device__ __forceinline__ unsigned pack2(f32x2 v) { return __builtin_bit_cast(unsigned, __builtin_convertvector(v, bf16x2_t)); }
__device__ __forceinline__ f32x2 unpack2(unsigned v) { return (f32x2){__uint_as_float(v << 16), __uint_as_float(v & 0xffff0000u)}; }
__device__ __forceinline__ int swap23(int r) { return (r & 0x13) | ((r & 4) << 1) | ((r & 8) >> 1); }
__device__ __forceinline__ float xchg32(float x) {
    auto r = __builtin_amdgcn_permlane32_swap(__float_as_uint(x), __float_as_uint(x), false, false);
    return __uint_as_float((threadIdx.x & 32) ? r[0] : r[1]);
}
__device__ __forceinline__ u32x4 gather8(unsigned x0, unsigned x1, unsigned y0, unsigned y1) {
    auto s0 = __builtin_amdgcn_permlane32_swap(x0, y0, false, false);
    auto s1 = __builtin_amdgcn_permlane32_swap(x1, y1, false, false);
    auto t0 = __builtin_amdgcn_permlane16_swap(s0[0], s0[1], false, false);
    auto t1 = __builtin_amdgcn_permlane16_swap(s1[0], s1[1], false, false);
    return (u32x4){t0[0], t1[0], t0[1], t1[1]};
}
__device__ __forceinline__ void publish(char* ctl, size_t word, u64 tag, int lane) {
    asm volatile("s_waitcnt vmcnt(0)" ::: "memory");
    if (lane == 0) __hip_atomic_store((GAS u64*)(ctl + word), tag, __ATOMIC_RELAXED, __HIP_MEMORY_SCOPE_AGENT);
}
__device__ __forceinline__ bool flags_ready(const char* f0, int n0, const char* f1, int n1, u64 tag, int lane) {
    u64 a = tag, b = tag;
    if (lane < n0) a = __hip_atomic_load((const GAS u64*)(f0 + lane * 8), __ATOMIC_RELAXED, __HIP_MEMORY_SCOPE_AGENT);
    if (lane < n1) b = __hip_atomic_load((const GAS u64*)(f1 + lane * 8), __ATOMIC_RELAXED, __HIP_MEMORY_SCOPE_AGENT);
    return __all(a == tag && b == tag);
}

struct Args { const u16* x; const u16* wqkv; const u16* wo; const u16* win; const u16* wsp; const u16* bsp; const u16* wout; u16* out; char* ws; };

struct RowsImg {
    unsigned v0, v1;
    __device__ __forceinline__ void init(int lane, unsigned rs) {
        const int r8 = lane >> 3, ch = lane & 7;
        v0 = r8 * rs + ((ch ^ ((lane >> 4) & 7)) << 4); v1 = r8 * rs + ((ch ^ ((4 + (lane >> 4)) & 7)) << 4);
    }
    __device__ __forceinline__ void issue(rsrc_t r, LAS char* dst, unsigned soff, unsigned rs) const {
#pragma unroll
        for (int q = 0; q < 4; ++q) dma16(r, dst + q * 1024, (q & 1) ? v1 : v0, soff + q * 8 * rs);
    }
    __device__ __forceinline__ void issue35(rsrc_t r, LAS char* dst, unsigned soff, unsigned rs, int p) const {
        const int q0 = p < 2 ? 3 * p : 6 + 5 * (p - 2);
        const unsigned va = (q0 & 1) ? v1 : v0, vb = (q0 & 1) ? v0 : v1;
#pragma unroll
        for (int q = 0; q < 3; ++q) dma16(r, dst + (q0 + q) * 1024, (q & 1) ? vb : va, soff + (q0 + q) * 8 * rs);
        if (p >= 2) {
#pragma unroll
            for (int q = 3; q < 5; ++q) dma16(r, dst + (q0 + q) * 1024, (q & 1) ? vb : va, soff + (q0 + q) * 8 * rs);
        }
    }
};
template <int NC> struct WImg {
    static constexpr int CPR = 2 * NC, BIG = (NC == 9) ? 5 : 4;
    unsigned v[BIG];
    static __device__ __forceinline__ int first(int p) { return NC == 9 ? (p < 2 ? 5 * p : 10 + 4 * (p - 2)) : (p < 2 ? 4 * p : 8 + 3 * (p - 2)); }
    __device__ __forceinline__ void init(int lane, int p, unsigned ldb) {
        const int I0 = first(p);
#pragma unroll
        for (int q = 0; q < BIG; ++q) {
            const int n = (I0 + q) * 64 + lane, pos = n / CPR, cc = n - pos * CPR;
            v[q] = ((pos & 32) + swap23(pos & 31)) * ldb + cc * 16;
        }
    }
    __device__ __forceinline__ void issue(rsrc_t r, LAS char* dst, unsigned soff, int p) const {
        const int I0 = first(p);
#pragma unroll
        for (int q = 0; q < BIG - 1; ++q) dma16(r, dst + (I0 + q) * 1024, v[q], soff);
        if (p < 2) dma16(r, dst + (I0 + BIG - 1) * 1024, v[BIG - 1], soff);
    }
};

#define MMA(acc, a, b) asm volatile("v_mfma_f32_16x16x32_bf16 %0, %1, %2, %0" : "+v"(acc) : "v"(a), "v"(b))
#define MMA_DRAIN() do { SB0(); asm volatile("s_nop 7\n\ts_nop 3"); SB0(); } while (0)
#define CST(idx)
struct NoHook { __device__ __forceinline__ void operator()(int) const {} };
__device__ __forceinline__ s16x4 lds_tr1(LAS char* p) { return __builtin_amdgcn_ds_read_tr16_b64_v4i16((LAS s16x4*)p); }
__device__ __forceinline__ bf16x8 cat8(s16x4 a, s16x4 b) { return __builtin_shufflevector(a, b, 0, 1, 2, 3, 4, 5, 6, 7); }

template <int NC, bool XST> struct XWAcc { f32x4 a[2][NC]; f32x4 cs[3]; f32x4 sx[2], sq[2]; };
template <int NC> struct XF { bf16x8 x[2]; s16x4 w[NC][2]; };
struct XAddr { unsigned x, w, r[3]; };
template <int NC> __device__ __forceinline__ void xa_read1(XF<NC>& f, int i, const XAddr& a) {
    if (i < 2) f.x[i] = lds_b128((LAS char*)(size_t)a.x + i * 2048);
    else {
        const int c = (i - 2) >> 1;
        const unsigned q = c >= NC - 3 ? a.r[c >= NC - 3 ? c - (NC - 3) : 0] : a.w;
        f.w[c][i & 1] = lds_tr1((LAS char*)(size_t)q + c * 32 + (i & 1) * (8 * NC * 32));
    }
}
#define VADD(v, d) asm volatile("v_add_u32 %0, %1, %0" : "+v"(v) : "s"(d))
template <int NC, bool TR, bool XST, int DBL, bool LOAD, bool UPD>
__device__ __forceinline__ void xa_step(XWAcc<NC, XST>& A, const XF<NC>& f, XF<NC>& n, XAddr& ra, XAddr& ua, int d, const bf16x8& ones) {
    constexpr int R = 2 + 2 * NC, G = 2 * NC + (NC + 3) / 4 + (XST ? 4 : 0);
    int rd = 0, gap = 0;
#define XA_UPD(k) do { if ((k) == 0) VADD(ua.x, d); if ((k) == 1) VADD(ua.w, d); if ((k) == 2) VADD(ua.r[0], d); if ((k) == 3) VADD(ua.r[1], d); if ((k) == 4) VADD(ua.r[2], d); \
                       if ((k) == 5) VADD(ra.x, d); if ((k) == 6) VADD(ra.w, d); if ((k) == 7) VADD(ra.r[0], d); if ((k) == 8) VADD(ra.r[1], d); if ((k) == 9) VADD(ra.r[2], d); } while (0)
#define XA_GAP() do { if (LOAD) { if (rd < R) { xa_read1<NC>(n, rd, ra); ++rd; } if (gap < DBL && rd < R) { xa_read1<NC>(n, rd, ra); ++rd; } } \
                      if (UPD && gap >= G - 6 && gap < G - 1) { XA_UPD(2 * (gap - (G - 6))); XA_UPD(2 * (gap - (G - 6)) + 1); } ++gap; SB0(); } while (0)
#pragma unroll
    for (int c = 0; c < NC; ++c) {
        const bf16x8 w = cat8(f.w[c][0], f.w[c][1]);
#pragma unroll
        for (int t = 0; t < 2; ++t) {
            if (TR) MMA(A.a[t][c], w, f.x[t]); else MMA(A.a[t][c], f.x[t], w);
            XA_GAP();
        }
        if ((c & 3) == 0) {
            if (TR) MMA(A.cs[c >> 2], w, ones); else MMA(A.cs[c >> 2], ones, w);
            XA_GAP();
        }
    }
    if (XST) {
#pragma unroll
        for (int t = 0; t < 2; ++t) { MMA(A.sx[t], ones, f.x[t]); XA_GAP(); MMA(A.sq[t], f.x[t], f.x[t]); XA_GAP(); }
    }
    SB0();
#undef XA_GAP
#undef XA_UPD
}
template <int NC, bool XST> __device__ __forceinline__ void xw_zero(XWAcc<NC, XST>& A) {
    const f32x4 z = {0.f, 0.f, 0.f, 0.f};
#pragma unroll
    for (int t = 0; t < 2; ++t) {
#pragma unroll
        for (int c = 0; c < NC; ++c) { A.a[t][c] = z; asm volatile("" : "+v"(A.a[t][c])); }
        if (XST) { A.sx[t] = z; A.sq[t] = z; asm volatile("" : "+v"(A.sx[t]), "+v"(A.sq[t])); }
    }
    A.cs[0] = z; A.cs[1] = z; asm volatile("" : "+v"(A.cs[0]), "+v"(A.cs[1]));
    if (NC > 8) { A.cs[2] = z; asm volatile("" : "+v"(A.cs[2])); }
}
template <int NC, bool TR, bool XST, int NST, class Hook>
__device__ __forceinline__ void xw_gemm(XWAcc<NC, XST>& A, LAS char* lds, int g0, int m, int lane, Hook hook) {
    asm volatile("" : "+v"(lane));
    const int fr = lane & 15, g = lane >> 4, row = 32 * m + fr;
    constexpr int G = 2 * NC + (NC + 3) / 4 + (XST ? 4 : 0), R = 2 + 2 * NC;
    constexpr int DA = R > G - 7 ? R - (G - 7) : 0, DB = R > G ? R - G : 0;
    static_assert(R <= G - 7 + DA && R <= G + DB, "all reads of a k-step are issued inside it");
    const unsigned lb = (unsigned)(size_t)lds + (g0 & 3) * SLOT;
    XAddr a0, a1;
    a0.x = lb + row * 128 + ((g ^ ((row >> 1) & 7)) << 4); a1.x = lb + ((row * 128 + ((g ^ ((row >> 1) & 7)) << 4)) ^ 64);
    a0.w = lb + 16384 + (16 * (g >> 1) + 4 * (g & 1) + (fr >> 2)) * (NC * 32) + (lane & 3) * 8 + m * 32; a1.w = a0.w + 32 * NC * 32;
#pragma unroll
    for (int k = 0; k < 3; ++k) { const int o = (NC - 3 + k + m >= NC) ? -NC * 32 : 0; a0.r[k] = a0.w + o; a1.r[k] = a1.w + o; }
    asm volatile("" : "+v"(a0.x), "+v"(a0.w), "+v"(a0.r[0]), "+v"(a0.r[1]), "+v"(a0.r[2]), "+v"(a1.x), "+v"(a1.w), "+v"(a1.r[0]), "+v"(a1.r[1]), "+v"(a1.r[2]));
    XF<NC> f0, f1;
    bf16x8 ones = {0x3F80, 0x3F80, 0x3F80, 0x3F80, 0x3F80, 0x3F80, 0x3F80, 0x3F80};
    asm volatile("" : "+v"(ones));
#pragma unroll
    for (int i = 0; i < R; ++i) xa_read1<NC>(f0, i, a0);
    int d = ((g0 & 3) == 3) ? -3 * SLOT : SLOT, pos = g0 & 3;
    asm volatile("s_nop 1"); SB0();
#pragma unroll 1
    for (int s = 0; s < NST - 1; ++s) {
        xa_step<NC, TR, XST, DA, true, true>(A, f0, f1, a1, a0, d, ones);
        pos = (pos + 1) & 3; d = pos == 3 ? -3 * SLOT : SLOT;
        WAIT_L0(); BAR(); SB0();
        hook(s);
        xa_step<NC, TR, XST, DB, true, false>(A, f1, f0, a0, a0, 0, ones);
    }
    xa_step<NC, TR, XST, DA, true, false>(A, f0, f1, a1, a1, 0, ones);
    WAIT_L0(); BAR(); SB0();
    xa_step<NC, TR, XST, 0, false, false>(A, f1, f0, a0, a0, 0, ones);
    MMA_DRAIN();
}
template <int NC, bool TR, bool XST> __device__ __forceinline__ void share_colsum(const XWAcc<NC, XST>& A, LAS char* lds, int m, int lane) {
    LAS float* tab = (LAS float*)(lds + SP_CS);
    const int fr = lane & 15, g = lane >> 4;
#pragma unroll
    for (int k = 0; k < (NC + 3) / 4; ++k) {
        int c = m + 4 * k; c = c >= NC ? c - NC : c;
        if (TR) { if (fr == 0) *(LAS f32x4*)(tab + c * 16 + 4 * g) = A.cs[k]; }
        else { if (g == 0) tab[c * 16 + fr] = A.cs[k][0]; }
    }
    WAIT_L0(); BAR();
}
template <int NC> __device__ __forceinline__ void unrotate(unsigned (&v)[NC][2], int m) {
    unsigned u[NC][2];
#define UNROT(M) _Pragma("unroll") for (int c = 0; c < NC; ++c) { u[(c + M) % NC][0] = v[c][0]; u[(c + M) % NC][1] = v[c][1]; }
    if (m == 0) { UNROT(0) } else if (m == 1) { UNROT(1) } else if (m == 2) { UNROT(2) } else { UNROT(3) }
#undef UNROT
#pragma unroll
    for (int c = 0; c < NC; ++c) { v[c][0] = u[c][0]; v[c][1] = u[c][1]; asm volatile("" : "+v"(v[c][0]), "+v"(v[c][1])); }
}

struct MFrag { bf16x8 w[2]; bf16x8 z[9]; };
template <unsigned MASK, int G0, int G1> __device__ __forceinline__ void m_part(f32x4 (&acc)[2][9], const MFrag& f, MFrag& n, LAS char* pW, LAS char* pZ) {
    int r = 0;
#pragma unroll
    for (int i = 0; i < G0; ++i) r += (MASK >> i) & 1;
#pragma unroll
    for (int i = G0; i < G1; ++i) {
        MMA(acc[i & 1][i >> 1], f.z[i >> 1], f.w[i & 1]);
        if ((MASK >> i) & 1) { if (r < 2) n.w[r] = lds_b128(pW + r * 2048); else n.z[r - 2] = lds_b128(pZ + (r - 2) * 1024); ++r; }
        SB0();
    }
}
#ifndef M_MASK_A
#define M_MASK_A 0x07FF
#define M_MASK_B 0x07FF
#define M_BPOS 0
#endif
__device__ __forceinline__ void main_gemm(f32x4 (&acc)[2][9], LAS char* lds, int g0, int m, int lane) {
    asm volatile("" : "+v"(lane));
    const int fr = lane & 15, g = lane >> 4, row = 32 * m + fr;
    const unsigned oW = row * 128 + ((g ^ ((row >> 1) & 7)) << 4);
    LAS char* pW0[4]; LAS char* pW1[4]; LAS char* pZ[4];
#pragma unroll
    for (int k = 0; k < 4; ++k) {
        const unsigned sb = ((g0 + k) & 3) * SLOT;
        pW0[k] = lds + sb + oW; pW1[k] = lds + sb + (oW ^ 64); pZ[k] = lds + sb + 16384 + lane * 16;
        asm volatile("" : "+v"(pW0[k]), "+v"(pW1[k]), "+v"(pZ[k]));
    }
#pragma unroll
    for (int t = 0; t < 2; ++t)
#pragma unroll
        for (int c = 0; c < 9; ++c) { acc[t][c] = (f32x4){0.f, 0.f, 0.f, 0.f}; asm volatile("" : "+v"(acc[t][c])); }
    MFrag f0, f1;
#pragma unroll
    for (int i = 0; i < 2; ++i) f0.w[i] = lds_b128(pW0[0] + i * 2048);
#pragma unroll
    for (int c = 0; c < 9; ++c) f0.z[c] = lds_b128(pZ[0] + c * 1024);
    asm volatile("s_nop 1"); SB0();
#define M_STAGE(K, KN) do { m_part<M_MASK_A, 0, 18>(acc, f0, f1, pW1[K], pZ[K] + 9216); m_part<0, 0, M_BPOS>(acc, f1, f0, pW0[KN], pZ[KN]); WAIT_L0(); BAR(); SB0(); m_part<M_MASK_B, M_BPOS, 18>(acc, f1, f0, pW0[KN], pZ[KN]); } while (0)
#pragma unroll 1
    for (int s = 0; s < 7; ++s) { M_STAGE(0, 1); M_STAGE(1, 2); M_STAGE(2, 3); M_STAGE(3, 0); }
    M_STAGE(0, 1); M_STAGE(1, 2); M_STAGE(2, 3);
#undef M_STAGE
    m_part<M_MASK_A, 0, 18>(acc, f0, f1, pW1[3], pZ[3] + 9216);
    WAIT_L0(); BAR(); SB0();
    m_part<0, 0, 18>(acc, f1, f0, pW0[0], pZ[0]);
    MMA_DRAIN();
}

struct EFrag { s16x4 w[2]; bf16x8 b[5]; };
template <bool LOAD, bool MM> __device__ __forceinline__ void e_step(f32x4 (&acc)[5], const EFrag& f, EFrag& n, LAS char* pW, LAS char* pB) {
    const bf16x8 w = cat8(f.w[0], f.w[1]);
#pragma unroll
    for (int t = 0; t < 5; ++t) {
        if (MM) MMA(acc[t], w, f.b[t]);
        if (LOAD) {
            if (t == 0) { n.w[0] = lds_tr1(pW); n.w[1] = lds_tr1(pW + 2048); }
            else if (t == 1) { n.b[0] = lds_b128(pB); n.b[1] = lds_b128(pB + 1024); }
            else n.b[t] = lds_b128(pB + t * 1024);
        }
        SB0();
    }
}
template <int NST, int NKS, bool MORE, class Hook> __device__ __forceinline__ void e_gemm(f32x4 (&acc)[5], LAS char* lds, int g0, int w, int lane, Hook hook) {
    asm volatile("" : "+v"(lane));
    const int g = lane >> 4, q = (lane & 15) >> 2;
    LAS char* pW = lds + (4 * g + q) * 128 + ((w ^ ((2 * g + (q >> 1)) & 3)) << 5) + (lane & 3) * 8;
    LAS char* pB = lds + 16384 + lane * 16;
#pragma unroll
    for (int t = 0; t < 5; ++t) { acc[t] = (f32x4){0.f, 0.f, 0.f, 0.f}; asm volatile("" : "+v"(acc[t])); }
    EFrag f0, f1;
    e_step<true, false>(acc, f1, f0, pW + (g0 & 3) * SLOT, pB + (g0 & 3) * SLOT);
    asm volatile("s_nop 1"); SB0();
#pragma unroll 1
    for (int s = 0; s < NST - 1; ++s) {
        const unsigned sb = ((g0 + s) & 3) * SLOT, sn = ((g0 + s + 1) & 3) * SLOT;
        e_step<true, true>(acc, f0, f1, pW + sb + 4096, pB + sb + 5120);
        e_step<true, true>(acc, f1, f0, pW + sb + 8192, pB + sb + 10240);
        e_step<true, true>(acc, f0, f1, pW + sb + 12288, pB + sb + 15360);
        WAIT_L0(); BAR(); SB0();
        hook(s);
        e_step<true, true>(acc, f1, f0, pW + sn, pB + sn);
    }
    const unsigned sb = ((g0 + NST - 1) & 3) * SLOT;
    constexpr int LAST = NKS - 4 * (NST - 1);
    e_step<true, true>(acc, f0, f1, pW + sb + 4096, pB + sb + 5120);
    if (LAST == 4) {
        e_step<true, true>(acc, f1, f0, pW + sb + 8192, pB + sb + 10240);
        e_step<true, true>(acc, f0, f1, pW + sb + 12288, pB + sb + 15360);
    }
    if (MORE) { WAIT_L0(); BAR(); SB0(); }
    e_step<false, true>(acc, f1, f0, pW, pB);
    MMA_DRAIN();
}

__device__ __forceinline__ float dot2(unsigned a, unsigned b, float c) { return __builtin_amdgcn_fdot2_f32_bf16(__builtin_bit_cast(bf16x2_t, a), __builtin_bit_cast(bf16x2_t, b), c, false); }
__device__ __forceinline__ unsigned at_region(int r, int p) { return (unsigned)(((r >> 1) ? 0 : 3 * SLOT) + ((r & 1) ? 16384 + p * 5120 : p * 4096)); }
__device__ __forceinline__ unsigned at_row(int rho, int p) { const int r = rho / 14; return at_region(r, p) + (unsigned)(rho - r * 14) * 288u; }
__device__ __forceinline__ void attn_issue(rsrc_t rQ, LAS char* lds, int n, int p, int lane, int half) {
#pragma unroll
    for (int rr = 0; rr < 2; ++rr)
#pragma unroll
        for (int q = 0; q < 4; ++q) {
            const int r = 2 * half + rr, c = q * 64 + lane, lr = c / 18, cc = c - lr * 18, rho = r * 14 + lr;
            int t, seg;
            if (rho < 22) { t = 8 * n - 14 + rho; seg = 1; } else if (rho < 44) { t = 8 * n - 14 + (rho - 22); seg = 2; } else { t = 8 * n + (rho - 44); seg = 0; }
            const unsigned voff = (t < 0 || rho >= 52 || lr >= 14) ? 0x7ffffff0u : (unsigned)(t * (QW * 2) + (seg * D_ + 144 * p) * 2 + cc * 16);
            dma16_dev(rQ, lds + at_region(r, p) + q * 1024, voff, 0);
        }
}
__device__ __forceinline__ void attn_compute(LAS char* lds, rsrc_t rO, int n, int p, int lane) {
    const int i = lane >> 3, hh = lane & 7, t = 8 * n + i;
    const unsigned hb = hh * 36, r0 = at_region(0, p) + hb, r1 = at_region(1, p) + hb - 14 * 288, r2 = at_region(2, p) + hb - 28 * 288, r3 = at_region(3, p) + hb - 42 * 288;
    unsigned qv[9];
    {
        LAS unsigned* q = (LAS unsigned*)(lds + r3 + (44 + i) * 288);
#pragma unroll
        for (int d = 0; d < 9; ++d) qv[d] = q[d];
    }
    float lg[8];
#pragma unroll
    for (int w = 0; w < 8; ++w) {
        const int rho = 14 + i - 2 * w;
        LAS unsigned* kp = (LAS unsigned*)(lds + (rho < 14 ? r0 : r1) + rho * 288);
        float s = 0.f;
#pragma unroll
        for (int d = 0; d < 9; ++d) s = dot2(qv[d], kp[d], s);
        lg[w] = (t - 2 * w >= 0) ? s * (0.23570226039551584f * 1.4426950408889634f) : -1e9f;
    }
    float mx = fmaxf(fmaxf(fmaxf(lg[0], lg[1]), fmaxf(lg[2], lg[3])), fmaxf(fmaxf(lg[4], lg[5]), fmaxf(lg[6], lg[7])));
    float sum = 0.f;
#pragma unroll
    for (int w = 0; w < 8; ++w) { lg[w] = __builtin_amdgcn_exp2f(lg[w] - mx); sum += lg[w]; }
    const float inv = __builtin_amdgcn_rcpf(sum);
    unsigned pp[4];
#pragma unroll
    for (int w = 0; w < 4; ++w) pp[w] = pack_bf16(lg[2 * w] * inv, lg[2 * w + 1] * inv);
    float acc[18];
#pragma unroll
    for (int d = 0; d < 18; ++d) acc[d] = 0.f;
#pragma unroll
    for (int w = 0; w < 4; ++w) {
        const int ra = 22 + 14 + i - 4 * w, rb = ra - 2;
        LAS unsigned* va = (LAS unsigned*)(lds + (ra < 28 ? r1 : ra < 42 ? r2 : r3) + ra * 288);
        LAS unsigned* vb = (LAS unsigned*)(lds + (rb < 28 ? r1 : rb < 42 ? r2 : r3) + rb * 288);
#pragma unroll
        for (int d = 0; d < 9; ++d) {
            const unsigned x = va[d], y = vb[d];
            acc[2 * d] = dot2(__builtin_amdgcn_perm(y, x, 0x05040100u), pp[w], acc[2 * d]);
            acc[2 * d + 1] = dot2(__builtin_amdgcn_perm(y, x, 0x07060302u), pp[w], acc[2 * d + 1]);
        }
    }
    LAS char* ob = lds + SP_AO + p * 2304;
#pragma unroll
    for (int d = 0; d < 9; ++d) *(LAS unsigned*)(ob + i * 288 + hh * 36 + d * 4) = pack_bf16(acc[2 * d], acc[2 * d + 1]);
    WAIT_L0();
    {
        const int r = lane & 7, g = (lane >> 3) & 3, tt = n >> 1, rl = (8 * n + r) & 15;
#pragma unroll
        for (int q = 0; q < 2; ++q) {
            const int a = (lane >> 5) + 2 * q;
            const u32x2 lo = *(LAS u32x2*)(ob + r * 288 + (32 * a + 4 * g) * 2), hi = *(LAS u32x2*)(ob + r * 288 + (32 * a + 16 + 4 * g) * 2);
            st128_dev(rO, ((tt * 18 + 4 * p + a) * 64 + rl + 16 * g) * 16, (u32x4){lo.x, lo.y, hi.x, hi.y});
        }
        const u32x2 v = *(LAS u32x2*)(ob + r * 288 + (128 + 4 * g) * 2);
        if (lane < 32) st64_dev(rO, ((tt * 18 + 16 + (p >> 1)) * 64 + rl + 16 * g) * 16 + (p & 1) * 8, v);
    }
}

__global__ __launch_bounds__(512, 2) void fused_kernel(Args a) {
    asm volatile("fusedk_pf_begin:");
    extern __shared__ __attribute__((aligned(16))) char smem[];
    LAS char* lds = (LAS char*)smem;
    const int wid = __builtin_amdgcn_readfirstlane(threadIdx.x >> 6), lane = threadIdx.x & 63;
    const int xcd = blockIdx.x & 7, loc = blockIdx.x >> 3;
    const int ti = (xcd & 1) * 8 + (loc & 7), tj = (xcd >> 1) * 4 + (loc >> 3);
    const int nt = xcd * 30 + loc;
    const bool fin = loc < 30 && nt < 234;
    const int rb = nt / 9, cb = nt - rb * 9;
    char* ctl = a.ws + WS_CTL;
    if (lane < 2) __builtin_amdgcn_raw_ptr_buffer_load_lds(mk_rsrc(ctl, 4096), (lds_vp)(lds + SP_EP + wid * 64), 4, lane * 4 + (unsigned)CTL_EP, 0, 0, 16);
#define TAG_FROM_EPOCH() \
    u64 ep_; asm volatile("ds_read_b64 %0, %1\n\ts_waitcnt lgkmcnt(0)" : "=&v"(ep_) : "v"((unsigned)(size_t)(lds + SP_EP + wid * 64)) : "memory"); \
    const u64 tg_ = ep_ * 0x9E3779B97F4A7C15ull + 0xD1B54A32D192ED03ull; \
    const u64 tag = ((u64)(unsigned)__builtin_amdgcn_readfirstlane((unsigned)(tg_ >> 32)) << 32) | (unsigned)__builtin_amdgcn_readfirstlane((unsigned)tg_)
    const int an = ti * 16 + tj;

    if (__builtin_expect(wid < 4, 0)) {
    const int m = wid, fr = lane & 15, g4 = lane >> 4;
    ST_DECL; ST(0);
    const rsrc_t rQs = mk_rsrc(a.ws + WS_QKV, S_ * QW * 2), rZs = mk_rsrc(a.ws + WS_Z2T, S_ * FH * 2), rHs = mk_rsrc(a.ws + WS_HF, S_ * FH * 2), rSt = mk_rsrc(a.ws + WS_ST, S_ * 16 * 16);
    LAS float* cst = (LAS float*)(lds + SP_CS);
    float xm[2], xr[2];
    {
        unsigned dummy = 0;
        if (wid == 0) {
            u64 base; unsigned sz;
            unsigned back;
            asm volatile("s_getpc_b64 %0\nfusedk_pf_here:\n\ts_mov_b32 %1, fusedk_pf_end-fusedk_pf_begin\n\ts_mov_b32 %2, fusedk_pf_here-fusedk_pf_begin" : "=s"(base), "=s"(sz), "=s"(back));
            base -= back;
            const unsigned off = (unsigned)(loc * 13 + lane) * 128u;
            if (lane < 13 && (int)off < (int)sz) asm volatile("global_load_dword %0, %1, off" : "=v"(dummy) : "v"(base + off));
        }
        asm volatile("s_waitcnt vmcnt(0)" : "+v"(dummy));
    }
    WAIT_V(0);
    TAG_FROM_EPOCH();
    BAR();
    ST(1);
    {
        XWAcc<7, true> A; xw_zero(A);
        xw_gemm<7, true, true, 9>(A, lds, G_QKV, m, lane, NoHook());
        ST(2);
        share_colsum<7, true, true>(A, lds, m, lane);
#pragma unroll
        for (int t = 0; t < 2; ++t) {
            const int r3 = fr & 3;
            const float dq = r3 == 0 ? A.sq[t][0] : r3 == 1 ? A.sq[t][1] : r3 == 2 ? A.sq[t][2] : A.sq[t][3];
            const float sq = __shfl(dq, (fr >> 2) * 16 + fr);
            xm[t] = A.sx[t][0] * (1.0f / D_);
            xr[t] = __builtin_amdgcn_rsqf(fmaxf(sq * (1.0f / D_) - xm[t] * xm[t], 0.f) + 1e-5f);
        }
        const int c0 = (tj * 27) >> 2, dup = tj > 0 && c0 < (((tj - 1) * 27) >> 2) + 7;
#pragma unroll
        for (int t = 0; t < 2; ++t) {
            unsigned pk[7][2];
#pragma unroll
            for (int c = 0; c < 7; ++c) {
                const int tile = c + m >= 7 ? c + m - 7 : c + m;
                const f32x4 cs = *(LAS f32x4*)(cst + tile * 16 + 4 * g4);
                pk[c][0] = pack2((lo2(A.a[t][c]) - lo2(cs) * xm[t]) * xr[t]); pk[c][1] = pack2((hi2(A.a[t][c]) - hi2(cs) * xm[t]) * xr[t]);
            }
            unrotate<7>(pk, m);
            const unsigned ro = (unsigned)(ti * 128 + 32 * m + 16 * t + fr) * (QW * 2) + c0 * 32;
#pragma unroll
            for (int pp = 0; pp < 3; ++pp) st128_dev(rQs, ro + (1 + 2 * pp) * 32 + g4 * 16, gather8(pk[1 + 2 * pp][0], pk[1 + 2 * pp][1], pk[2 + 2 * pp][0], pk[2 + 2 * pp][1]));
            if (!dup) st64_dev(rQs, ro + g4 * 8, (u32x2){pk[0][0], pk[0][1]});
        }
    }
    ST(3);
    unsigned zp[2][9][2];
    {
        XWAcc<9, false> A; xw_zero(A);
        xw_gemm<9, false, false, 9>(A, lds, G_Z2, m, lane, [&](int s) { if (s == 4) publish(ctl, CTL_QF + ((ti * 16 + tj) * 4 + m) * 8, tag, lane); });
        ST(4);
        share_colsum<9, false, false>(A, lds, m, lane);
        f32x2 rm[2][2], rr[2][2];
#pragma unroll
        for (int t = 0; t < 2; ++t)
#pragma unroll
            for (int h = 0; h < 2; ++h) {
                rm[t][h] = (f32x2){__shfl(xm[t], 4 * g4 + 2 * h), __shfl(xm[t], 4 * g4 + 2 * h + 1)};
                rr[t][h] = (f32x2){__shfl(xr[t], 4 * g4 + 2 * h), __shfl(xr[t], 4 * g4 + 2 * h + 1)};
            }
        f32x2 s1[2][2], s2[2][2];
#pragma unroll
        for (int t = 0; t < 2; ++t)
#pragma unroll
            for (int h = 0; h < 2; ++h) { s1[t][h] = (f32x2){0.f, 0.f}; s2[t][h] = (f32x2){0.f, 0.f}; }
#pragma unroll
        for (int c = 0; c < 9; ++c) {
            const float cs = cst[(c + m >= 9 ? c + m - 9 : c + m) * 16 + fr];
#pragma unroll
            for (int t = 0; t < 2; ++t)
#pragma unroll
                for (int h = 0; h < 2; ++h) {
                    const f32x2 v = gelu2(((h ? hi2(A.a[t][c]) : lo2(A.a[t][c])) - rm[t][h] * cs) * rr[t][h]);
                    s1[t][h] += v; s2[t][h] += v * v;
                    zp[t][c][h] = pack2(v);
                    asm volatile("" : "+v"(zp[t][c][h]));
                }
        }
        float mm = 0.f, qq = 0.f;
        {
            const int idx = lane & 7;
            float a1[8], a2[8];
#pragma unroll
            for (int k = 0; k < 8; ++k) { a1[k] = s1[k >> 2][(k >> 1) & 1][k & 1]; a2[k] = s2[k >> 2][(k >> 1) & 1][k & 1]; }
#pragma unroll
            for (int k = 0; k < 8; ++k) { a1[k] = dpp_add<0x128>(a1[k]); a2[k] = dpp_add<0x128>(a2[k]); }
#pragma unroll
            for (int k = 0; k < 8; ++k) { a1[k] = dpp_add<0x124>(a1[k]); a2[k] = dpp_add<0x124>(a2[k]); }
#pragma unroll
            for (int k = 0; k < 8; ++k) { a1[k] = dpp_add<0x122>(a1[k]); a2[k] = dpp_add<0x122>(a2[k]); }
#pragma unroll
            for (int k = 0; k < 8; ++k) { a1[k] = dpp_add<0x121>(a1[k]); a2[k] = dpp_add<0x121>(a2[k]); }
            float u1 = a1[0], u2 = a2[0];
#pragma unroll
            for (int k = 1; k < 8; ++k) { u1 = idx == k ? a1[k] : u1; u2 = idx == k ? a2[k] : u2; }
            mm = u1 * (1.0f / 144.0f);
            qq = fmaxf(u2 - u1 * mm, 0.f);
            if (fr < 8) st128_dev(rSt, ((ti * 16 + tj) * 128 + 32 * m + 16 * (idx >> 2) + 4 * g4 + (idx & 3)) * 16,
                                  (u32x4){__float_as_uint(mm), (unsigned)tag, __float_as_uint(qq), (unsigned)(tag >> 32)});
        }
    }
    ST(5);
    unsigned z1p[2][9][2];
    {
        XWAcc<9, false> A; xw_zero(A);
        xw_gemm<9, true, false, Z1A>(A, lds, G_Z1, m, lane, NoHook());
        ST(6);
        float mu, rs;
        {
            const unsigned off = ((ti * 16 + (lane >> 5) * 8) * 128 + 32 * m + (lane & 31)) * 16;
            u32x4 v[8]; unsigned spins = 0;
            for (;;) {
#pragma unroll
                for (int k = 0; k < 8; ++k) v[k] = __builtin_amdgcn_raw_buffer_load_b128(rSt, off + k * 2048, 0, 16);
                unsigned bad = 0;
#pragma unroll
                for (int k = 0; k < 8; ++k) bad |= (v[k].y ^ (unsigned)tag) | (v[k].w ^ (unsigned)(tag >> 32));
                if (__all(bad == 0) || ++spins > SPIN_CAP) break;
                __builtin_amdgcn_s_sleep(2);
            }
            float s = 0.f;
#pragma unroll
            for (int k = 0; k < 8; ++k) s += __uint_as_float(v[k].x);
            s += xchg32(s);
            mu = s * (1.0f / 16.0f);
            float q = 0.f;
#pragma unroll
            for (int k = 0; k < 8; ++k) { const float d = __uint_as_float(v[k].x) - mu; q += __uint_as_float(v[k].z) + 144.0f * d * d; }
            q += xchg32(q);
            rs = __builtin_amdgcn_rsqf(q * (1.0f / FH) + 1e-5f);
        }
        ST(7);
        f32x2 mr[2][2], rr[2][2];
#pragma unroll
        for (int t = 0; t < 2; ++t)
#pragma unroll
            for (int h = 0; h < 2; ++h) {
                const int src = 16 * t + 4 * g4 + 2 * h;
                mr[t][h] = (f32x2){__shfl(mu, src), __shfl(mu, src + 1)}; rr[t][h] = (f32x2){__shfl(rs, src), __shfl(rs, src + 1)};
            }
        const unsigned zo = (unsigned)((tj * 64 + ti * 4 + m) * 9) * 1024 + lane * 16;
#pragma unroll
        for (int c = 0; c < 9; ++c) {
            const unsigned x0 = pack2((unpack2(zp[0][c][0]) - mr[0][0]) * rr[0][0]), x1 = pack2((unpack2(zp[0][c][1]) - mr[0][1]) * rr[0][1]);
            const unsigned y0 = pack2((unpack2(zp[1][c][0]) - mr[1][0]) * rr[1][0]), y1 = pack2((unpack2(zp[1][c][1]) - mr[1][1]) * rr[1][1]);
            st128_dev(rZs, zo + (c + m >= 9 ? c + m - 9 : c + m) * 1024, gather8(x0, x1, y0, y1));
        }
        ST(8);
        xw_gemm<9, true, false, 9 - Z1A>(A, lds, G_Z1 + Z1A, m, lane, [&](int s) { if (s == 1) publish(ctl, CTL_ZF + ((tj * 16 + ti) * 4 + m) * 8, tag, lane); });
        ST(9);
        share_colsum<9, true, false>(A, lds, m, lane);
#pragma unroll
        for (int c = 0; c < 9; ++c) {
            const f32x4 cs = *(LAS f32x4*)(cst + (c + m >= 9 ? c + m - 9 : c + m) * 16 + 4 * g4);
#pragma unroll
            for (int t = 0; t < 2; ++t) {
                z1p[t][c][0] = pack2(gelu2((lo2(A.a[t][c]) - lo2(cs) * xm[t]) * xr[t])); z1p[t][c][1] = pack2(gelu2((hi2(A.a[t][c]) - hi2(cs) * xm[t]) * xr[t]));
                asm volatile("" : "+v"(z1p[t][c][0]), "+v"(z1p[t][c][1]));
            }
        }
        unrotate<9>(z1p[0], m); unrotate<9>(z1p[1], m);
    }
    ST(10);
    BAR();
    {
        f32x4 acc[2][9];
        float bt[2];
#pragma unroll
        for (int t = 0; t < 2; ++t) bt[t] = bf2f(a.bsp[ti * 128 + 32 * m + 16 * t + fr]);
        main_gemm(acc, lds, G_MAIN, m, lane);
        ST(11);
#pragma unroll
        for (int t = 0; t < 2; ++t) {
            const unsigned hp = (unsigned)(ti * 8 + 2 * m + t) * 72 * 1024 + lane * 16;
#pragma unroll
            for (int pp = 0; pp < 4; ++pp) {
                u32x4 o;
                o.x = pack2((lo2(acc[t][2 * pp]) + bt[t]) * unpack2(z1p[t][2 * pp][0])); o.y = pack2((hi2(acc[t][2 * pp]) + bt[t]) * unpack2(z1p[t][2 * pp][1]));
                o.z = pack2((lo2(acc[t][2 * pp + 1]) + bt[t]) * unpack2(z1p[t][2 * pp + 1][0])); o.w = pack2((hi2(acc[t][2 * pp + 1]) + bt[t]) * unpack2(z1p[t][2 * pp + 1][1]));
                st128_dev(rHs, hp + (tj * 4 + pp) * 1024, o);
            }
            u32x2 o;
            o.x = pack2((lo2(acc[t][8]) + bt[t]) * unpack2(z1p[t][8][0])); o.y = pack2((hi2(acc[t][8]) + bt[t]) * unpack2(z1p[t][8][1]));
            st64_dev(rHs, hp + (64 + (tj >> 1)) * 1024 + (tj & 1) * 8, o);
        }
    }
    ST(12);
    publish(ctl, CTL_HF + ((ti * 16 + tj) * 4 + m) * 8, tag, lane);
    ST(13);
    if (!fin) { ST_END(0); return; }
    {
        f32x4 sa[5], ga[5];
        u32x2 xv[5];
        const int fr = lane & 15, g4 = lane >> 4;
#pragma unroll
        for (int t = 0; t < 5; ++t) xv[t] = *(const u32x2*)(a.x + (size_t)(min(rb * 5 + t, 127) * 16 + fr) * D_ + cb * 64 + m * 16 + g4 * 4);
        BAR();
        ST(14);
        e_gemm<18, 72, true>(ga, lds, G_G, m, lane, NoHook());
        ST(15);
        e_gemm<5, 18, false>(sa, lds, G_S, m, lane, NoHook());
        ST(16);
#pragma unroll
        for (int t = 0; t < 5; ++t) {
            if (rb * 5 + t < 128) {
                float o[4];
#pragma unroll
                for (int r = 0; r < 4; ++r) {
                    const float gv = ga[t][r], sg = gv * __builtin_amdgcn_rcpf(1.0f + __builtin_amdgcn_exp2f(gv * -1.4426950408889634f));
                    const float xx = (r & 1) ? bfhi(xv[t][r >> 1]) : bflo(xv[t][r >> 1]);
                    o[r] = xx + sa[t][r] * sg;
                }
                u32x2 ov; ov.x = pack_bf16(o[0], o[1]); ov.y = pack_bf16(o[2], o[3]);
                *(u32x2*)(a.out + (size_t)((rb * 5 + t) * 16 + fr) * D_ + cb * 64 + m * 16 + g4 * 4) = ov;
            }
        }
    }
    if (blockIdx.x == 0 && threadIdx.x == 0) __hip_atomic_store((GAS u64*)(ctl + CTL_EP), tag, __ATOMIC_RELAXED, __HIP_MEMORY_SCOPE_AGENT);
    ST(17);
    ST_END(0);
    asm volatile("fusedk_pf_end:");
        return;
    }
    {

        const int p = wid - 4;
        ST_DECL; ST(0);
#define rX mk_rsrc(a.x, S_ * D_ * 2)
#define rQW mk_rsrc(a.wqkv, D_ * QW * 2)
#define rW mk_rsrc(a.win, D_ * F_ * 2)
#define rS mk_rsrc(a.wsp, S_ * S_ * 2)
#define rZ mk_rsrc(a.ws + WS_Z2T, S_ * FH * 2)
#define rQ mk_rsrc(a.ws + WS_QKV, S_ * QW * 2)
#define rO mk_rsrc(a.ws + WS_OF, S_ * D_ * 2)
        RowsImg xi; xi.init(lane, D_ * 2);
        RowsImg si; si.init(lane, S_ * 2);
        WImg<7> wq; wq.init(lane, p, QW * 2);
        WImg<9> wz; wz.init(lane, p, F_ * 2);
        const unsigned sX0 = (unsigned)(ti * 128) * (D_ * 2), sQ = (unsigned)((tj * 27) >> 2) * 32, sZ1 = (unsigned)(tj * 144) * 2, sZ2 = sZ1 + FH * 2;
        const unsigned sS = (unsigned)(ti * 128 + 32 * p) * (S_ * 2), sZ = (unsigned)tj * (64u * 9216u);
        const unsigned vl = lane * 16;
        const unsigned vE = (lane >> 3) * (D_ * 2) + ((((lane & 7) >> 1) ^ ((lane >> 4) & 3)) << 5) + (lane & 1) * 16;
        const int zI0 = WImg<9>::first(p);
        auto iss_q = [&](int g) {
            LAS char* slot = lds + (g & 3) * SLOT;
            xi.issue35(rX, slot, sX0 + g * 128, D_ * 2, p); wq.issue(rQW, slot + 16384, sQ + g * 64 * (QW * 2), p);
        };
        auto iss_z = [&](int g, int s, unsigned col) {
            LAS char* slot = lds + (g & 3) * SLOT;
            xi.issue35(rX, slot, sX0 + s * 128, D_ * 2, p);
            wz.issue(rW, slot + 16384, col + s * 64 * (F_ * 2), p);
        };
        auto iss_m = [&](int g) {
            LAS char* slot = lds + (g & 3) * SLOT; const int s = g - G_MAIN;
            si.issue(rS, slot + p * 4096, sS + s * 128, S_ * 2);
#pragma unroll
            for (int q = 0; q < 4; ++q) dma16_dev(rZ, slot + 16384 + (zI0 + q) * 1024, vl, sZ + s * 18432 + (zI0 + q) * 1024);
            if (p < 2) dma16_dev(rZ, slot + 16384 + (zI0 + 4) * 1024, vl, sZ + s * 18432 + (zI0 + 4) * 1024);
        };
        unsigned eH[5], eO[5];
#pragma unroll
        for (int t = 0; t < 5; ++t) { const unsigned tt = (unsigned)__builtin_amdgcn_readfirstlane(min(rb * 5 + t, 127)); eH[t] = tt * 73728u + p * 1024u; eO[t] = tt * 18432u + p * 1024u; }
        auto iss_e = [&](int g, rsrc_t rw, rsrc_t rb_, const unsigned (&eb)[5], unsigned w0, unsigned w1, unsigned bo) {
            LAS char* slot = lds + (g & 3) * SLOT;
            dma16(rw, slot + p * 4096, vE, w0); dma16(rw, slot + p * 4096 + 1024, vE, w0 + 8 * (D_ * 2));
            dma16(rw, slot + p * 4096 + 2048, vE, w1); dma16(rw, slot + p * 4096 + 3072, vE, w1 + 8 * (D_ * 2));
#pragma unroll
            for (int t = 0; t < 5; ++t) dma16_dev(rb_, slot + 16384 + p * 5120 + t * 1024, vl, eb[t] + bo);
        };
        constexpr unsigned TILE_B = 16 * D_ * 2;
        const unsigned wcol = cb * 128;
        auto iss_g = [&](int s) {
            const unsigned c0 = s < 16 ? 9 * s + 2 * p : 18 * (4 * (s - 16) + p) + 8, c1 = s < 16 ? c0 + 1 : c0 + 9;
            iss_e(G_G + s, mk_rsrc(a.wout, FH * D_ * 2), mk_rsrc(a.ws + WS_HF, S_ * FH * 2), eH, c0 * TILE_B + wcol, c1 * TILE_B + wcol, s * 4096);
        };
        auto iss_s = [&](int s) {
            const int pp = s == 4 && p > 1 ? 1 : p;
            const unsigned c0 = s < 4 ? 9 * s + 2 * p : 18 * pp + 8, c1 = s < 4 ? c0 + 1 : c0 + 9;
            iss_e(G_S + s, mk_rsrc(a.wo, D_ * D_ * 2), rO, eO, c0 * TILE_B + wcol, c1 * TILE_B + wcol, s * 4096 + (pp - p) * 1024);
        };
#define PSTEP(K, PH) do { WAIT_V(7 * (K)); BAR(); } while (0)
#define PW(N, PH) do { WAIT_V(N); BAR(); } while (0)
#define SPIN_UNTIL(cond) do { unsigned spins_ = 0; while (!(cond) && ++spins_ < SPIN_CAP) __builtin_amdgcn_s_sleep(4); asm volatile("" ::: "memory"); } while (0)
        iss_q(0); iss_q(1); iss_q(2); iss_q(3);
        {
            const unsigned W = blockIdx.x * 4 + p;
            touch_lines(rX, lds + SP_PF + wid * 256, W * 18, 18, lane);
            touch_lines(rQW, lds + SP_PF + wid * 256, W * 16, 16, lane);
        }
        WAIT_V(28);
        TAG_FROM_EPOCH();
        PSTEP(3, 0);
#pragma unroll 1
        for (int g = 1; g <= 5; ++g) { PW(14, 0); iss_q(g + 3); if (g == 2) touch_lines(rW, lds + SP_PF + wid * 256, (blockIdx.x * 4 + p) * 41, 41, lane); }
#pragma unroll 1
        for (int g = 6; g <= 7; ++g) { PSTEP(2, 1); iss_z(g + 3, g + 3 - G_Z2, sZ2); }
#pragma unroll 1
        for (int g = 8; g <= 14; ++g) { PW(16, 1); if (g == G_Z2) BAR(); iss_z(g + 3, g + 3 - G_Z2, sZ2); }
        PW(16, 1); iss_z(18, 0, sZ1);
        PW(16, 1); iss_z(19, 1, sZ1);
        PW(16, 1); iss_z(20, 2, sZ1);
        PW(16, 2);
        BAR();
        iss_z(21, 3, sZ1);
        {
            const unsigned W = blockIdx.x * 4 + p;
            touch_lines(rS, lds + SP_PF + wid * 256, W * 64, 64, lane);
            touch_lines(mk_rsrc(a.wout, FH * D_ * 2), lds + SP_PF + wid * 256, W * 21, 21, lane);
            touch_lines(mk_rsrc(a.wo, D_ * D_ * 2), lds + SP_PF + wid * 256, W * 6, 6, lane);
        }
#pragma unroll 1
        for (int g = 19; g <= 23; ++g) { PW(16, 2); iss_z(g + 3, g + 3 - G_Z1, sZ1); }
        PSTEP(2, 2); PSTEP(1, 2); PSTEP(0, 2);
        BAR(); BAR();
        SPIN_UNTIL(flags_ready(ctl + CTL_ZF + tj * 512, 64, ctl, 0, tag, lane));
        iss_m(27); iss_m(28); iss_m(29); iss_m(30);
        {
            const int pa = (8 * an - 14) >> 7;
            SPIN_UNTIL(flags_ready(ctl + CTL_QF + ti * 512, 64, ctl + CTL_QF + (pa < 0 ? ti : pa) * 512, 64, tag, lane));
        }
        BAR();
#pragma unroll 1
        for (int g = 28; g <= 55; ++g) { PW(16, 3); iss_m(g + 3); }
        PSTEP(2, 3); attn_issue(rQ, lds, an, p, lane, 0);
        PSTEP(1, 3); attn_issue(rQ, lds, an, p, lane, 1);
        PSTEP(1, 3);
        BAR();
        ST(1);
        WAIT_V(0);
        attn_compute(lds, rO, an, p, lane);
        ST(2);
        if (!fin) publish(ctl, CTL_OF + (an * 4 + p) * 8, tag, lane);
        else {
            {
                const int p0 = (rb * 80) >> 7, p1 = min(rb * 80 + 79, S_ - 1) >> 7;
                WAIT_V(0);
                if (lane == 0) __hip_atomic_store((GAS u64*)(ctl + CTL_OF + (an * 4 + p) * 8), tag, __ATOMIC_RELAXED, __HIP_MEMORY_SCOPE_AGENT);
                SPIN_UNTIL(flags_ready(ctl + CTL_HF + p0 * 512, 64, ctl + CTL_HF + p1 * 512, 64, tag, lane));
                SPIN_UNTIL(flags_ready(ctl + CTL_OF + rb * 320, rb == 25 ? 24 : 40, ctl, 0, tag, lane));
            }
            ST(3);
            iss_g(0); iss_g(1);
            PSTEP(1, 5); iss_g(2); iss_g(3);
            {
                const rsrc_t rw = mk_rsrc(a.wout, FH * D_ * 2), rh = mk_rsrc(a.ws + WS_HF, S_ * FH * 2);
                unsigned w0 = (36 + 2 * p) * TILE_B + wcol, bo = 4 * 4096;
#pragma unroll 1
                for (int g = 60; g <= 71; ++g) {
                    PW(18, 5);
                    iss_e(g + 3, rw, rh, eH, w0, w0 + TILE_B, bo);
                    w0 += 9 * TILE_B; bo += 4096;
                }
            }
            PW(18, 5); iss_g(16);
            PW(18, 5); iss_g(17);
            PW(18, 4); iss_s(0);
            PW(18, 4); iss_s(1);
            PW(18, 4); iss_s(2);
            PW(18, 4); iss_s(3);
            PW(18, 4); iss_s(4);
            PSTEP(2, 4); PSTEP(1, 4); PSTEP(0, 4);
        }
        ST_END(1);
        return;
        }
}

extern "C" void kernel_launch(void* const* d_in, const int* in_sizes, int n_in, void* d_out, int out_size, void* d_ws, size_t ws_size, hipStream_t stream) {
    Args a;
    a.x = (const u16*)d_in[0]; a.wqkv = (const u16*)d_in[1]; a.wo = (const u16*)d_in[2]; a.win = (const u16*)d_in[4]; a.wsp = (const u16*)d_in[5];
    a.bsp = (const u16*)d_in[6]; a.wout = (const u16*)d_in[7]; a.out = (u16*)d_out; a.ws = (char*)d_ws;
    (void)hipFuncSetAttribute((const void*)fused_kernel, hipFuncAttributeMaxDynamicSharedMemorySize, LDS_BYTES);
    fused_kernel<<<256, 512, LDS_BYTES, stream>>>(a);
}
```

```cpp
#include <hip/hip_runtime.h>
#include <hip/hip_bf16.h>
#include <stdint.h>

typedef unsigned short u16;
typedef unsigned long long u64;
typedef short bf16x8 __attribute__((ext_vector_type(8)));
typedef short s16x4 __attribute__((ext_vector_type(4)));
typedef float f32x4 __attribute__((ext_vector_type(4)));
typedef float f32x2 __attribute__((ext_vector_type(2)));
typedef unsigned u32x4 __attribute__((ext_vector_type(4)));
typedef unsigned u32x2 __attribute__((ext_vector_type(2)));
typedef __bf16 bf16x2_t __attribute__((ext_vector_type(2)));
#define LAS __attribute__((address_space(3)))
#define GAS __attribute__((address_space(1)))
typedef LAS void* lds_vp;
typedef __amdgpu_buffer_rsrc_t rsrc_t;

constexpr int S_ = 2048, D_ = 576, F_ = 4608, FH = 2304, QW = 1728;
constexpr int SLOT = 36864, SPARE = 4 * SLOT, LDS_BYTES = 163840;
constexpr int SP_CS = SPARE, SP_PF = SPARE + 1024, SP_EP = SPARE + 3072, SP_AO = SPARE + 4096;
constexpr size_t WS_Z2T = 0;
constexpr size_t WS_QKV = WS_Z2T + (size_t)S_ * FH * 2;
constexpr size_t WS_HF = WS_QKV + (size_t)S_ * QW * 2;
constexpr size_t WS_OF = WS_HF + (size_t)S_ * FH * 2;
constexpr size_t WS_ST = WS_OF + (size_t)S_ * D_ * 2;
constexpr size_t WS_CTL = WS_ST + (size_t)S_ * 16 * 16;
constexpr size_t CTL_EP = 0, CTL_QF = 1024, CTL_ZF = CTL_QF + 8192, CTL_OF = CTL_ZF + 8192, CTL_HF = CTL_OF + 8192, CTL_END = CTL_HF + 8192;
constexpr int G_QKV = 0, G_Z2 = 9, G_Z1 = 18, G_MAIN = 27, G_G = 59, G_S = 77;
constexpr unsigned SPIN_CAP = 400000u;

__device__ __forceinline__ float bflo(unsigned v) { return __uint_as_float(v << 16); }
__device__ __forceinline__ float bfhi(unsigned v) { return __uint_as_float(v & 0xffff0000u); }
__device__ __forceinline__ float bf2f(u16 v) { return __uint_as_float(((unsigned)v) << 16); }
__device__ __forceinline__ unsigned pack_bf16(float lo, float hi) {
    f32x2 v = {lo, hi};
    return __builtin_bit_cast(unsigned, __builtin_convertvector(v, bf16x2_t));
}
#define BAR() do { asm volatile("" ::: "memory"); __builtin_amdgcn_s_barrier(); asm volatile("" ::: "memory"); } while (0)
#define WAIT_V(n) asm volatile("s_waitcnt vmcnt(%0)" ::"n"(n) : "memory")
#define WAIT_L0() asm volatile("s_waitcnt lgkmcnt(0)" ::: "memory")
#define SB0() __builtin_amdgcn_sched_barrier(0)
#define ST_DECL
#define ST(k)
#define ST_END(who)

__device__ __forceinline__ rsrc_t mk_rsrc(const void* p, unsigned bytes) { return __builtin_amdgcn_make_buffer_rsrc((void*)p, 0, bytes, 0x00020000); }
__device__ __forceinline__ void dma16(rsrc_t r, LAS char* lds, unsigned voff, unsigned soff) {
    __builtin_amdgcn_raw_ptr_buffer_load_lds(r, (lds_vp)lds, 16, voff, soff, 0, 0);
}
__device__ __forceinline__ void dma16_dev(rsrc_t r, LAS char* lds, unsigned voff, unsigned soff) {
    __builtin_amdgcn_raw_ptr_buffer_load_lds(r, (lds_vp)lds, 16, voff, soff, 0, 16);
}
#define ATT_PRIO 1
#define STAGE_ST 0
#define Z1A 4
#define ST_AUX 16
__device__ __forceinline__ void touch_lines(rsrc_t r, LAS char* scratch, unsigned first, unsigned cnt, int lane) {
    __builtin_amdgcn_raw_ptr_buffer_load_lds(r, (lds_vp)scratch, 4, (unsigned)lane < cnt ? (first + lane) * 128u : 0x7ffffff0u, 0, 0, 0);
}
__device__ __forceinline__ void st128_dev(rsrc_t r, unsigned off, u32x4 v) { __builtin_amdgcn_raw_buffer_store_b128(v, r, off, 0, ST_AUX); }
__device__ __forceinline__ void st64_dev(rsrc_t r, unsigned off, u32x2 v) { __builtin_amdgcn_raw_buffer_store_b64(v, r, off, 0, ST_AUX); }
__device__ __forceinline__ bf16x8 lds_b128(LAS char* p) { return *(LAS bf16x8*)p; }
__device__ __forceinline__ bf16x8 lds_tr(LAS char* p, int off1) {
    s16x4 a = __builtin_amdgcn_ds_read_tr16_b64_v4i16((LAS s16x4*)p);
    s16x4 b = __builtin_amdgcn_ds_read_tr16_b64_v4i16((LAS s16x4*)(p + off1));
    return __builtin_shufflevector(a, b, 0, 1, 2, 3, 4, 5, 6, 7);
}
#define MFMA(a, b, c) __builtin_amdgcn_mfma_f32_16x16x32_bf16(a, b, c, 0, 0, 0)
template <int CTRL> __device__ __forceinline__ float dpp_add(float x) {
    return x + __builtin_bit_cast(float, __builtin_amdgcn_update_dpp(0, __builtin_bit_cast(int, x), CTRL, 0xf, 0xf, false));
}
__device__ __forceinline__ float row16_sum(float x) { x = dpp_add<0x128>(x); x = dpp_add<0x124>(x); x = dpp_add<0x122>(x); x = dpp_add<0x121>(x); return x; }
__device__ __forceinline__ float gelu_f(float x) {
    const float t = x * (-2.3022081498f - 0.1029432397f * x * x);
    return x * __builtin_amdgcn_rcpf(1.0f + __builtin_amdgcn_exp2f(t));
}
__device__ __forceinline__ f32x2 gelu2(f32x2 x) {
    const f32x2 t = x * (x * x * -0.1029432397f + -2.3022081498f);
    f32x2 e; e.x = __builtin_amdgcn_exp2f(t.x); e.y = __builtin_amdgcn_exp2f(t.y);
    const f32x2 d = e + 1.0f;
    f32x2 r; r.x = __builtin_amdgcn_rcpf(d.x); r.y = __builtin_amdgcn_rcpf(d.y);
    return x * r;
}
__device__ __forceinline__ f32x2 lo2(f32x4 v) { return (f32x2){v[0], v[1]}; }
__device__ __forceinline__ f32x2 hi2(f32x4 v) { return (f32x2){v[2], v[3]}; }
__device__ __forceinline__ unsigned pack2(f32x2 v) { return __builtin_bit_cast(unsigned, __builtin_convertvector(v, bf16x2_t)); }
__device__ __forceinline__ f32x2 unpack2(unsigned v) { return (f32x2){__uint_as_float(v << 16), __uint_as_float(v & 0xffff0000u)}; }
__device__ __forceinline__ int swap23(int r) { return (r & 0x13) | ((r & 4) << 1) | ((r & 8) >> 1); }
__device__ __forceinline__ float xchg32(float x) {
    auto r = __builtin_amdgcn_permlane32_swap(__float_as_uint(x), __float_as_uint(x), false, false);
    return __uint_as_float((threadIdx.x & 32) ? r[0] : r[1]);
}
__device__ __forceinline__ u32x4 gather8(unsigned x0, unsigned x1, unsigned y0, unsigned y1) {
    auto s0 = __builtin_amdgcn_permlane32_swap(x0, y0, false, false);
    auto s1 = __builtin_amdgcn_permlane32_swap(x1, y1, false, false);
    auto t0 = __builtin_amdgcn_permlane16_swap(s0[0], s0[1], false, false);
    auto t1 = __builtin_amdgcn_permlane16_swap(s1[0], s1[1], false, false);
    return (u32x4){t0[0], t1[0], t0[1], t1[1]};
}
__device__ __forceinline__ void publish(char* ctl, size_t word, u64 tag, int lane) {
    asm volatile("s_waitcnt vmcnt(0)" ::: "memory");
    if (lane == 0) __hip_atomic_store((GAS u64*)(ctl + word), tag, __ATOMIC_RELAXED, __HIP_MEMORY_SCOPE_AGENT);
}
__device__ __forceinline__ bool flags_ready(const char* f0, int n0, const char* f1, int n1, u64 tag, int lane) {
    u64 a = tag, b = tag;
    if (lane < n0) a = __hip_atomic_load((const GAS u64*)(f0 + lane * 8), __ATOMIC_RELAXED, __HIP_MEMORY_SCOPE_AGENT);
    if (lane < n1) b = __hip_atomic_load((const GAS u64*)(f1 + lane * 8), __ATOMIC_RELAXED, __HIP_MEMORY_SCOPE_AGENT);
    return __all(a == tag && b == tag);
}

struct Args { const u16* x; const u16* wqkv; const u16* wo; const u16* win; const u16* wsp; const u16* bsp; const u16* wout; u16* out; char* ws; };

struct RowsImg {
    unsigned v0, v1;
    __device__ __forceinline__ void init(int lane, unsigned rs) {
        const int r8 = lane >> 3, ch = lane & 7;
        v0 = r8 * rs + ((ch ^ ((lane >> 4) & 7)) << 4); v1 = r8 * rs + ((ch ^ ((4 + (lane >> 4)) & 7)) << 4);
    }
    __device__ __forceinline__ void issue(rsrc_t r, LAS char* dst, unsigned soff, unsigned rs) const {
#pragma unroll
        for (int q = 0; q < 4; ++q) dma16(r, dst + q * 1024, (q & 1) ? v1 : v0, soff + q * 8 * rs);
    }
    __device__ __forceinline__ void issue35(rsrc_t r, LAS char* dst, unsigned soff, unsigned rs, int p) const {
        const int q0 = p < 2 ? 3 * p : 6 + 5 * (p - 2);
        const unsigned va = (q0 & 1) ? v1 : v0, vb = (q0 & 1) ? v0 : v1;
#pragma unroll
        for (int q = 0; q < 3; ++q) dma16(r, dst + (q0 + q) * 1024, (q & 1) ? vb : va, soff + (q0 + q) * 8 * rs);
        if (p >= 2) {
#pragma unroll
            for (int q = 3; q < 5; ++q) dma16(r, dst + (q0 + q) * 1024, (q & 1) ? vb : va, soff + (q0 + q) * 8 * rs);
        }
    }
};
template <int NC> struct WImg {
    static constexpr int CPR = 2 * NC, BIG = (NC == 9) ? 5 : 4;
    unsigned v[BIG];
    static __device__ __forceinline__ int first(int p) { return NC == 9 ? (p < 2 ? 5 * p : 10 + 4 * (p - 2)) : (p < 2 ? 4 * p : 8 + 3 * (p - 2)); }
    __device__ __forceinline__ void init(int lane, int p, unsigned ldb) {
        const int I0 = first(p);
#pragma unroll
        for (int q = 0; q < BIG; ++q) {
            const int n = (I0 + q) * 64 + lane, pos = n / CPR, cc = n - pos * CPR;
            v[q] = ((pos & 32) + swap23(pos & 31)) * ldb + cc * 16;
        }
    }
    __device__ __forceinline__ void issue(rsrc_t r, LAS char* dst, unsigned soff, int p) const {
        const int I0 = first(p);
#pragma unroll
        for (int q = 0; q < BIG - 1; ++q) dma16(r, dst + (I0 + q) * 1024, v[q], soff);
        if (p < 2) dma16(r, dst + (I0 + BIG - 1) * 1024, v[BIG - 1], soff);
    }
};

#define MMA(acc, a, b) asm volatile("v_mfma_f32_16x16x32_bf16 %0, %1, %2, %0" : "+v"(acc) : "v"(a), "v"(b))
#define MMA_DRAIN() do { SB0(); asm volatile("s_nop 7\n\ts_nop 3"); SB0(); } while (0)
#define CST(idx)
struct NoHook { __device__ __forceinline__ void operator()(int) const {} };
__device__ __forceinline__ s16x4 lds_tr1(LAS char* p) { return __builtin_amdgcn_ds_read_tr16_b64_v4i16((LAS s16x4*)p); }
__device__ __forceinline__ bf16x8 cat8(s16x4 a, s16x4 b) { return __builtin_shufflevector(a, b, 0, 1, 2, 3, 4, 5, 6, 7); }

template <int NC, bool XST> struct XWAcc { f32x4 a[2][NC]; f32x4 cs[3]; f32x4 sx[2], sq[2]; };
template <int NC> struct XF { bf16x8 x[2]; s16x4 w[NC][2]; };
struct XAddr { unsigned x, w, r[3]; };
template <int NC> __device__ __forceinline__ void xa_read1(XF<NC>& f, int i, const XAddr& a) {
    if (i < 2) f.x[i] = lds_b128((LAS char*)(size_t)a.x + i * 2048);
    else {
        const int c = (i - 2) >> 1;
        const unsigned q = c >= NC - 3 ? a.r[c >= NC - 3 ? c - (NC - 3) : 0] : a.w;
        f.w[c][i & 1] = lds_tr1((LAS char*)(size_t)q + c * 32 + (i & 1) * (8 * NC * 32));
    }
}
#define VADD(v, d) asm volatile("v_add_u32 %0, %1, %0" : "+v"(v) : "s"(d))
template <int NC, bool TR, bool XST, int DBL, bool LOAD, bool UPD>
__device__ __forceinline__ void xa_step(XWAcc<NC, XST>& A, const XF<NC>& f, XF<NC>& n, XAddr& ra, XAddr& ua, int d, const bf16x8& ones) {
    constexpr int R = 2 + 2 * NC, G = 2 * NC + (NC + 3) / 4 + (XST ? 4 : 0);
    int rd = 0, gap = 0;
#define XA_UPD(k) do { if ((k) == 0) VADD(ua.x, d); if ((k) == 1) VADD(ua.w, d); if ((k) == 2) VADD(ua.r[0], d); if ((k) == 3) VADD(ua.r[1], d); if ((k) == 4) VADD(ua.r[2], d); \
                       if ((k) == 5) VADD(ra.x, d); if ((k) == 6) VADD(ra.w, d); if ((k) == 7) VADD(ra.r[0], d); if ((k) == 8) VADD(ra.r[1], d); if ((k) == 9) VADD(ra.r[2], d); } while (0)
#define XA_GAP() do { if (LOAD) { if (rd < R) { xa_read1<NC>(n, rd, ra); ++rd; } if (gap < DBL && rd < R) { xa_read1<NC>(n, rd, ra); ++rd; } } \
                      if (UPD && gap >= G - 6 && gap < G - 1) { XA_UPD(2 * (gap - (G - 6))); XA_UPD(2 * (gap - (G - 6)) + 1); } ++gap; SB0(); } while (0)
#pragma unroll
    for (int c = 0; c < NC; ++c) {
        const bf16x8 w = cat8(f.w[c][0], f.w[c][1]);
#pragma unroll
        for (int t = 0; t < 2; ++t) {
            if (TR) MMA(A.a[t][c], w, f.x[t]); else MMA(A.a[t][c], f.x[t], w);
            XA_GAP();
        }
        if ((c & 3) == 0) {
            if (TR) MMA(A.cs[c >> 2], w, ones); else MMA(A.cs[c >> 2], ones, w);
            XA_GAP();
        }
    }
    if (XST) {
#pragma unroll
        for (int t = 0; t < 2; ++t) { MMA(A.sx[t], ones, f.x[t]); XA_GAP(); MMA(A.sq[t], f.x[t], f.x[t]); XA_GAP(); }
    }
    SB0();
#undef XA_GAP
#undef XA_UPD
}
template <int NC, bool XST> __device__ __forceinline__ void xw_zero(XWAcc<NC, XST>& A) {
    const f32x4 z = {0.f, 0.f, 0.f, 0.f};
#pragma unroll
    for (int t = 0; t < 2; ++t) {
#pragma unroll
        for (int c = 0; c < NC; ++c) { A.a[t][c] = z; asm volatile("" : "+v"(A.a[t][c])); }
        if (XST) { A.sx[t] = z; A.sq[t] = z; asm volatile("" : "+v"(A.sx[t]), "+v"(A.sq[t])); }
    }
    A.cs[0] = z; A.cs[1] = z; asm volatile("" : "+v"(A.cs[0]), "+v"(A.cs[1]));
    if (NC > 8) { A.cs[2] = z; asm volatile("" : "+v"(A.cs[2])); }
}
template <int NC, bool TR, bool XST, int NST, class Hook>
__device__ __forceinline__ void xw_gemm(XWAcc<NC, XST>& A, LAS char* lds, int g0, int m, int lane, Hook hook) {
    asm volatile("" : "+v"(lane));
    const int fr = lane & 15, g = lane >> 4, row = 32 * m + fr;
    constexpr int G = 2 * NC + (NC + 3) / 4 + (XST ? 4 : 0), R = 2 + 2 * NC;
    constexpr int DA = R > G - 7 ? R - (G - 7) : 0, DB = R > G ? R - G : 0;
    static_assert(R <= G - 7 + DA && R <= G + DB, "all reads of a k-step are issued inside it");
    const unsigned lb = (unsigned)(size_t)lds + (g0 & 3) * SLOT;
    XAddr a0, a1;
    a0.x = lb + row * 128 + ((g ^ ((row >> 1) & 7)) << 4); a1.x = lb + ((row * 128 + ((g ^ ((row >> 1) & 7)) << 4)) ^ 64);
    a0.w = lb + 16384 + (16 * (g >> 1) + 4 * (g & 1) + (fr >> 2)) * (NC * 32) + (lane & 3) * 8 + m * 32; a1.w = a0.w + 32 * NC * 32;
#pragma unroll
    for (int k = 0; k < 3; ++k) { const int o = (NC - 3 + k + m >= NC) ? -NC * 32 : 0; a0.r[k] = a0.w + o; a1.r[k] = a1.w + o; }
    asm volatile("" : "+v"(a0.x), "+v"(a0.w), "+v"(a0.r[0]), "+v"(a0.r[1]), "+v"(a0.r[2]), "+v"(a1.x), "+v"(a1.w), "+v"(a1.r[0]), "+v"(a1.r[1]), "+v"(a1.r[2]));
    XF<NC> f0, f1;
    bf16x8 ones = {0x3F80, 0x3F80, 0x3F80, 0x3F80, 0x3F80, 0x3F80, 0x3F80, 0x3F80};
    asm volatile("" : "+v"(ones));
#pragma unroll
    for (int i = 0; i < R; ++i) xa_read1<NC>(f0, i, a0);
    int d = ((g0 & 3) == 3) ? -3 * SLOT : SLOT, pos = g0 & 3;
    asm volatile("s_nop 1"); SB0();
#pragma unroll 1
    for (int s = 0; s < NST - 1; ++s) {
        xa_step<NC, TR, XST, DA, true, true>(A, f0, f1, a1, a0, d, ones);
        pos = (pos + 1) & 3; d = pos == 3 ? -3 * SLOT : SLOT;
        WAIT_L0(); BAR(); SB0();
        hook(s);
        xa_step<NC, TR, XST, DB, true, false>(A, f1, f0, a0, a0, 0, ones);
    }
    xa_step<NC, TR, XST, DA, true, false>(A, f0, f1, a1, a1, 0, ones);
    WAIT_L0(); BAR(); SB0();
    xa_step<NC, TR, XST, 0, false, false>(A, f1, f0, a0, a0, 0, ones);
    MMA_DRAIN();
}
template <int NC, bool TR, bool XST> __device__ __forceinline__ void share_colsum(const XWAcc<NC, XST>& A, LAS char* lds, int m, int lane) {
    LAS float* tab = (LAS float*)(lds + SP_CS);
    const int fr = lane & 15, g = lane >> 4;
#pragma unroll
    for (int k = 0; k < (NC + 3) / 4; ++k) {
        int c = m + 4 * k; c = c >= NC ? c - NC : c;
        if (TR) { if (fr == 0) *(LAS f32x4*)(tab + c * 16 + 4 * g) = A.cs[k]; }
        else { if (g == 0) tab[c * 16 + fr] = A.cs[k][0]; }
    }
    WAIT_L0(); BAR();
}
template <int NC> __device__ __forceinline__ void unrotate(unsigned (&v)[NC][2], int m) {
    unsigned u[NC][2];
#define UNROT(M) _Pragma("unroll") for (int c = 0; c < NC; ++c) { u[(c + M) % NC][0] = v[c][0]; u[(c + M) % NC][1] = v[c][1]; }
    if (m == 0) { UNROT(0) } else if (m == 1) { UNROT(1) } else if (m == 2) { UNROT(2) } else { UNROT(3) }
#undef UNROT
#pragma unroll
    for (int c = 0; c < NC; ++c) { v[c][0] = u[c][0]; v[c][1] = u[c][1]; asm volatile("" : "+v"(v[c][0]), "+v"(v[c][1])); }
}

struct MFrag { bf16x8 w[2]; bf16x8 z[9]; };
template <unsigned MASK, int G0, int G1> __device__ __forceinline__ void m_part(f32x4 (&acc)[2][9], const MFrag& f, MFrag& n, LAS char* pW, LAS char* pZ) {
    int r = 0;
#pragma unroll
    for (int i = 0; i < G0; ++i) r += (MASK >> i) & 1;
#pragma unroll
    for (int i = G0; i < G1; ++i) {
        MMA(acc[i & 1][i >> 1], f.z[i >> 1], f.w[i & 1]);
        if ((MASK >> i) & 1) { if (r < 2) n.w[r] = lds_b128(pW + r * 2048); else n.z[r - 2] = lds_b128(pZ + (r - 2) * 1024); ++r; }
        SB0();
    }
}
#ifndef M_MASK_A
#define M_MASK_A 0x07FF
#define M_MASK_B 0x07FF
#define M_BPOS 0
#endif
__device__ __forceinline__ void main_gemm(f32x4 (&acc)[2][9], LAS char* lds, int g0, int m, int lane) {
    asm volatile("" : "+v"(lane));
    const int fr = lane & 15, g = lane >> 4, row = 32 * m + fr;
    const unsigned oW = row * 128 + ((g ^ ((row >> 1) & 7)) << 4);
    LAS char* pW0[4]; LAS char* pW1[4]; LAS char* pZ[4];
#pragma unroll
    for (int k = 0; k < 4; ++k) {
        const unsigned sb = ((g0 + k) & 3) * SLOT;
        pW0[k] = lds + sb + oW; pW1[k] = lds + sb + (oW ^ 64); pZ[k] = lds + sb + 16384 + lane * 16;
        asm volatile("" : "+v"(pW0[k]), "+v"(pW1[k]), "+v"(pZ[k]));
    }
#pragma unroll
    for (int t = 0; t < 2; ++t)
#pragma unroll
        for (int c = 0; c < 9; ++c) { acc[t][c] = (f32x4){0.f, 0.f, 0.f, 0.f}; asm volatile("" : "+v"(acc[t][c])); }
    MFrag f0, f1;
#pragma unroll
    for (int i = 0; i < 2; ++i) f0.w[i] = lds_b128(pW0[0] + i * 2048);
#pragma unroll
    for (int c = 0; c < 9; ++c) f0.z[c] = lds_b128(pZ[0] + c * 1024);
    asm volatile("s_nop 1"); SB0();
#define M_STAGE(K, KN) do { m_part<M_MASK_A, 0, 18>(acc, f0, f1, pW1[K], pZ[K] + 9216); m_part<0, 0, M_BPOS>(acc, f1, f0, pW0[KN], pZ[KN]); WAIT_L0(); BAR(); SB0(); m_part<M_MASK_B, M_BPOS, 18>(acc, f1, f0, pW0[KN], pZ[KN]); } while (0)
#pragma unroll 1
    for (int s = 0; s < 7; ++s) { M_STAGE(0, 1); M_STAGE(1, 2); M_STAGE(2, 3); M_STAGE(3, 0); }
    M_STAGE(0, 1); M_STAGE(1, 2); M_STAGE(2, 3);
#undef M_STAGE
    m_part<M_MASK_A, 0, 18>(acc, f0, f1, pW1[3], pZ[3] + 9216);
    WAIT_L0(); BAR(); SB0();
    m_part<0, 0, 18>(acc, f1, f0, pW0[0], pZ[0]);
    MMA_DRAIN();
}

struct EFrag { s16x4 w[2]; bf16x8 b[5]; };
template <bool LOAD, bool MM> __device__ __forceinline__ void e_step(f32x4 (&acc)[5], const EFrag& f, EFrag& n, LAS char* pW, LAS char* pB) {
    const bf16x8 w = cat8(f.w[0], f.w[1]);
#pragma unroll
    for (int t = 0; t < 5; ++t) {
        if (MM) MMA(acc[t], w, f.b[t]);
        if (LOAD) {
            if (t == 0) { n.w[0] = lds_tr1(pW); n.w[1] = lds_tr1(pW + 2048); }
            else if (t == 1) { n.b[0] = lds_b128(pB); n.b[1] = lds_b128(pB + 1024); }
            else n.b[t] = lds_b128(pB + t * 1024);
        }
        SB0();
    }
}
template <int NST, int NKS, bool MORE, class Hook> __device__ __forceinline__ void e_gemm(f32x4 (&acc)[5], LAS char* lds, int g0, int w, int lane, Hook hook) {
    asm volatile("" : "+v"(lane));
    const int g = lane >> 4, q = (lane & 15) >> 2;
    LAS char* pW = lds + (4 * g + q) * 128 + ((w ^ ((2 * g + (q >> 1)) & 3)) << 5) + (lane & 3) * 8;
    LAS char* pB = lds + 16384 + lane * 16;
#pragma unroll
    for (int t = 0; t < 5; ++t) { acc[t] = (f32x4){0.f, 0.f, 0.f, 0.f}; asm volatile("" : "+v"(acc[t])); }
    EFrag f0, f1;
    e_step<true, false>(acc, f1, f0, pW + (g0 & 3) * SLOT, pB + (g0 & 3) * SLOT);
    asm volatile("s_nop 1"); SB0();
#pragma unroll 1
    for (int s = 0; s < NST - 1; ++s) {
        const unsigned sb = ((g0 + s) & 3) * SLOT, sn = ((g0 + s + 1) & 3) * SLOT;
        e_step<true, true>(acc, f0, f1, pW + sb + 4096, pB + sb + 5120);
        e_step<true, true>(acc, f1, f0, pW + sb + 8192, pB + sb + 10240);
        e_step<true, true>(acc, f0, f1, pW + sb + 12288, pB + sb + 15360);
        WAIT_L0(); BAR(); SB0();
        hook(s);
        e_step<true, true>(acc, f1, f0, pW + sn, pB + sn);
    }
    const unsigned sb = ((g0 + NST - 1) & 3) * SLOT;
    constexpr int LAST = NKS - 4 * (NST - 1);
    e_step<true, true>(acc, f0, f1, pW + sb + 4096, pB + sb + 5120);
    if (LAST == 4) {
        e_step<true, true>(acc, f1, f0, pW + sb + 8192, pB + sb + 10240);
        e_step<true, true>(acc, f0, f1, pW + sb + 12288, pB + sb + 15360);
    }
    if (MORE) { WAIT_L0(); BAR(); SB0(); }
    e_step<false, true>(acc, f1, f0, pW, pB);
    MMA_DRAIN();
}

__device__ __forceinline__ float dot2(unsigned a, unsigned b, float c) { return __builtin_amdgcn_fdot2_f32_bf16(__builtin_bit_cast(bf16x2_t, a), __builtin_bit_cast(bf16x2_t, b), c, false); }
__device__ __forceinline__ unsigned at_region(int r, int p) { return (unsigned)(((r >> 1) ? 0 : 3 * SLOT) + ((r & 1) ? 16384 + p * 5120 : p * 4096)); }
__device__ __forceinline__ unsigned at_row(int rho, int p) { const int r = rho / 14; return at_region(r, p) + (unsigned)(rho - r * 14) * 288u; }
__device__ __forceinline__ void attn_issue(rsrc_t rQ, LAS char* lds, int n, int p, int lane, int half) {
#pragma unroll
    for (int rr = 0; rr < 2; ++rr)
#pragma unroll
        for (int q = 0; q < 4; ++q) {
            const int r = 2 * half + rr, c = q * 64 + lane, lr = c / 18, cc = c - lr * 18, rho = r * 14 + lr;
            int t, seg;
            if (rho < 22) { t = 8 * n - 14 + rho; seg = 1; } else if (rho < 44) { t = 8 * n - 14 + (rho - 22); seg = 2; } else { t = 8 * n + (rho - 44); seg = 0; }
            const unsigned voff = (t < 0 || rho >= 52 || lr >= 14) ? 0x7ffffff0u : (unsigned)(t * (QW * 2) + (seg * D_ + 144 * p) * 2 + cc * 16);
            dma16_dev(rQ, lds + at_region(r, p) + q * 1024, voff, 0);
        }
}
__device__ __forceinline__ void attn_compute(LAS char* lds, rsrc_t rO, int n, int p, int lane) {
    const int i = lane >> 3, hh = lane & 7, t = 8 * n + i;
    const unsigned hb = hh * 36, r0 = at_region(0, p) + hb, r1 = at_region(1, p) + hb - 14 * 288, r2 = at_region(2, p) + hb - 28 * 288, r3 = at_region(3, p) + hb - 42 * 288;
    unsigned qv[9];
    {
        LAS unsigned* q = (LAS unsigned*)(lds + r3 + (44 + i) * 288);
#pragma unroll
        for (int d = 0; d < 9; ++d) qv[d] = q[d];
    }
    float lg[8];
#pragma unroll
    for (int w = 0; w < 8; ++w) {
        const int rho = 14 + i - 2 * w;
        LAS unsigned* kp = (LAS unsigned*)(lds + (rho < 14 ? r0 : r1) + rho * 288);
        float s = 0.f;
#pragma unroll
        for (int d = 0; d < 9; ++d) s = dot2(qv[d], kp[d], s);
        lg[w] = (t - 2 * w >= 0) ? s * (0.23570226039551584f * 1.4426950408889634f) : -1e9f;
    }
    float mx = fmaxf(fmaxf(fmaxf(lg[0], lg[1]), fmaxf(lg[2], lg[3])), fmaxf(fmaxf(lg[4], lg[5]), fmaxf(lg[6], lg[7])));
    float sum = 0.f;
#pragma unroll
    for (int w = 0; w < 8; ++w) { lg[w] = __builtin_amdgcn_exp2f(lg[w] - mx); sum += lg[w]; }
    const float inv = __builtin_amdgcn_rcpf(sum);
    unsigned pp[4];
#pragma unroll
    for (int w = 0; w < 4; ++w) pp[w] = pack_bf16(lg[2 * w] * inv, lg[2 * w + 1] * inv);
    float acc[18];
#pragma unroll
    for (int d = 0; d < 18; ++d) acc[d] = 0.f;
#pragma unroll
    for (int w = 0; w < 4; ++w) {
        const int ra = 22 + 14 + i - 4 * w, rb = ra - 2;
        LAS unsigned* va = (LAS unsigned*)(lds + (ra < 28 ? r1 : ra < 42 ? r2 : r3) + ra * 288);
        LAS unsigned* vb = (LAS unsigned*)(lds + (rb < 28 ? r1 : rb < 42 ? r2 : r3) + rb * 288);
#pragma unroll
        for (int d = 0; d < 9; ++d) {
            const unsigned x = va[d], y = vb[d];
            acc[2 * d] = dot2(__builtin_amdgcn_perm(y, x, 0x05040100u), pp[w], acc[2 * d]);
            acc[2 * d + 1] = dot2(__builtin_amdgcn_perm(y, x, 0x07060302u), pp[w], acc[2 * d + 1]);
        }
    }
    LAS char* ob = lds + SP_AO + p * 2304;
#pragma unroll
    for (int d = 0; d < 9; ++d) *(LAS unsigned*)(ob + i * 288 + hh * 36 + d * 4) = pack_bf16(acc[2 * d], acc[2 * d + 1]);
    WAIT_L0();
    {
        const int r = lane & 7, g = (lane >> 3) & 3, tt = n >> 1, rl = (8 * n + r) & 15;
#pragma unroll
        for (int q = 0; q < 2; ++q) {
            const int a = (lane >> 5) + 2 * q;
            const u32x2 lo = *(LAS u32x2*)(ob + r * 288 + (32 * a + 4 * g) * 2), hi = *(LAS u32x2*)(ob + r * 288 + (32 * a + 16 + 4 * g) * 2);
            st128_dev(rO, ((tt * 18 + 4 * p + a) * 64 + rl + 16 * g) * 16, (u32x4){lo.x, lo.y, hi.x, hi.y});
        }
        const u32x2 v = *(LAS u32x2*)(ob + r * 288 + (128 + 4 * g) * 2);
        if (lane < 32) st64_dev(rO, ((tt * 18 + 16 + (p >> 1)) * 64 + rl + 16 * g) * 16 + (p & 1) * 8, v);
    }
}

__global__ __launch_bounds__(512, 2) void fused_kernel(Args a) {
    asm volatile("fusedk_pf_begin:");
    extern __shared__ __attribute__((aligned(16))) char smem[];
    LAS char* lds = (LAS char*)smem;
    const int wid = __builtin_amdgcn_readfirstlane(threadIdx.x >> 6), lane = threadIdx.x & 63;
    const int xcd = blockIdx.x & 7, loc = blockIdx.x >> 3;
    unsigned pf_dummy = 0;
    if (wid == 0) {
        u64 base; unsigned sz, back;
        asm volatile("s_getpc_b64 %0\nfusedk_pf_here:\n\ts_mov_b32 %1, fusedk_pf_end-fusedk_pf_begin\n\ts_mov_b32 %2, fusedk_pf_here-fusedk_pf_begin" : "=s"(base), "=s"(sz), "=s"(back));
        base -= back;
        const unsigned off = (unsigned)(loc * 13 + lane) * 128u;
        if (lane < 13 && (int)off < (int)sz) asm volatile("global_load_dword %0, %1, off" : "=v"(pf_dummy) : "v"(base + off));
    }
    const int ti = (xcd & 1) * 8 + (loc & 7), tj = (xcd >> 1) * 4 + (loc >> 3);
    const int nt = xcd * 30 + loc;
    const bool fin = loc < 30 && nt < 234;
    const int rb = nt / 9, cb = nt - rb * 9;
    {
        RowsImg xi0; xi0.init(lane, D_ * 2);
        const rsrc_t rx = mk_rsrc(a.x, S_ * D_ * 2);
        const unsigned so = (unsigned)(ti * 128 + 16 * wid) * (D_ * 2);
        dma16(rx, lds + wid * 2048, xi0.v0, so); dma16(rx, lds + wid * 2048 + 1024, xi0.v1, so + 8 * (D_ * 2));
        dma16(rx, lds + SLOT + wid * 2048, xi0.v0, so + 128); dma16(rx, lds + SLOT + wid * 2048 + 1024, xi0.v1, so + 8 * (D_ * 2) + 128);
    }
    char* ctl = a.ws + WS_CTL;
    if (lane < 2) __builtin_amdgcn_raw_ptr_buffer_load_lds(mk_rsrc(ctl, 4096), (lds_vp)(lds + SP_EP + wid * 64), 4, lane * 4 + (unsigned)CTL_EP, 0, 0, 16);
#define TAG_FROM_EPOCH() \
    u64 ep_; asm volatile("ds_read_b64 %0, %1\n\ts_waitcnt lgkmcnt(0)" : "=&v"(ep_) : "v"((unsigned)(size_t)(lds + SP_EP + wid * 64)) : "memory"); \
    const u64 tg_ = ep_ * 0x9E3779B97F4A7C15ull + 0xD1B54A32D192ED03ull; \
    const u64 tag = ((u64)(unsigned)__builtin_amdgcn_readfirstlane((unsigned)(tg_ >> 32)) << 32) | (unsigned)__builtin_amdgcn_readfirstlane((unsigned)tg_)
    const int an = ti * 16 + tj;

    if (__builtin_expect(wid < 4, 0)) {
    const int m = wid, fr = lane & 15, g4 = lane >> 4;
    ST_DECL; ST(0);
    const rsrc_t rQs = mk_rsrc(a.ws + WS_QKV, S_ * QW * 2), rZs = mk_rsrc(a.ws + WS_Z2T, S_ * FH * 2), rHs = mk_rsrc(a.ws + WS_HF, S_ * FH * 2), rSt = mk_rsrc(a.ws + WS_ST, S_ * 16 * 16);
    LAS float* cst = (LAS float*)(lds + SP_CS);
    float xm[2], xr[2];
    asm volatile("s_waitcnt vmcnt(0)" : "+v"(pf_dummy));
    WAIT_V(0);
    TAG_FROM_EPOCH();
    BAR();
    ST(1);
    {
        XWAcc<7, true> A; xw_zero(A);
        xw_gemm<7, true, true, 9>(A, lds, G_QKV, m, lane, NoHook());
        ST(2);
        share_colsum<7, true, true>(A, lds, m, lane);
#pragma unroll
        for (int t = 0; t < 2; ++t) {
            const int r3 = fr & 3;
            const float dq = r3 == 0 ? A.sq[t][0] : r3 == 1 ? A.sq[t][1] : r3 == 2 ? A.sq[t][2] : A.sq[t][3];
            const float sq = __shfl(dq, (fr >> 2) * 16 + fr);
            xm[t] = A.sx[t][0] * (1.0f / D_);
            xr[t] = __builtin_amdgcn_rsqf(fmaxf(sq * (1.0f / D_) - xm[t] * xm[t], 0.f) + 1e-5f);
        }
        const int c0 = (tj * 27) >> 2, dup = tj > 0 && c0 < (((tj - 1) * 27) >> 2) + 7;
#pragma unroll
        for (int t = 0; t < 2; ++t) {
            unsigned pk[7][2];
#pragma unroll
            for (int c = 0; c < 7; ++c) {
                const int tile = c + m >= 7 ? c + m - 7 : c + m;
                const f32x4 cs = *(LAS f32x4*)(cst + tile * 16 + 4 * g4);
                pk[c][0] = pack2((lo2(A.a[t][c]) - lo2(cs) * xm[t]) * xr[t]); pk[c][1] = pack2((hi2(A.a[t][c]) - hi2(cs) * xm[t]) * xr[t]);
            }
            unrotate<7>(pk, m);
            const unsigned ro = (unsigned)(ti * 128 + 32 * m + 16 * t + fr) * (QW * 2) + c0 * 32;
#pragma unroll
            for (int pp = 0; pp < 3; ++pp) st128_dev(rQs, ro + (1 + 2 * pp) * 32 + g4 * 16, gather8(pk[1 + 2 * pp][0], pk[1 + 2 * pp][1], pk[2 + 2 * pp][0], pk[2 + 2 * pp][1]));
            if (!dup) st64_dev(rQs, ro + g4 * 8, (u32x2){pk[0][0], pk[0][1]});
        }
    }
    ST(3);
    unsigned zp[2][9][2];
    {
        XWAcc<9, false> A; xw_zero(A);
        xw_gemm<9, false, false, 9>(A, lds, G_Z2, m, lane, [&](int s) { if (s == 4) publish(ctl, CTL_QF + ((ti * 16 + tj) * 4 + m) * 8, tag, lane); });
        ST(4);
        share_colsum<9, false, false>(A, lds, m, lane);
        f32x2 rm[2][2], rr[2][2];
#pragma unroll
        for (int t = 0; t < 2; ++t)
#pragma unroll
            for (int h = 0; h < 2; ++h) {
                rm[t][h] = (f32x2){__shfl(xm[t], 4 * g4 + 2 * h), __shfl(xm[t], 4 * g4 + 2 * h + 1)};
                rr[t][h] = (f32x2){__shfl(xr[t], 4 * g4 + 2 * h), __shfl(xr[t], 4 * g4 + 2 * h + 1)};
            }
        f32x2 s1[2][2], s2[2][2];
#pragma unroll
        for (int t = 0; t < 2; ++t)
#pragma unroll
            for (int h = 0; h < 2; ++h) { s1[t][h] = (f32x2){0.f, 0.f}; s2[t][h] = (f32x2){0.f, 0.f}; }
#pragma unroll
        for (int c = 0; c < 9; ++c) {
            const float cs = cst[(c + m >= 9 ? c + m - 9 : c + m) * 16 + fr];
#pragma unroll
            for (int t = 0; t < 2; ++t)
#pragma unroll
                for (int h = 0; h < 2; ++h) {
                    const f32x2 v = gelu2(((h ? hi2(A.a[t][c]) : lo2(A.a[t][c])) - rm[t][h] * cs) * rr[t][h]);
                    s1[t][h] += v; s2[t][h] += v * v;
                    zp[t][c][h] = pack2(v);
                    asm volatile("" : "+v"(zp[t][c][h]));
                }
        }
        float mm = 0.f, qq = 0.f;
        {
            const int idx = lane & 7;
            float a1[8], a2[8];
#pragma unroll
            for (int k = 0; k < 8; ++k) { a1[k] = s1[k >> 2][(k >> 1) & 1][k & 1]; a2[k] = s2[k >> 2][(k >> 1) & 1][k & 1]; }
#pragma unroll
            for (int k = 0; k < 8; ++k) { a1[k] = dpp_add<0x128>(a1[k]); a2[k] = dpp_add<0x128>(a2[k]); }
#pragma unroll
            for (int k = 0; k < 8; ++k) { a1[k] = dpp_add<0x124>(a1[k]); a2[k] = dpp_add<0x124>(a2[k]); }
#pragma unroll
            for (int k = 0; k < 8; ++k) { a1[k] = dpp_add<0x122>(a1[k]); a2[k] = dpp_add<0x122>(a2[k]); }
#pragma unroll
            for (int k = 0; k < 8; ++k) { a1[k] = dpp_add<0x121>(a1[k]); a2[k] = dpp_add<0x121>(a2[k]); }
            float u1 = a1[0], u2 = a2[0];
#pragma unroll
            for (int k = 1; k < 8; ++k) { u1 = idx == k ? a1[k] : u1; u2 = idx == k ? a2[k] : u2; }
            mm = u1 * (1.0f / 144.0f);
            qq = fmaxf(u2 - u1 * mm, 0.f);
            if (fr < 8) st128_dev(rSt, ((ti * 16 + tj) * 128 + 32 * m + 16 * (idx >> 2) + 4 * g4 + (idx & 3)) * 16,
                                  (u32x4){__float_as_uint(mm), (unsigned)tag, __float_as_uint(qq), (unsigned)(tag >> 32)});
        }
    }
    ST(5);
    unsigned z1p[2][9][2];
    {
        XWAcc<9, false> A; xw_zero(A);
        xw_gemm<9, true, false, Z1A>(A, lds, G_Z1, m, lane, NoHook());
        ST(6);
        float mu, rs;
        {
            const unsigned off = ((ti * 16 + (lane >> 5) * 8) * 128 + 32 * m + (lane & 31)) * 16;
            u32x4 v[8]; unsigned spins = 0;
            for (;;) {
#pragma unroll
                for (int k = 0; k < 8; ++k) v[k] = __builtin_amdgcn_raw_buffer_load_b128(rSt, off + k * 2048, 0, 16);
                unsigned bad = 0;
#pragma unroll
                for (int k = 0; k < 8; ++k) bad |= (v[k].y ^ (unsigned)tag) | (v[k].w ^ (unsigned)(tag >> 32));
                if (__all(bad == 0) || ++spins > SPIN_CAP) break;
                __builtin_amdgcn_s_sleep(2);
            }
            float s = 0.f;
#pragma unroll
            for (int k = 0; k < 8; ++k) s += __uint_as_float(v[k].x);
            s += xchg32(s);
            mu = s * (1.0f / 16.0f);
            float q = 0.f;
#pragma unroll
            for (int k = 0; k < 8; ++k) { const float d = __uint_as_float(v[k].x) - mu; q += __uint_as_float(v[k].z) + 144.0f * d * d; }
            q += xchg32(q);
            rs = __builtin_amdgcn_rsqf(q * (1.0f / FH) + 1e-5f);
        }
        ST(7);
        f32x2 mr[2][2], rr[2][2];
#pragma unroll
        for (int t = 0; t < 2; ++t)
#pragma unroll
            for (int h = 0; h < 2; ++h) {
                const int src = 16 * t + 4 * g4 + 2 * h;
                mr[t][h] = (f32x2){__shfl(mu, src), __shfl(mu, src + 1)}; rr[t][h] = (f32x2){__shfl(rs, src), __shfl(rs, src + 1)};
            }
        const unsigned zo = (unsigned)((tj * 64 + ti * 4 + m) * 9) * 1024 + lane * 16;
#pragma unroll
        for (int c = 0; c < 9; ++c) {
            const unsigned x0 = pack2((unpack2(zp[0][c][0]) - mr[0][0]) * rr[0][0]), x1 = pack2((unpack2(zp[0][c][1]) - mr[0][1]) * rr[0][1]);
            const unsigned y0 = pack2((unpack2(zp[1][c][0]) - mr[1][0]) * rr[1][0]), y1 = pack2((unpack2(zp[1][c][1]) - mr[1][1]) * rr[1][1]);
            st128_dev(rZs, zo + (c + m >= 9 ? c + m - 9 : c + m) * 1024, gather8(x0, x1, y0, y1));
        }
        ST(8);
        xw_gemm<9, true, false, 9 - Z1A>(A, lds, G_Z1 + Z1A, m, lane, [&](int s) { if (s == 1) publish(ctl, CTL_ZF + ((tj * 16 + ti) * 4 + m) * 8, tag, lane); });
        ST(9);
        share_colsum<9, true, false>(A, lds, m, lane);
#pragma unroll
        for (int c = 0; c < 9; ++c) {
            const f32x4 cs = *(LAS f32x4*)(cst + (c + m >= 9 ? c + m - 9 : c + m) * 16 + 4 * g4);
#pragma unroll
            for (int t = 0; t < 2; ++t) {
                z1p[t][c][0] = pack2(gelu2((lo2(A.a[t][c]) - lo2(cs) * xm[t]) * xr[t])); z1p[t][c][1] = pack2(gelu2((hi2(A.a[t][c]) - hi2(cs) * xm[t]) * xr[t]));
                asm volatile("" : "+v"(z1p[t][c][0]), "+v"(z1p[t][c][1]));
            }
        }
        unrotate<9>(z1p[0], m); unrotate<9>(z1p[1], m);
    }
    ST(10);
    BAR();
    {
        f32x4 acc[2][9];
        float bt[2];
#pragma unroll
        for (int t = 0; t < 2; ++t) bt[t] = bf2f(a.bsp[ti * 128 + 32 * m + 16 * t + fr]);
        main_gemm(acc, lds, G_MAIN, m, lane);
        ST(11);
#pragma unroll
        for (int t = 0; t < 2; ++t) {
            const unsigned hp = (unsigned)(ti * 8 + 2 * m + t) * 72 * 1024 + lane * 16;
#pragma unroll
            for (int pp = 0; pp < 4; ++pp) {
                u32x4 o;
                o.x = pack2((lo2(acc[t][2 * pp]) + bt[t]) * unpack2(z1p[t][2 * pp][0])); o.y = pack2((hi2(acc[t][2 * pp]) + bt[t]) * unpack2(z1p[t][2 * pp][1]));
                o.z = pack2((lo2(acc[t][2 * pp + 1]) + bt[t]) * unpack2(z1p[t][2 * pp + 1][0])); o.w = pack2((hi2(acc[t][2 * pp + 1]) + bt[t]) * unpack2(z1p[t][2 * pp + 1][1]));
                st128_dev(rHs, hp + (tj * 4 + pp) * 1024, o);
            }
            u32x2 o;
            o.x = pack2((lo2(acc[t][8]) + bt[t]) * unpack2(z1p[t][8][0])); o.y = pack2((hi2(acc[t][8]) + bt[t]) * unpack2(z1p[t][8][1]));
            st64_dev(rHs, hp + (64 + (tj >> 1)) * 1024 + (tj & 1) * 8, o);
        }
    }
    ST(12);
    publish(ctl, CTL_HF + ((ti * 16 + tj) * 4 + m) * 8, tag, lane);
    ST(13);
    if (!fin) { ST_END(0); return; }
    {
        f32x4 sa[5], ga[5];
        u32x2 xv[5];
        const int fr = lane & 15, g4 = lane >> 4;
#pragma unroll
        for (int t = 0; t < 5; ++t) xv[t] = *(const u32x2*)(a.x + (size_t)(min(rb * 5 + t, 127) * 16 + fr) * D_ + cb * 64 + m * 16 + g4 * 4);
        BAR();
        ST(14);
        e_gemm<18, 72, true>(ga, lds, G_G, m, lane, NoHook());
        ST(15);
        e_gemm<5, 18, false>(sa, lds, G_S, m, lane, NoHook());
        ST(16);
#pragma unroll
        for (int t = 0; t < 5; ++t) {
            if (rb * 5 + t < 128) {
                float o[4];
#pragma unroll
                for (int r = 0; r < 4; ++r) {
                    const float gv = ga[t][r], sg = gv * __builtin_amdgcn_rcpf(1.0f + __builtin_amdgcn_exp2f(gv * -1.4426950408889634f));
                    const float xx = (r & 1) ? bfhi(xv[t][r >> 1]) : bflo(xv[t][r >> 1]);
                    o[r] = xx + sa[t][r] * sg;
                }
                u32x2 ov; ov.x = pack_bf16(o[0], o[1]); ov.y = pack_bf16(o[2], o[3]);
                *(u32x2*)(a.out + (size_t)((rb * 5 + t) * 16 + fr) * D_ + cb * 64 + m * 16 + g4 * 4) = ov;
            }
        }
    }
    if (blockIdx.x == 0 && threadIdx.x == 0) __hip_atomic_store((GAS u64*)(ctl + CTL_EP), tag, __ATOMIC_RELAXED, __HIP_MEMORY_SCOPE_AGENT);
    ST(17);
    ST_END(0);
    asm volatile("fusedk_pf_end:");
        return;
    }
    {

        const int p = wid - 4;
        ST_DECL; ST(0);
#define rX mk_rsrc(a.x, S_ * D_ * 2)
#define rQW mk_rsrc(a.wqkv, D_ * QW * 2)
#define rW mk_rsrc(a.win, D_ * F_ * 2)
#define rS mk_rsrc(a.wsp, S_ * S_ * 2)
#define rZ mk_rsrc(a.ws + WS_Z2T, S_ * FH * 2)
#define rQ mk_rsrc(a.ws + WS_QKV, S_ * QW * 2)
#define rO mk_rsrc(a.ws + WS_OF, S_ * D_ * 2)
        RowsImg xi; xi.init(lane, D_ * 2);
        RowsImg si; si.init(lane, S_ * 2);
        WImg<7> wq; wq.init(lane, p, QW * 2);
        WImg<9> wz; wz.init(lane, p, F_ * 2);
        const unsigned sX0 = (unsigned)(ti * 128) * (D_ * 2), sQ = (unsigned)((tj * 27) >> 2) * 32, sZ1 = (unsigned)(tj * 144) * 2, sZ2 = sZ1 + FH * 2;
        const unsigned sS = (unsigned)(ti * 128 + 32 * p) * (S_ * 2), sZ = (unsigned)tj * (64u * 9216u);
        const unsigned vl = lane * 16;
        const unsigned vE = (lane >> 3) * (D_ * 2) + ((((lane & 7) >> 1) ^ ((lane >> 4) & 3)) << 5) + (lane & 1) * 16;
        const int zI0 = WImg<9>::first(p);
        auto iss_q = [&](int g) {
            LAS char* slot = lds + (g & 3) * SLOT;
            xi.issue35(rX, slot, sX0 + g * 128, D_ * 2, p); wq.issue(rQW, slot + 16384, sQ + g * 64 * (QW * 2), p);
        };
        auto iss_z = [&](int g, int s, unsigned col) {
            LAS char* slot = lds + (g & 3) * SLOT;
            xi.issue35(rX, slot, sX0 + s * 128, D_ * 2, p);
            wz.issue(rW, slot + 16384, col + s * 64 * (F_ * 2), p);
        };
        auto iss_m = [&](int g) {
            LAS char* slot = lds + (g & 3) * SLOT; const int s = g - G_MAIN;
            si.issue(rS, slot + p * 4096, sS + s * 128, S_ * 2);
#pragma unroll
            for (int q = 0; q < 4; ++q) dma16_dev(rZ, slot + 16384 + (zI0 + q) * 1024, vl, sZ + s * 18432 + (zI0 + q) * 1024);
            if (p < 2) dma16_dev(rZ, slot + 16384 + (zI0 + 4) * 1024, vl, sZ + s * 18432 + (zI0 + 4) * 1024);
        };
        unsigned eH[5], eO[5];
#pragma unroll
        for (int t = 0; t < 5; ++t) { const unsigned tt = (unsigned)__builtin_amdgcn_readfirstlane(min(rb * 5 + t, 127)); eH[t] = tt * 73728u + p * 1024u; eO[t] = tt * 18432u + p * 1024u; }
        auto iss_ew = [&](int g, rsrc_t rw, unsigned w0, unsigned w1) {
            LAS char* slot = lds + (g & 3) * SLOT;
            dma16(rw, slot + p * 4096, vE, w0); dma16(rw, slot + p * 4096 + 1024, vE, w0 + 8 * (D_ * 2));
            dma16(rw, slot + p * 4096 + 2048, vE, w1); dma16(rw, slot + p * 4096 + 3072, vE, w1 + 8 * (D_ * 2));
        };
        auto iss_eb = [&](int g, rsrc_t rb_, const unsigned (&eb)[5], unsigned bo) {
            LAS char* slot = lds + (g & 3) * SLOT;
#pragma unroll
            for (int t = 0; t < 5; ++t) dma16_dev(rb_, slot + 16384 + p * 5120 + t * 1024, vl, eb[t] + bo);
        };
        auto iss_e = [&](int g, rsrc_t rw, rsrc_t rb_, const unsigned (&eb)[5], unsigned w0, unsigned w1, unsigned bo) { iss_ew(g, rw, w0, w1); iss_eb(g, rb_, eb, bo); };
        constexpr unsigned TILE_B = 16 * D_ * 2;
        const unsigned wcol = cb * 128;
        auto iss_g = [&](int s) {
            const unsigned c0 = s < 16 ? 9 * s + 2 * p : 18 * (4 * (s - 16) + p) + 8, c1 = s < 16 ? c0 + 1 : c0 + 9;
            iss_e(G_G + s, mk_rsrc(a.wout, FH * D_ * 2), mk_rsrc(a.ws + WS_HF, S_ * FH * 2), eH, c0 * TILE_B + wcol, c1 * TILE_B + wcol, s * 4096);
        };
        auto iss_s = [&](int s) {
            const int pp = s == 4 && p > 1 ? 1 : p;
            const unsigned c0 = s < 4 ? 9 * s + 2 * p : 18 * pp + 8, c1 = s < 4 ? c0 + 1 : c0 + 9;
            iss_e(G_S + s, mk_rsrc(a.wo, D_ * D_ * 2), rO, eO, c0 * TILE_B + wcol, c1 * TILE_B + wcol, s * 4096 + (pp - p) * 1024);
        };
#define PSTEP(K, PH) do { WAIT_V(7 * (K)); BAR(); } while (0)
#define PW(N, PH) do { WAIT_V(N); BAR(); } while (0)
#define SPIN_UNTIL(cond) do { unsigned spins_ = 0; while (!(cond) && ++spins_ < SPIN_CAP) __builtin_amdgcn_s_sleep(4); asm volatile("" ::: "memory"); } while (0)
        wq.issue(rQW, lds + 16384, sQ, p); wq.issue(rQW, lds + SLOT + 16384, sQ + 64 * (QW * 2), p);
        iss_q(2); iss_q(3);
        {
            const unsigned W = blockIdx.x * 4 + p;
            touch_lines(rX, lds + SP_PF + wid * 256, W * 18, 18, lane);
            touch_lines(rQW, lds + SP_PF + wid * 256, W * 16, 16, lane);
        }
        WAIT_V(20);
        TAG_FROM_EPOCH();
        WAIT_V(14); BAR();
#pragma unroll 1
        for (int g = 1; g <= 5; ++g) { PW(14, 0); iss_q(g + 3); if (g == 2) touch_lines(rW, lds + SP_PF + wid * 256, (blockIdx.x * 4 + p) * 41, 41, lane); }
#pragma unroll 1
        for (int g = 6; g <= 7; ++g) { PSTEP(2, 1); iss_z(g + 3, g + 3 - G_Z2, sZ2); }
#pragma unroll 1
        for (int g = 8; g <= 14; ++g) { PW(16, 1); if (g == G_Z2) BAR(); iss_z(g + 3, g + 3 - G_Z2, sZ2); }
        PW(16, 1); iss_z(18, 0, sZ1);
        PW(16, 1); iss_z(19, 1, sZ1);
        PW(16, 1); iss_z(20, 2, sZ1);
        PW(16, 2);
        BAR();
        iss_z(21, 3, sZ1);
        {
            const unsigned W = blockIdx.x * 4 + p;
            touch_lines(rS, lds + SP_PF + wid * 256, W * 64, 64, lane);
            touch_lines(mk_rsrc(a.wout, FH * D_ * 2), lds + SP_PF + wid * 256, W * 21, 21, lane);
            touch_lines(mk_rsrc(a.wo, D_ * D_ * 2), lds + SP_PF + wid * 256, W * 6, 6, lane);
        }
#pragma unroll 1
        for (int g = 19; g <= 23; ++g) { PW(16, 2); iss_z(g + 3, g + 3 - G_Z1, sZ1); }
        PSTEP(2, 2); PSTEP(1, 2); PSTEP(0, 2);
        BAR(); BAR();
        SPIN_UNTIL(flags_ready(ctl + CTL_ZF + tj * 512, 64, ctl, 0, tag, lane));
        iss_m(27); iss_m(28); iss_m(29); iss_m(30);
        {
            const int pa = (8 * an - 14) >> 7;
            SPIN_UNTIL(flags_ready(ctl + CTL_QF + ti * 512, 64, ctl + CTL_QF + (pa < 0 ? ti : pa) * 512, 64, tag, lane));
        }
        BAR();
#pragma unroll 1
        for (int g = 28; g <= 55; ++g) { PW(16, 3); iss_m(g + 3); }
        PSTEP(2, 3); attn_issue(rQ, lds, an, p, lane, 0);
        PSTEP(1, 3); attn_issue(rQ, lds, an, p, lane, 1);
        PSTEP(1, 3);
        BAR();
        ST(1);
        WAIT_V(0);
        attn_compute(lds, rO, an, p, lane);
        ST(2);
        if (!fin) publish(ctl, CTL_OF + (an * 4 + p) * 8, tag, lane);
        else {
            {
                const int p0 = (rb * 80) >> 7, p1 = min(rb * 80 + 79, S_ - 1) >> 7;
                WAIT_V(0);
                if (lane == 0) __hip_atomic_store((GAS u64*)(ctl + CTL_OF + (an * 4 + p) * 8), tag, __ATOMIC_RELAXED, __HIP_MEMORY_SCOPE_AGENT);
#pragma unroll
                for (int s4 = 0; s4 < 4; ++s4) iss_ew(G_G + s4, mk_rsrc(a.wout, FH * D_ * 2), (9 * s4 + 2 * p) * TILE_B + wcol, (9 * s4 + 2 * p + 1) * TILE_B + wcol);
                SPIN_UNTIL(flags_ready(ctl + CTL_HF + p0 * 512, 64, ctl + CTL_HF + p1 * 512, 64, tag, lane));
                SPIN_UNTIL(flags_ready(ctl + CTL_OF + rb * 320, rb == 25 ? 24 : 40, ctl, 0, tag, lane));
            }
            ST(3);
            {
                const rsrc_t rh = mk_rsrc(a.ws + WS_HF, S_ * FH * 2);
                iss_eb(G_G, rh, eH, 0); iss_eb(G_G + 1, rh, eH, 4096); iss_eb(G_G + 2, rh, eH, 8192); iss_eb(G_G + 3, rh, eH, 12288);
            }
            PSTEP(2, 5);
            PW(10, 5); iss_g(4);
            PW(14, 5); iss_g(5);
            {
                const rsrc_t rw = mk_rsrc(a.wout, FH * D_ * 2), rh = mk_rsrc(a.ws + WS_HF, S_ * FH * 2);
                unsigned w0 = (54 + 2 * p) * TILE_B + wcol, bo = 6 * 4096;
#pragma unroll 1
                for (int g = 62; g <= 71; ++g) {
                    PW(18, 5);
                    iss_e(g + 3, rw, rh, eH, w0, w0 + TILE_B, bo);
                    w0 += 9 * TILE_B; bo += 4096;
                }
            }
            PW(18, 5); iss_g(16);
            PW(18, 5); iss_g(17);
            PW(18, 4); iss_s(0);
            PW(18, 4); iss_s(1);
            PW(18, 4); iss_s(2);
            PW(18, 4); iss_s(3);
            PW(18, 4); iss_s(4);
            PSTEP(2, 4); PSTEP(1, 4); PSTEP(0, 4);
        }
        ST_END(1);
        return;
        }
}

extern "C" void kernel_launch(void* const* d_in, const int* in_sizes, int n_in, void* d_out, int out_size, void* d_ws, size_t ws_size, hipStream_t stream) {
    Args a;
    a.x = (const u16*)d_in[0]; a.wqkv = (const u16*)d_in[1]; a.wo = (const u16*)d_in[2]; a.win = (const u16*)d_in[4]; a.wsp = (const u16*)d_in[5];
    a.bsp = (const u16*)d_in[6]; a.wout = (const u16*)d_in[7]; a.out = (u16*)d_out; a.ws = (char*)d_ws;
    (void)hipFuncSetAttribute((const void*)fused_kernel, hipFuncAttributeMaxDynamicSharedMemorySize, LDS_BYTES);
    fused_kernel<<<256, 512, LDS_BYTES, stream>>>(a);
}
```

```cpp
#include <hip/hip_runtime.h>
#include <hip/hip_bf16.h>
#include <stdint.h>

typedef unsigned short u16;
typedef unsigned long long u64;
typedef short bf16x8 __attribute__((ext_vector_type(8)));
typedef short s16x4 __attribute__((ext_vector_type(4)));
typedef float f32x4 __attribute__((ext_vector_type(4)));
typedef float f32x2 __attribute__((ext_vector_type(2)));
typedef unsigned u32x4 __attribute__((ext_vector_type(4)));
typedef unsigned u32x2 __attribute__((ext_vector_type(2)));
typedef __bf16 bf16x2_t __attribute__((ext_vector_type(2)));
#define LAS __attribute__((address_space(3)))
#define GAS __attribute__((address_space(1)))
typedef LAS void* lds_vp;
typedef __amdgpu_buffer_rsrc_t rsrc_t;

constexpr int S_ = 2048, D_ = 576, F_ = 4608, FH = 2304, QW = 1728;
constexpr int SLOT = 36864, SPARE = 4 * SLOT, LDS_BYTES = 163840;
constexpr int SP_CS = SPARE, SP_PF = SPARE + 1024, SP_EP = SPARE + 3072, SP_AO = SPARE + 4096;
constexpr size_t WS_Z2T = 0;
constexpr size_t WS_QKV = WS_Z2T + (size_t)S_ * FH * 2;
constexpr size_t WS_HF = WS_QKV + (size_t)S_ * QW * 2;
constexpr size_t WS_OF = WS_HF + (size_t)S_ * FH * 2;
constexpr size_t WS_ST = WS_OF + (size_t)S_ * D_ * 2;
constexpr size_t WS_CTL = WS_ST + (size_t)S_ * 16 * 16;
constexpr size_t CTL_EP = 0, CTL_QF = 1024, CTL_ZF = CTL_QF + 8192, CTL_OF = CTL_ZF + 8192, CTL_HF = CTL_OF + 8192, CTL_END = CTL_HF + 8192;
constexpr int G_QKV = 0, G_Z2 = 9, G_Z1 = 18, G_MAIN = 27, G_G = 59, G_S = 77;
constexpr unsigned SPIN_CAP = 400000u;

__device__ __forceinline__ float bflo(unsigned v) { return __uint_as_float(v << 16); }
__device__ __forceinline__ float bfhi(unsigned v) { return __uint_as_float(v & 0xffff0000u); }
__device__ __forceinline__ float bf2f(u16 v) { return __uint_as_float(((unsigned)v) << 16); }
__device__ __forceinline__ unsigned pack_bf16(float lo, float hi) {
    f32x2 v = {lo, hi};
    return __builtin_bit_cast(unsigned, __builtin_convertvector(v, bf16x2_t));
}
#define BAR() do { asm volatile("" ::: "memory"); __builtin_amdgcn_s_barrier(); asm volatile("" ::: "memory"); } while (0)
#define WAIT_V(n) asm volatile("s_waitcnt vmcnt(%0)" ::"n"(n) : "memory")
#define WAIT_L0() asm volatile("s_waitcnt lgkmcnt(0)" ::: "memory")
#define SB0() __builtin_amdgcn_sched_barrier(0)
#define ST_DECL
#define ST(k)
#define ST_END(who)

__device__ __forceinline__ rsrc_t mk_rsrc(const void* p, unsigned bytes) { return __builtin_amdgcn_make_buffer_rsrc((void*)p, 0, bytes, 0x00020000); }
__device__ __forceinline__ void dma16(rsrc_t r, LAS char* lds, unsigned voff, unsigned soff) {
    __builtin_amdgcn_raw_ptr_buffer_load_lds(r, (lds_vp)lds, 16, voff, soff, 0, 0);
}
__device__ __forceinline__ void dma16_dev(rsrc_t r, LAS char* lds, unsigned voff, unsigned soff) {
    __builtin_amdgcn_raw_ptr_buffer_load_lds(r, (lds_vp)lds, 16, voff, soff, 0, 16);
}
#define ATT_PRIO 1
#define STAGE_ST 0
#define Z1A 4
#define ST_AUX 16
__device__ __forceinline__ void touch_lines(rsrc_t r, LAS char* scratch, unsigned first, unsigned cnt, int lane) {
    __builtin_amdgcn_raw_ptr_buffer_load_lds(r, (lds_vp)scratch, 4, (unsigned)lane < cnt ? (first + lane) * 128u : 0x7ffffff0u, 0, 0, 0);
}
__device__ __forceinline__ void st128_dev(rsrc_t r, unsigned off, u32x4 v) { __builtin_amdgcn_raw_buffer_store_b128(v, r, off, 0, ST_AUX); }
__device__ __forceinline__ void st64_dev(rsrc_t r, unsigned off, u32x2 v) { __builtin_amdgcn_raw_buffer_store_b64(v, r, off, 0, ST_AUX); }
__device__ __forceinline__ bf16x8 lds_b128(LAS char* p) { return *(LAS bf16x8*)p; }
__device__ __forceinline__ bf16x8 lds_tr(LAS char* p, int off1) {
    s16x4 a = __builtin_amdgcn_ds_read_tr16_b64_v4i16((LAS s16x4*)p);
    s16x4 b = __builtin_amdgcn_ds_read_tr16_b64_v4i16((LAS s16x4*)(p + off1));
    return __builtin_shufflevector(a, b, 0, 1, 2, 3, 4, 5, 6, 7);
}
#define MFMA(a, b, c) __builtin_amdgcn_mfma_f32_16x16x32_bf16(a, b, c, 0, 0, 0)
template <int CTRL> __device__ __forceinline__ float dpp_add(float x) {
    return x + __builtin_bit_cast(float, __builtin_amdgcn_update_dpp(0, __builtin_bit_cast(int, x), CTRL, 0xf, 0xf, false));
}
__device__ __forceinline__ float row16_sum(float x) { x = dpp_add<0x128>(x); x = dpp_add<0x124>(x); x = dpp_add<0x122>(x); x = dpp_add<0x121>(x); return x; }
__device__ __forceinline__ float gelu_f(float x) {
    const float t = x * (-2.3022081498f - 0.1029432397f * x * x);
    return x * __builtin_amdgcn_rcpf(1.0f + __builtin_amdgcn_exp2f(t));
}
__device__ __forceinline__ f32x2 gelu2(f32x2 x) {
    const f32x2 t = x * (x * x * -0.1029432397f + -2.3022081498f);
    f32x2 e; e.x = __builtin_amdgcn_exp2f(t.x); e.y = __builtin_amdgcn_exp2f(t.y);
    const f32x2 d = e + 1.0f;
    f32x2 r; r.x = __builtin_amdgcn_rcpf(d.x); r.y = __builtin_amdgcn_rcpf(d.y);
    return x * r;
}
__device__ __forceinline__ f32x2 lo2(f32x4 v) { return (f32x2){v[0], v[1]}; }
__device__ __forceinline__ f32x2 hi2(f32x4 v) { return (f32x2){v[2], v[3]}; }
__device__ __forceinline__ unsigned pack2(f32x2 v) { return __builtin_bit_cast(unsigned, __builtin_convertvector(v, bf16x2_t)); }
__device__ __forceinline__ f32x2 unpack2(unsigned v) { return (f32x2){__uint_as_float(v << 16), __uint_as_float(v & 0xffff0000u)}; }
__device__ __forceinline__ int swap23(int r) { return (r & 0x13) | ((r & 4) << 1) | ((r & 8) >> 1); }
__device__ __forceinline__ float xchg32(float x) {
    auto r = __builtin_amdgcn_permlane32_swap(__float_as_uint(x), __float_as_uint(x), false, false);
    return __uint_as_float((threadIdx.x & 32) ? r[0] : r[1]);
}
__device__ __forceinline__ u32x4 gather8(unsigned x0, unsigned x1, unsigned y0, unsigned y1) {
    auto s0 = __builtin_amdgcn_permlane32_swap(x0, y0, false, false);
    auto s1 = __builtin_amdgcn_permlane32_swap(x1, y1, false, false);
    auto t0 = __builtin_amdgcn_permlane16_swap(s0[0], s0[1], false, false);
    auto t1 = __builtin_amdgcn_permlane16_swap(s1[0], s1[1], false, false);
    return (u32x4){t0[0], t1[0], t0[1], t1[1]};
}
__device__ __forceinline__ void publish(char* ctl, size_t word, u64 tag, int lane) {
    asm volatile("s_waitcnt vmcnt(0)" ::: "memory");
    if (lane == 0) __hip_atomic_store((GAS u64*)(ctl + word), tag, __ATOMIC_RELAXED, __HIP_MEMORY_SCOPE_AGENT);
}
__device__ __forceinline__ bool flags_ready(const char* f0, int n0, const char* f1, int n1, u64 tag, int lane) {
    u64 a = tag, b = tag;
    if (lane < n0) a = __hip_atomic_load((const GAS u64*)(f0 + lane * 8), __ATOMIC_RELAXED, __HIP_MEMORY_SCOPE_AGENT);
    if (lane < n1) b = __hip_atomic_load((const GAS u64*)(f1 + lane * 8), __ATOMIC_RELAXED, __HIP_MEMORY_SCOPE_AGENT);
    return __all(a == tag && b == tag);
}

struct Args { const u16* x; const u16* wqkv; const u16* wo; const u16* win; const u16* wsp; const u16* bsp; const u16* wout; u16* out; char* ws; };

struct RowsImg {
    unsigned v0, v1;
    __device__ __forceinline__ void init(int lane, unsigned rs) {
        const int r8 = lane >> 3, ch = lane & 7;
        v0 = r8 * rs + ((ch ^ ((lane >> 4) & 7)) << 4); v1 = r8 * rs + ((ch ^ ((4 + (lane >> 4)) & 7)) << 4);
    }
    __device__ __forceinline__ void issue(rsrc_t r, LAS char* dst, unsigned soff, unsigned rs) const {
#pragma unroll
        for (int q = 0; q < 4; ++q) dma16(r, dst + q * 1024, (q & 1) ? v1 : v0, soff + q * 8 * rs);
    }
    __device__ __forceinline__ void issue35(rsrc_t r, LAS char* dst, unsigned soff, unsigned rs, int p) const {
        const int q0 = p < 2 ? 3 * p : 6 + 5 * (p - 2);
        const unsigned va = (q0 & 1) ? v1 : v0, vb = (q0 & 1) ? v0 : v1;
#pragma unroll
        for (int q = 0; q < 3; ++q) dma16(r, dst + (q0 + q) * 1024, (q & 1) ? vb : va, soff + (q0 + q) * 8 * rs);
        if (p >= 2) {
#pragma unroll
            for (int q = 3; q < 5; ++q) dma16(r, dst + (q0 + q) * 1024, (q & 1) ? vb : va, soff + (q0 + q) * 8 * rs);
        }
    }
};
template <int NC> struct WImg {
    static constexpr int CPR = 2 * NC, BIG = (NC == 9) ? 5 : 4;
    unsigned v[BIG];
    static __device__ __forceinline__ int first(int p) { return NC == 9 ? (p < 2 ? 5 * p : 10 + 4 * (p - 2)) : (p < 2 ? 4 * p : 8 + 3 * (p - 2)); }
    __device__ __forceinline__ void init(int lane, int p, unsigned ldb) {
        const int I0 = first(p);
#pragma unroll
        for (int q = 0; q < BIG; ++q) {
            const int n = (I0 + q) * 64 + lane, pos = n / CPR, cc = n - pos * CPR;
            v[q] = ((pos & 32) + swap23(pos & 31)) * ldb + cc * 16;
        }
    }
    __device__ __forceinline__ void issue(rsrc_t r, LAS char* dst, unsigned soff, int p) const {
        const int I0 = first(p);
#pragma unroll
        for (int q = 0; q < BIG - 1; ++q) dma16(r, dst + (I0 + q) * 1024, v[q], soff);
        if (p < 2) dma16(r, dst + (I0 + BIG - 1) * 1024, v[BIG - 1], soff);
    }
};

#define MMA(acc, a, b) asm volatile("v_mfma_f32_16x16x32_bf16 %0, %1, %2, %0" : "+v"(acc) : "v"(a), "v"(b))
#define MMA_DRAIN() do { SB0(); asm volatile("s_nop 7\n\ts_nop 3"); SB0(); } while (0)
#define CST(idx)
struct NoHook { __device__ __forceinline__ void operator()(int) const {} };
__device__ __forceinline__ s16x4 lds_tr1(LAS char* p) { return __builtin_amdgcn_ds_read_tr16_b64_v4i16((LAS s16x4*)p); }
__device__ __forceinline__ bf16x8 cat8(s16x4 a, s16x4 b) { return __builtin_shufflevector(a, b, 0, 1, 2, 3, 4, 5, 6, 7); }

template <int NC, bool XST> struct XWAcc { f32x4 a[2][NC]; f32x4 cs[3]; f32x4 sx[2], sq[2]; };
template <int NC> struct XF { bf16x8 x[2]; s16x4 w[NC][2]; };
struct XAddr { unsigned x, w, r[3]; };
template <int NC> __device__ __forceinline__ void xa_read1(XF<NC>& f, int i, const XAddr& a) {
    if (i < 2) f.x[i] = lds_b128((LAS char*)(size_t)a.x + i * 2048);
    else {
        const int c = (i - 2) >> 1;
        const unsigned q = c >= NC - 3 ? a.r[c >= NC - 3 ? c - (NC - 3) : 0] : a.w;
        f.w[c][i & 1] = lds_tr1((LAS char*)(size_t)q + c * 32 + (i & 1) * (8 * NC * 32));
    }
}
#define VADD(v, d) asm volatile("v_add_u32 %0, %1, %0" : "+v"(v) : "s"(d))
template <int NC, bool TR, bool XST, int DBL, bool LOAD, bool UPD>
__device__ __forceinline__ void xa_step(XWAcc<NC, XST>& A, const XF<NC>& f, XF<NC>& n, XAddr& ra, XAddr& ua, int d, const bf16x8& ones) {
    constexpr int R = 2 + 2 * NC, G = 2 * NC + (NC + 3) / 4 + (XST ? 4 : 0);
    int rd = 0, gap = 0;
#define XA_UPD(k) do { if ((k) == 0) VADD(ua.x, d); if ((k) == 1) VADD(ua.w, d); if ((k) == 2) VADD(ua.r[0], d); if ((k) == 3) VADD(ua.r[1], d); if ((k) == 4) VADD(ua.r[2], d); \
                       if ((k) == 5) VADD(ra.x, d); if ((k) == 6) VADD(ra.w, d); if ((k) == 7) VADD(ra.r[0], d); if ((k) == 8) VADD(ra.r[1], d); if ((k) == 9) VADD(ra.r[2], d); } while (0)
#define XA_GAP() do { if (LOAD) { if (rd < R) { xa_read1<NC>(n, rd, ra); ++rd; } if (gap < DBL && rd < R) { xa_read1<NC>(n, rd, ra); ++rd; } } \
                      if (UPD && gap >= G - 6 && gap < G - 1) { XA_UPD(2 * (gap - (G - 6))); XA_UPD(2 * (gap - (G - 6)) + 1); } ++gap; SB0(); } while (0)
#pragma unroll
    for (int c = 0; c < NC; ++c) {
        const bf16x8 w = cat8(f.w[c][0], f.w[c][1]);
#pragma unroll
        for (int t = 0; t < 2; ++t) {
            if (TR) MMA(A.a[t][c], w, f.x[t]); else MMA(A.a[t][c], f.x[t], w);
            XA_GAP();
        }
        if ((c & 3) == 0) {
            if (TR) MMA(A.cs[c >> 2], w, ones); else MMA(A.cs[c >> 2], ones, w);
            XA_GAP();
        }
    }
    if (XST) {
#pragma unroll
        for (int t = 0; t < 2; ++t) { MMA(A.sx[t], ones, f.x[t]); XA_GAP(); MMA(A.sq[t], f.x[t], f.x[t]); XA_GAP(); }
    }
    SB0();
#undef XA_GAP
#undef XA_UPD
}
template <int NC, bool XST> __device__ __forceinline__ void xw_zero(XWAcc<NC, XST>& A) {
    const f32x4 z = {0.f, 0.f, 0.f, 0.f};
#pragma unroll
    for (int t = 0; t < 2; ++t) {
#pragma unroll
        for (int c = 0; c < NC; ++c) { A.a[t][c] = z; asm volatile("" : "+v"(A.a[t][c])); }
        if (XST) { A.sx[t] = z; A.sq[t] = z; asm volatile("" : "+v"(A.sx[t]), "+v"(A.sq[t])); }
    }
    A.cs[0] = z; A.cs[1] = z; asm volatile("" : "+v"(A.cs[0]), "+v"(A.cs[1]));
    if (NC > 8) { A.cs[2] = z; asm volatile("" : "+v"(A.cs[2])); }
}
template <int NC, bool TR, bool XST, int NST, class Hook>
__device__ __forceinline__ void xw_gemm(XWAcc<NC, XST>& A, LAS char* lds, int g0, int m, int lane, Hook hook) {
    asm volatile("" : "+v"(lane));
    const int fr = lane & 15, g = lane >> 4, row = 32 * m + fr;
    constexpr int G = 2 * NC + (NC + 3) / 4 + (XST ? 4 : 0), R = 2 + 2 * NC;
    constexpr int DA = R > G - 7 ? R - (G - 7) : 0, DB = R > G ? R - G : 0;
    static_assert(R <= G - 7 + DA && R <= G + DB, "all reads of a k-step are issued inside it");
    const unsigned lb = (unsigned)(size_t)lds + (g0 & 3) * SLOT;
    XAddr a0, a1;
    a0.x = lb + row * 128 + ((g ^ ((row >> 1) & 7)) << 4); a1.x = lb + ((row * 128 + ((g ^ ((row >> 1) & 7)) << 4)) ^ 64);
    a0.w = lb + 16384 + (16 * (g >> 1) + 4 * (g & 1) + (fr >> 2)) * (NC * 32) + (lane & 3) * 8 + m * 32; a1.w = a0.w + 32 * NC * 32;
#pragma unroll
    for (int k = 0; k < 3; ++k) { const int o = (NC - 3 + k + m >= NC) ? -NC * 32 : 0; a0.r[k] = a0.w + o; a1.r[k] = a1.w + o; }
    asm volatile("" : "+v"(a0.x), "+v"(a0.w), "+v"(a0.r[0]), "+v"(a0.r[1]), "+v"(a0.r[2]), "+v"(a1.x), "+v"(a1.w), "+v"(a1.r[0]), "+v"(a1.r[1]), "+v"(a1.r[2]));
    XF<NC> f0, f1;
    bf16x8 ones = {0x3F80, 0x3F80, 0x3F80, 0x3F80, 0x3F80, 0x3F80, 0x3F80, 0x3F80};
    asm volatile("" : "+v"(ones));
#pragma unroll
    for (int i = 0; i < R; ++i) xa_read1<NC>(f0, i, a0);
    int d = ((g0 & 3) == 3) ? -3 * SLOT : SLOT, pos = g0 & 3;
    asm volatile("s_nop 1"); SB0();
#pragma unroll 1
    for (int s = 0; s < NST - 1; ++s) {
        xa_step<NC, TR, XST, DA, true, true>(A, f0, f1, a1, a0, d, ones);
        pos = (pos + 1) & 3; d = pos == 3 ? -3 * SLOT : SLOT;
        WAIT_L0(); BAR(); SB0();
        hook(s);
        xa_step<NC, TR, XST, DB, true, false>(A, f1, f0, a0, a0, 0, ones);
    }
    xa_step<NC, TR, XST, DA, true, false>(A, f0, f1, a1, a1, 0, ones);
    WAIT_L0(); BAR(); SB0();
    xa_step<NC, TR, XST, 0, false, false>(A, f1, f0, a0, a0, 0, ones);
    MMA_DRAIN();
}
template <int NC, bool TR, bool XST> __device__ __forceinline__ void share_colsum(const XWAcc<NC, XST>& A, LAS char* lds, int m, int lane) {
    LAS float* tab = (LAS float*)(lds + SP_CS);
    const int fr = lane & 15, g = lane >> 4;
#pragma unroll
    for (int k = 0; k < (NC + 3) / 4; ++k) {
        int c = m + 4 * k; c = c >= NC ? c - NC : c;
        if (TR) { if (fr == 0) *(LAS f32x4*)(tab + c * 16 + 4 * g) = A.cs[k]; }
        else { if (g == 0) tab[c * 16 + fr] = A.cs[k][0]; }
    }
    WAIT_L0(); BAR();
}
template <int NC> __device__ __forceinline__ void unrotate(unsigned (&v)[NC][2], int m) {
    unsigned u[NC][2];
#define UNROT(M) _Pragma("unroll") for (int c = 0; c < NC; ++c) { u[(c + M) % NC][0] = v[c][0]; u[(c + M) % NC][1] = v[c][1]; }
    if (m == 0) { UNROT(0) } else if (m == 1) { UNROT(1) } else if (m == 2) { UNROT(2) } else { UNROT(3) }
#undef UNROT
#pragma unroll
    for (int c = 0; c < NC; ++c) { v[c][0] = u[c][0]; v[c][1] = u[c][1]; asm volatile("" : "+v"(v[c][0]), "+v"(v[c][1])); }
}

struct MFrag { bf16x8 w[2]; bf16x8 z[9]; };
template <unsigned MASK, int G0, int G1> __device__ __forceinline__ void m_part(f32x4 (&acc)[2][9], const MFrag& f, MFrag& n, LAS char* pW, LAS char* pZ) {
    int r = 0;
#pragma unroll
    for (int i = 0; i < G0; ++i) r += (MASK >> i) & 1;
#pragma unroll
    for (int i = G0; i < G1; ++i) {
        MMA(acc[i & 1][i >> 1], f.z[i >> 1], f.w[i & 1]);
        if ((MASK >> i) & 1) { if (r < 2) n.w[r] = lds_b128(pW + r * 2048); else n.z[r - 2] = lds_b128(pZ + (r - 2) * 1024); ++r; }
        SB0();
    }
}
#ifndef M_MASK_A
#define M_MASK_A 0x07FF
#define M_MASK_B 0x07FF
#define M_BPOS 0
#endif
__device__ __forceinline__ void main_gemm(f32x4 (&acc)[2][9], LAS char* lds, int g0, int m, int lane) {
    asm volatile("" : "+v"(lane));
    const int fr = lane & 15, g = lane >> 4, row = 32 * m + fr;
    const unsigned oW = row * 128 + ((g ^ ((row >> 1) & 7)) << 4);
    LAS char* pW0[4]; LAS char* pW1[4]; LAS char* pZ[4];
#pragma unroll
    for (int k = 0; k < 4; ++k) {
        const unsigned sb = ((g0 + k) & 3) * SLOT;
        pW0[k] = lds + sb + oW; pW1[k] = lds + sb + (oW ^ 64); pZ[k] = lds + sb + 16384 + lane * 16;
        asm volatile("" : "+v"(pW0[k]), "+v"(pW1[k]), "+v"(pZ[k]));
    }
#pragma unroll
    for (int t = 0; t < 2; ++t)
#pragma unroll
        for (int c = 0; c < 9; ++c) { acc[t][c] = (f32x4){0.f, 0.f, 0.f, 0.f}; asm volatile("" : "+v"(acc[t][c])); }
    MFrag f0, f1;
#pragma unroll
    for (int i = 0; i < 2; ++i) f0.w[i] = lds_b128(pW0[0] + i * 2048);
#pragma unroll
    for (int c = 0; c < 9; ++c) f0.z[c] = lds_b128(pZ[0] + c * 1024);
    asm volatile("s_nop 1"); SB0();
#define M_STAGE(K, KN) do { m_part<M_MASK_A, 0, 18>(acc, f0, f1, pW1[K], pZ[K] + 9216); m_part<0, 0, M_BPOS>(acc, f1, f0, pW0[KN], pZ[KN]); WAIT_L0(); BAR(); SB0(); m_part<M_MASK_B, M_BPOS, 18>(acc, f1, f0, pW0[KN], pZ[KN]); } while (0)
#pragma unroll 1
    for (int s = 0; s < 7; ++s) { M_STAGE(0, 1); M_STAGE(1, 2); M_STAGE(2, 3); M_STAGE(3, 0); }
    M_STAGE(0, 1); M_STAGE(1, 2); M_STAGE(2, 3);
#undef M_STAGE
    m_part<M_MASK_A, 0, 18>(acc, f0, f1, pW1[3], pZ[3] + 9216);
    WAIT_L0(); BAR(); SB0();
    m_part<0, 0, 18>(acc, f1, f0, pW0[0], pZ[0]);
    MMA_DRAIN();
}

struct EFrag { s16x4 w[2]; bf16x8 b[5]; };
template <bool LOAD, bool MM> __device__ __forceinline__ void e_step(f32x4 (&acc)[5], const EFrag& f, EFrag& n, LAS char* pW, LAS char* pB) {
    const bf16x8 w = cat8(f.w[0], f.w[1]);
#pragma unroll
    for (int t = 0; t < 5; ++t) {
        if (MM) MMA(acc[t], w, f.b[t]);
        if (LOAD) {
            if (t == 0) { n.w[0] = lds_tr1(pW); n.w[1] = lds_tr1(pW + 2048); }
            else if (t == 1) { n.b[0] = lds_b128(pB); n.b[1] = lds_b128(pB + 1024); }
            else n.b[t] = lds_b128(pB + t * 1024);
        }
        SB0();
    }
}
template <int NST, int NKS, bool MORE, class Hook> __device__ __forceinline__ void e_gemm(f32x4 (&acc)[5], LAS char* lds, int g0, int w, int lane, Hook hook) {
    asm volatile("" : "+v"(lane));
    const int g = lane >> 4, q = (lane & 15) >> 2;
    LAS char* pW = lds + (4 * g + q) * 128 + ((w ^ ((2 * g + (q >> 1)) & 3)) << 5) + (lane & 3) * 8;
    LAS char* pB = lds + 16384 + lane * 16;
#pragma unroll
    for (int t = 0; t < 5; ++t) { acc[t] = (f32x4){0.f, 0.f, 0.f, 0.f}; asm volatile("" : "+v"(acc[t])); }
    EFrag f0, f1;
    e_step<true, false>(acc, f1, f0, pW + (g0 & 3) * SLOT, pB + (g0 & 3) * SLOT);
    asm volatile("s_nop 1"); SB0();
#pragma unroll 1
    for (int s = 0; s < NST - 1; ++s) {
        const unsigned sb = ((g0 + s) & 3) * SLOT, sn = ((g0 + s + 1) & 3) * SLOT;
        e_step<true, true>(acc, f0, f1, pW + sb + 4096, pB + sb + 5120);
        e_step<true, true>(acc, f1, f0, pW + sb + 8192, pB + sb + 10240);
        e_step<true, true>(acc, f0, f1, pW + sb + 12288, pB + sb + 15360);
        WAIT_L0(); BAR(); SB0();
        hook(s);
        e_step<true, true>(acc, f1, f0, pW + sn, pB + sn);
    }
    const unsigned sb = ((g0 + NST - 1) & 3) * SLOT;
    constexpr int LAST = NKS - 4 * (NST - 1);
    e_step<true, true>(acc, f0, f1, pW + sb + 4096, pB + sb + 5120);
    if (LAST == 4) {
        e_step<true, true>(acc, f1, f0, pW + sb + 8192, pB + sb + 10240);
        e_step<true, true>(acc, f0, f1, pW + sb + 12288, pB + sb + 15360);
    }
    if (MORE) { WAIT_L0(); BAR(); SB0(); }
    e_step<false, true>(acc, f1, f0, pW, pB);
    MMA_DRAIN();
}

__device__ __forceinline__ float dot2(unsigned a, unsigned b, float c) { return __builtin_amdgcn_fdot2_f32_bf16(__builtin_bit_cast(bf16x2_t, a), __builtin_bit_cast(bf16x2_t, b), c, false); }
__device__ __forceinline__ unsigned at_region(int r, int p) { return (unsigned)(((r >> 1) ? 0 : 3 * SLOT) + ((r & 1) ? 16384 + p * 5120 : p * 4096)); }
__device__ __forceinline__ unsigned at_row(int rho, int p) { const int r = rho / 14; return at_region(r, p) + (unsigned)(rho - r * 14) * 288u; }
__device__ __forceinline__ void attn_issue(rsrc_t rQ, LAS char* lds, int n, int p, int lane, int half) {
#pragma unroll
    for (int rr = 0; rr < 2; ++rr)
#pragma unroll
        for (int q = 0; q < 4; ++q) {
            const int r = 2 * half + rr, c = q * 64 + lane, lr = c / 18, cc = c - lr * 18, rho = r * 14 + lr;
            int t, seg;
            if (rho < 22) { t = 8 * n - 14 + rho; seg = 1; } else if (rho < 44) { t = 8 * n - 14 + (rho - 22); seg = 2; } else { t = 8 * n + (rho - 44); seg = 0; }
            const unsigned voff = (t < 0 || rho >= 52 || lr >= 14) ? 0x7ffffff0u : (unsigned)(t * (QW * 2) + (seg * D_ + 144 * p) * 2 + cc * 16);
            dma16_dev(rQ, lds + at_region(r, p) + q * 1024, voff, 0);
        }
}
__device__ __forceinline__ void attn_compute(LAS char* lds, rsrc_t rO, int n, int p, int lane) {
    const int i = lane >> 3, hh = lane & 7, t = 8 * n + i;
    const unsigned hb = hh * 36, r0 = at_region(0, p) + hb, r1 = at_region(1, p) + hb - 14 * 288, r2 = at_region(2, p) + hb - 28 * 288, r3 = at_region(3, p) + hb - 42 * 288;
    unsigned qv[9];
    {
        LAS unsigned* q = (LAS unsigned*)(lds + r3 + (44 + i) * 288);
#pragma unroll
        for (int d = 0; d < 9; ++d) qv[d] = q[d];
    }
    float lg[8];
#pragma unroll
    for (int w = 0; w < 8; ++w) {
        const int rho = 14 + i - 2 * w;
        LAS unsigned* kp = (LAS unsigned*)(lds + (rho < 14 ? r0 : r1) + rho * 288);
        float s = 0.f;
#pragma unroll
        for (int d = 0; d < 9; ++d) s = dot2(qv[d], kp[d], s);
        lg[w] = (t - 2 * w >= 0) ? s * (0.23570226039551584f * 1.4426950408889634f) : -1e9f;
    }
    float mx = fmaxf(fmaxf(fmaxf(lg[0], lg[1]), fmaxf(lg[2], lg[3])), fmaxf(fmaxf(lg[4], lg[5]), fmaxf(lg[6], lg[7])));
    float sum = 0.f;
#pragma unroll
    for (int w = 0; w < 8; ++w) { lg[w] = __builtin_amdgcn_exp2f(lg[w] - mx); sum += lg[w]; }
    const float inv = __builtin_amdgcn_rcpf(sum);
    unsigned pp[4];
#pragma unroll
    for (int w = 0; w < 4; ++w) pp[w] = pack_bf16(lg[2 * w] * inv, lg[2 * w + 1] * inv);
    float acc[18];
#pragma unroll
    for (int d = 0; d < 18; ++d) acc[d] = 0.f;
#pragma unroll
    for (int w = 0; w < 4; ++w) {
        const int ra = 22 + 14 + i - 4 * w, rb = ra - 2;
        LAS unsigned* va = (LAS unsigned*)(lds + (ra < 28 ? r1 : ra < 42 ? r2 : r3) + ra * 288);
        LAS unsigned* vb = (LAS unsigned*)(lds + (rb < 28 ? r1 : rb < 42 ? r2 : r3) + rb * 288);
#pragma unroll
        for (int d = 0; d < 9; ++d) {
            const unsigned x = va[d], y = vb[d];
            acc[2 * d] = dot2(__builtin_amdgcn_perm(y, x, 0x05040100u), pp[w], acc[2 * d]);
            acc[2 * d + 1] = dot2(__builtin_amdgcn_perm(y, x, 0x07060302u), pp[w], acc[2 * d + 1]);
        }
    }
    LAS char* ob = lds + SP_AO + p * 2304;
#pragma unroll
    for (int d = 0; d < 9; ++d) *(LAS unsigned*)(ob + i * 288 + hh * 36 + d * 4) = pack_bf16(acc[2 * d], acc[2 * d + 1]);
    WAIT_L0();
    {
        const int r = lane & 7, g = (lane >> 3) & 3, tt = n >> 1, rl = (8 * n + r) & 15;
#pragma unroll
        for (int q = 0; q < 2; ++q) {
            const int a = (lane >> 5) + 2 * q;
            const u32x2 lo = *(LAS u32x2*)(ob + r * 288 + (32 * a + 4 * g) * 2), hi = *(LAS u32x2*)(ob + r * 288 + (32 * a + 16 + 4 * g) * 2);
            st128_dev(rO, ((tt * 18 + 4 * p + a) * 64 + rl + 16 * g) * 16, (u32x4){lo.x, lo.y, hi.x, hi.y});
        }
        const u32x2 v = *(LAS u32x2*)(ob + r * 288 + (128 + 4 * g) * 2);
        if (lane < 32) st64_dev(rO, ((tt * 18 + 16 + (p >> 1)) * 64 + rl + 16 * g) * 16 + (p & 1) * 8, v);
    }
}

__global__ __launch_bounds__(512, 2) void fused_kernel(Args a) {
    asm volatile("fusedk_pf_begin:");
    extern __shared__ __attribute__((aligned(16))) char smem[];
    LAS char* lds = (LAS char*)smem;
    const int wid = __builtin_amdgcn_readfirstlane(threadIdx.x >> 6), lane = threadIdx.x & 63;
    const int xcd = blockIdx.x & 7, loc = blockIdx.x >> 3;
    unsigned pf_dummy = 0;
    if (wid == 0) {
        u64 base; unsigned sz, back;
        asm volatile("s_getpc_b64 %0\nfusedk_pf_here:\n\ts_mov_b32 %1, fusedk_pf_end-fusedk_pf_begin\n\ts_mov_b32 %2, fusedk_pf_here-fusedk_pf_begin" : "=s"(base), "=s"(sz), "=s"(back));
        base -= back;
        const unsigned off = (unsigned)(loc * 13 + lane) * 128u;
        if (lane < 13 && (int)off < (int)sz) asm volatile("global_load_dword %0, %1, off" : "=v"(pf_dummy) : "v"(base + off));
    }
    const int ti = (xcd & 1) * 8 + (loc & 7), tj = (xcd >> 1) * 4 + (loc >> 3);
    const int nt = xcd * 30 + loc;
    const bool fin = loc < 30 && nt < 234;
    const int rb = nt / 9, cb = nt - rb * 9;
    {
        RowsImg xi0; xi0.init(lane, D_ * 2);
        const rsrc_t rx = mk_rsrc(a.x, S_ * D_ * 2);
        const unsigned so = (unsigned)(ti * 128 + 16 * wid) * (D_ * 2);
        dma16(rx, lds + wid * 2048, xi0.v0, so); dma16(rx, lds + wid * 2048 + 1024, xi0.v1, so + 8 * (D_ * 2));
        dma16(rx, lds + SLOT + wid * 2048, xi0.v0, so + 128); dma16(rx, lds + SLOT + wid * 2048 + 1024, xi0.v1, so + 8 * (D_ * 2) + 128);
    }
    char* ctl = a.ws + WS_CTL;
    if (lane < 2) __builtin_amdgcn_raw_ptr_buffer_load_lds(mk_rsrc(ctl, 4096), (lds_vp)(lds + SP_EP + wid * 64), 4, lane * 4 + (unsigned)CTL_EP, 0, 0, 16);
#define TAG_FROM_EPOCH() \
    u64 ep_; asm volatile("ds_read_b64 %0, %1\n\ts_waitcnt lgkmcnt(0)" : "=&v"(ep_) : "v"((unsigned)(size_t)(lds + SP_EP + wid * 64)) : "memory"); \
    const u64 tg_ = ep_ * 0x9E3779B97F4A7C15ull + 0xD1B54A32D192ED03ull; \
    const u64 tag = ((u64)(unsigned)__builtin_amdgcn_readfirstlane((unsigned)(tg_ >> 32)) << 32) | (unsigned)__builtin_amdgcn_readfirstlane((unsigned)tg_)
    const int an = ti * 16 + tj;

    if (__builtin_expect(wid < 4, 0)) {
    const int m = wid, fr = lane & 15, g4 = lane >> 4;
    ST_DECL; ST(0);
    const rsrc_t rQs = mk_rsrc(a.ws + WS_QKV, S_ * QW * 2), rZs = mk_rsrc(a.ws + WS_Z2T, S_ * FH * 2), rHs = mk_rsrc(a.ws + WS_HF, S_ * FH * 2), rSt = mk_rsrc(a.ws + WS_ST, S_ * 16 * 16);
    LAS float* cst = (LAS float*)(lds + SP_CS);
    float xm[2], xr[2];
    unsigned pf1_dummy = 0;
    {
        const unsigned W = blockIdx.x * 4 + m;
        const bool isx = lane < 18;
        const unsigned ln = isx ? W * 18 + lane : W * 16 + (lane - 18);
        const char* src = isx ? (const char*)a.x : (const char*)a.wqkv;
        const unsigned lines = isx ? S_ * D_ * 2 / 128 : D_ * QW * 2 / 128;
        if (lane < 34 && ln < lines) asm volatile("global_load_dword %0, %1, off" : "=v"(pf1_dummy) : "v"(src + (size_t)ln * 128));
    }
    asm volatile("s_waitcnt vmcnt(1)" : "+v"(pf_dummy) :: "memory");
    TAG_FROM_EPOCH();
    BAR();
    ST(1);
    {
        XWAcc<7, true> A; xw_zero(A);
        xw_gemm<7, true, true, 9>(A, lds, G_QKV, m, lane, NoHook());
        ST(2);
        asm volatile("s_waitcnt vmcnt(0)" : "+v"(pf1_dummy) :: "memory");
        share_colsum<7, true, true>(A, lds, m, lane);
#pragma unroll
        for (int t = 0; t < 2; ++t) {
            const int r3 = fr & 3;
            const float dq = r3 == 0 ? A.sq[t][0] : r3 == 1 ? A.sq[t][1] : r3 == 2 ? A.sq[t][2] : A.sq[t][3];
            const float sq = __shfl(dq, (fr >> 2) * 16 + fr);
            xm[t] = A.sx[t][0] * (1.0f / D_);
            xr[t] = __builtin_amdgcn_rsqf(fmaxf(sq * (1.0f / D_) - xm[t] * xm[t], 0.f) + 1e-5f);
        }
        const int c0 = (tj * 27) >> 2, dup = tj > 0 && c0 < (((tj - 1) * 27) >> 2) + 7;
#pragma unroll
        for (int t = 0; t < 2; ++t) {
            unsigned pk[7][2];
#pragma unroll
            for (int c = 0; c < 7; ++c) {
                const int tile = c + m >= 7 ? c + m - 7 : c + m;
                const f32x4 cs = *(LAS f32x4*)(cst + tile * 16 + 4 * g4);
                pk[c][0] = pack2((lo2(A.a[t][c]) - lo2(cs) * xm[t]) * xr[t]); pk[c][1] = pack2((hi2(A.a[t][c]) - hi2(cs) * xm[t]) * xr[t]);
            }
            unrotate<7>(pk, m);
            const unsigned ro = (unsigned)(ti * 128 + 32 * m + 16 * t + fr) * (QW * 2) + c0 * 32;
#pragma unroll
            for (int pp = 0; pp < 3; ++pp) st128_dev(rQs, ro + (1 + 2 * pp) * 32 + g4 * 16, gather8(pk[1 + 2 * pp][0], pk[1 + 2 * pp][1], pk[2 + 2 * pp][0], pk[2 + 2 * pp][1]));
            if (!dup) st64_dev(rQs, ro + g4 * 8, (u32x2){pk[0][0], pk[0][1]});
        }
    }
    ST(3);
    unsigned zp[2][9][2];
    {
        XWAcc<9, false> A; xw_zero(A);
        xw_gemm<9, false, false, 9>(A, lds, G_Z2, m, lane, [&](int s) { if (s == 4) publish(ctl, CTL_QF + ((ti * 16 + tj) * 4 + m) * 8, tag, lane); });
        ST(4);
        share_colsum<9, false, false>(A, lds, m, lane);
        f32x2 rm[2][2], rr[2][2];
#pragma unroll
        for (int t = 0; t < 2; ++t)
#pragma unroll
            for (int h = 0; h < 2; ++h) {
                rm[t][h] = (f32x2){__shfl(xm[t], 4 * g4 + 2 * h), __shfl(xm[t], 4 * g4 + 2 * h + 1)};
                rr[t][h] = (f32x2){__shfl(xr[t], 4 * g4 + 2 * h), __shfl(xr[t], 4 * g4 + 2 * h + 1)};
            }
        f32x2 s1[2][2], s2[2][2];
#pragma unroll
        for (int t = 0; t < 2; ++t)
#pragma unroll
            for (int h = 0; h < 2; ++h) { s1[t][h] = (f32x2){0.f, 0.f}; s2[t][h] = (f32x2){0.f, 0.f}; }
#pragma unroll
        for (int c = 0; c < 9; ++c) {
            const float cs = cst[(c + m >= 9 ? c + m - 9 : c + m) * 16 + fr];
#pragma unroll
            for (int t = 0; t < 2; ++t)
#pragma unroll
                for (int h = 0; h < 2; ++h) {
                    const f32x2 v = gelu2(((h ? hi2(A.a[t][c]) : lo2(A.a[t][c])) - rm[t][h] * cs) * rr[t][h]);
                    s1[t][h] += v; s2[t][h] += v * v;
                    zp[t][c][h] = pack2(v);
                    asm volatile("" : "+v"(zp[t][c][h]));
                }
        }
        float mm = 0.f, qq = 0.f;
        {
            const int idx = lane & 7;
            float a1[8], a2[8];
#pragma unroll
            for (int k = 0; k < 8; ++k) { a1[k] = s1[k >> 2][(k >> 1) & 1][k & 1]; a2[k] = s2[k >> 2][(k >> 1) & 1][k & 1]; }
#pragma unroll
            for (int k = 0; k < 8; ++k) { a1[k] = dpp_add<0x128>(a1[k]); a2[k] = dpp_add<0x128>(a2[k]); }
#pragma unroll
            for (int k = 0; k < 8; ++k) { a1[k] = dpp_add<0x124>(a1[k]); a2[k] = dpp_add<0x124>(a2[k]); }
#pragma unroll
            for (int k = 0; k < 8; ++k) { a1[k] = dpp_add<0x122>(a1[k]); a2[k] = dpp_add<0x122>(a2[k]); }
#pragma unroll
            for (int k = 0; k < 8; ++k) { a1[k] = dpp_add<0x121>(a1[k]); a2[k] = dpp_add<0x121>(a2[k]); }
            float u1 = a1[0], u2 = a2[0];
#pragma unroll
            for (int k = 1; k < 8; ++k) { u1 = idx == k ? a1[k] : u1; u2 = idx == k ? a2[k] : u2; }
            mm = u1 * (1.0f / 144.0f);
            qq = fmaxf(u2 - u1 * mm, 0.f);
            if (fr < 8) st128_dev(rSt, ((ti * 16 + tj) * 128 + 32 * m + 16 * (idx >> 2) + 4 * g4 + (idx & 3)) * 16,
                                  (u32x4){__float_as_uint(mm), (unsigned)tag, __float_as_uint(qq), (unsigned)(tag >> 32)});
        }
    }
    ST(5);
    unsigned z1p[2][9][2];
    {
        XWAcc<9, false> A; xw_zero(A);
        xw_gemm<9, true, false, Z1A>(A, lds, G_Z1, m, lane, NoHook());
        ST(6);
        float mu, rs;
        {
            const unsigned off = ((ti * 16 + (lane >> 5) * 8) * 128 + 32 * m + (lane & 31)) * 16;
            u32x4 v[8]; unsigned spins = 0;
            for (;;) {
#pragma unroll
                for (int k = 0; k < 8; ++k) v[k] = __builtin_amdgcn_raw_buffer_load_b128(rSt, off + k * 2048, 0, 16);
                unsigned bad = 0;
#pragma unroll
                for (int k = 0; k < 8; ++k) bad |= (v[k].y ^ (unsigned)tag) | (v[k].w ^ (unsigned)(tag >> 32));
                if (__all(bad == 0) || ++spins > SPIN_CAP) break;
                __builtin_amdgcn_s_sleep(2);
            }
            float s = 0.f;
#pragma unroll
            for (int k = 0; k < 8; ++k) s += __uint_as_float(v[k].x);
            s += xchg32(s);
            mu = s * (1.0f / 16.0f);
            float q = 0.f;
#pragma unroll
            for (int k = 0; k < 8; ++k) { const float d = __uint_as_float(v[k].x) - mu; q += __uint_as_float(v[k].z) + 144.0f * d * d; }
            q += xchg32(q);
            rs = __builtin_amdgcn_rsqf(q * (1.0f / FH) + 1e-5f);
        }
        ST(7);
        f32x2 mr[2][2], rr[2][2];
#pragma unroll
        for (int t = 0; t < 2; ++t)
#pragma unroll
            for (int h = 0; h < 2; ++h) {
                const int src = 16 * t + 4 * g4 + 2 * h;
                mr[t][h] = (f32x2){__shfl(mu, src), __shfl(mu, src + 1)}; rr[t][h] = (f32x2){__shfl(rs, src), __shfl(rs, src + 1)};
            }
        const unsigned zo = (unsigned)((tj * 64 + ti * 4 + m) * 9) * 1024 + lane * 16;
#pragma unroll
        for (int c = 0; c < 9; ++c) {
            const unsigned x0 = pack2((unpack2(zp[0][c][0]) - mr[0][0]) * rr[0][0]), x1 = pack2((unpack2(zp[0][c][1]) - mr[0][1]) * rr[0][1]);
            const unsigned y0 = pack2((unpack2(zp[1][c][0]) - mr[1][0]) * rr[1][0]), y1 = pack2((unpack2(zp[1][c][1]) - mr[1][1]) * rr[1][1]);
            st128_dev(rZs, zo + (c + m >= 9 ? c + m - 9 : c + m) * 1024, gather8(x0, x1, y0, y1));
        }
        ST(8);
        xw_gemm<9, true, false, 9 - Z1A>(A, lds, G_Z1 + Z1A, m, lane, [&](int s) { if (s == 1) publish(ctl, CTL_ZF + ((tj * 16 + ti) * 4 + m) * 8, tag, lane); });
        ST(9);
        share_colsum<9, true, false>(A, lds, m, lane);
#pragma unroll
        for (int c = 0; c < 9; ++c) {
            const f32x4 cs = *(LAS f32x4*)(cst + (c + m >= 9 ? c + m - 9 : c + m) * 16 + 4 * g4);
#pragma unroll
            for (int t = 0; t < 2; ++t) {
                z1p[t][c][0] = pack2(gelu2((lo2(A.a[t][c]) - lo2(cs) * xm[t]) * xr[t])); z1p[t][c][1] = pack2(gelu2((hi2(A.a[t][c]) - hi2(cs) * xm[t]) * xr[t]));
                asm volatile("" : "+v"(z1p[t][c][0]), "+v"(z1p[t][c][1]));
            }
        }
        unrotate<9>(z1p[0], m); unrotate<9>(z1p[1], m);
    }
    ST(10);
    BAR();
    {
        f32x4 acc[2][9];
        float bt[2];
#pragma unroll
        for (int t = 0; t < 2; ++t) bt[t] = bf2f(a.bsp[ti * 128 + 32 * m + 16 * t + fr]);
        main_gemm(acc, lds, G_MAIN, m, lane);
        ST(11);
#pragma unroll
        for (int t = 0; t < 2; ++t) {
            const unsigned hp = (unsigned)(ti * 8 + 2 * m + t) * 72 * 1024 + lane * 16;
#pragma unroll
            for (int pp = 0; pp < 4; ++pp) {
                u32x4 o;
                o.x = pack2((lo2(acc[t][2 * pp]) + bt[t]) * unpack2(z1p[t][2 * pp][0])); o.y = pack2((hi2(acc[t][2 * pp]) + bt[t]) * unpack2(z1p[t][2 * pp][1]));
                o.z = pack2((lo2(acc[t][2 * pp + 1]) + bt[t]) * unpack2(z1p[t][2 * pp + 1][0])); o.w = pack2((hi2(acc[t][2 * pp + 1]) + bt[t]) * unpack2(z1p[t][2 * pp + 1][1]));
                st128_dev(rHs, hp + (tj * 4 + pp) * 1024, o);
            }
            u32x2 o;
            o.x = pack2((lo2(acc[t][8]) + bt[t]) * unpack2(z1p[t][8][0])); o.y = pack2((hi2(acc[t][8]) + bt[t]) * unpack2(z1p[t][8][1]));
            st64_dev(rHs, hp + (64 + (tj >> 1)) * 1024 + (tj & 1) * 8, o);
        }
    }
    ST(12);
    publish(ctl, CTL_HF + ((ti * 16 + tj) * 4 + m) * 8, tag, lane);
    ST(13);
    if (!fin) { ST_END(0); return; }
    {
        f32x4 sa[5], ga[5];
        u32x2 xv[5];
        const int fr = lane & 15, g4 = lane >> 4;
#pragma unroll
        for (int t = 0; t < 5; ++t) xv[t] = *(const u32x2*)(a.x + (size_t)(min(rb * 5 + t, 127) * 16 + fr) * D_ + cb * 64 + m * 16 + g4 * 4);
        BAR();
        ST(14);
        e_gemm<18, 72, true>(ga, lds, G_G, m, lane, NoHook());
        ST(15);
        e_gemm<5, 18, false>(sa, lds, G_S, m, lane, NoHook());
        ST(16);
#pragma unroll
        for (int t = 0; t < 5; ++t) {
            if (rb * 5 + t < 128) {
                float o[4];
#pragma unroll
                for (int r = 0; r < 4; ++r) {
                    const float gv = ga[t][r], sg = gv * __builtin_amdgcn_rcpf(1.0f + __builtin_amdgcn_exp2f(gv * -1.4426950408889634f));
                    const float xx = (r & 1) ? bfhi(xv[t][r >> 1]) : bflo(xv[t][r >> 1]);
                    o[r] = xx + sa[t][r] * sg;
                }
                u32x2 ov; ov.x = pack_bf16(o[0], o[1]); ov.y = pack_bf16(o[2], o[3]);
                *(u32x2*)(a.out + (size_t)((rb * 5 + t) * 16 + fr) * D_ + cb * 64 + m * 16 + g4 * 4) = ov;
            }
        }
    }
    if (blockIdx.x == 0 && threadIdx.x == 0) __hip_atomic_store((GAS u64*)(ctl + CTL_EP), tag, __ATOMIC_RELAXED, __HIP_MEMORY_SCOPE_AGENT);
    ST(17);
    ST_END(0);
    asm volatile("fusedk_pf_end:");
        return;
    }
    {

        const int p = wid - 4;
        ST_DECL; ST(0);
#define rX mk_rsrc(a.x, S_ * D_ * 2)
#define rQW mk_rsrc(a.wqkv, D_ * QW * 2)
#define rW mk_rsrc(a.win, D_ * F_ * 2)
#define rS mk_rsrc(a.wsp, S_ * S_ * 2)
#define rZ mk_rsrc(a.ws + WS_Z2T, S_ * FH * 2)
#define rQ mk_rsrc(a.ws + WS_QKV, S_ * QW * 2)
#define rO mk_rsrc(a.ws + WS_OF, S_ * D_ * 2)
        RowsImg xi; xi.init(lane, D_ * 2);
        RowsImg si; si.init(lane, S_ * 2);
        WImg<7> wq; wq.init(lane, p, QW * 2);
        WImg<9> wz; wz.init(lane, p, F_ * 2);
        const unsigned sX0 = (unsigned)(ti * 128) * (D_ * 2), sQ = (unsigned)((tj * 27) >> 2) * 32, sZ1 = (unsigned)(tj * 144) * 2, sZ2 = sZ1 + FH * 2;
        const unsigned sS = (unsigned)(ti * 128 + 32 * p) * (S_ * 2), sZ = (unsigned)tj * (64u * 9216u);
        const unsigned vl = lane * 16;
        const unsigned vE = (lane >> 3) * (D_ * 2) + ((((lane & 7) >> 1) ^ ((lane >> 4) & 3)) << 5) + (lane & 1) * 16;
        const int zI0 = WImg<9>::first(p);
        auto iss_q = [&](int g) {
            LAS char* slot = lds + (g & 3) * SLOT;
            xi.issue35(rX, slot, sX0 + g * 128, D_ * 2, p); wq.issue(rQW, slot + 16384, sQ + g * 64 * (QW * 2), p);
        };
        auto iss_z = [&](int g, int s, unsigned col) {
            LAS char* slot = lds + (g & 3) * SLOT;
            xi.issue35(rX, slot, sX0 + s * 128, D_ * 2, p);
            wz.issue(rW, slot + 16384, col + s * 64 * (F_ * 2), p);
        };
        auto iss_m = [&](int g) {
            LAS char* slot = lds + (g & 3) * SLOT; const int s = g - G_MAIN;
            si.issue(rS, slot + p * 4096, sS + s * 128, S_ * 2);
#pragma unroll
            for (int q = 0; q < 4; ++q) dma16_dev(rZ, slot + 16384 + (zI0 + q) * 1024, vl, sZ + s * 18432 + (zI0 + q) * 1024);
            if (p < 2) dma16_dev(rZ, slot + 16384 + (zI0 + 4) * 1024, vl, sZ + s * 18432 + (zI0 + 4) * 1024);
        };
        unsigned eH[5], eO[5];
#pragma unroll
        for (int t = 0; t < 5; ++t) { const unsigned tt = (unsigned)__builtin_amdgcn_readfirstlane(min(rb * 5 + t, 127)); eH[t] = tt * 73728u + p * 1024u; eO[t] = tt * 18432u + p * 1024u; }
        auto iss_ew = [&](int g, rsrc_t rw, unsigned w0, unsigned w1) {
            LAS char* slot = lds + (g & 3) * SLOT;
            dma16(rw, slot + p * 4096, vE, w0); dma16(rw, slot + p * 4096 + 1024, vE, w0 + 8 * (D_ * 2));
            dma16(rw, slot + p * 4096 + 2048, vE, w1); dma16(rw, slot + p * 4096 + 3072, vE, w1 + 8 * (D_ * 2));
        };
        auto iss_eb = [&](int g, rsrc_t rb_, const unsigned (&eb)[5], unsigned bo) {
            LAS char* slot = lds + (g & 3) * SLOT;
#pragma unroll
            for (int t = 0; t < 5; ++t) dma16_dev(rb_, slot + 16384 + p * 5120 + t * 1024, vl, eb[t] + bo);
        };
        auto iss_e = [&](int g, rsrc_t rw, rsrc_t rb_, const unsigned (&eb)[5], unsigned w0, unsigned w1, unsigned bo) { iss_ew(g, rw, w0, w1); iss_eb(g, rb_, eb, bo); };
        constexpr unsigned TILE_B = 16 * D_ * 2;
        const unsigned wcol = cb * 128;
        auto iss_g = [&](int s) {
            const unsigned c0 = s < 16 ? 9 * s + 2 * p : 18 * (4 * (s - 16) + p) + 8, c1 = s < 16 ? c0 + 1 : c0 + 9;
            iss_e(G_G + s, mk_rsrc(a.wout, FH * D_ * 2), mk_rsrc(a.ws + WS_HF, S_ * FH * 2), eH, c0 * TILE_B + wcol, c1 * TILE_B + wcol, s * 4096);
        };
        auto iss_s = [&](int s) {
            const int pp = s == 4 && p > 1 ? 1 : p;
            const unsigned c0 = s < 4 ? 9 * s + 2 * p : 18 * pp + 8, c1 = s < 4 ? c0 + 1 : c0 + 9;
            iss_e(G_S + s, mk_rsrc(a.wo, D_ * D_ * 2), rO, eO, c0 * TILE_B + wcol, c1 * TILE_B + wcol, s * 4096 + (pp - p) * 1024);
        };
#define PSTEP(K, PH) do { WAIT_V(7 * (K)); BAR(); } while (0)
#define PW(N, PH) do { WAIT_V(N); BAR(); } while (0)
#define SPIN_UNTIL(cond) do { unsigned spins_ = 0; while (!(cond) && ++spins_ < SPIN_CAP) __builtin_amdgcn_s_sleep(4); asm volatile("" ::: "memory"); } while (0)
        wq.issue(rQW, lds + 16384, sQ, p); wq.issue(rQW, lds + SLOT + 16384, sQ + 64 * (QW * 2), p);
        iss_q(2); iss_q(3);
        WAIT_V(20);
        TAG_FROM_EPOCH();
        WAIT_V(14); BAR();
#pragma unroll 1
        for (int g = 1; g <= 5; ++g) { PW(14, 0); iss_q(g + 3); if (g == 2) touch_lines(rW, lds + SP_PF + wid * 256, (blockIdx.x * 4 + p) * 41, 41, lane); }
#pragma unroll 1
        for (int g = 6; g <= 7; ++g) { PSTEP(2, 1); iss_z(g + 3, g + 3 - G_Z2, sZ2); }
#pragma unroll 1
        for (int g = 8; g <= 14; ++g) { PW(16, 1); if (g == G_Z2) BAR(); iss_z(g + 3, g + 3 - G_Z2, sZ2); }
        PW(16, 1); iss_z(18, 0, sZ1);
        PW(16, 1); iss_z(19, 1, sZ1);
        PW(16, 1); iss_z(20, 2, sZ1);
        PW(16, 2);
        BAR();
        iss_z(21, 3, sZ1);
        {
            const unsigned W = blockIdx.x * 4 + p;
            touch_lines(rS, lds + SP_PF + wid * 256, W * 64, 64, lane);
            touch_lines(mk_rsrc(a.wout, FH * D_ * 2), lds + SP_PF + wid * 256, W * 21, 21, lane);
            touch_lines(mk_rsrc(a.wo, D_ * D_ * 2), lds + SP_PF + wid * 256, W * 6, 6, lane);
        }
#pragma unroll 1
        for (int g = 19; g <= 23; ++g) { PW(16, 2); iss_z(g + 3, g + 3 - G_Z1, sZ1); }
        PSTEP(2, 2); PSTEP(1, 2); PSTEP(0, 2);
        BAR(); BAR();
        SPIN_UNTIL(flags_ready(ctl + CTL_ZF + tj * 512, 64, ctl, 0, tag, lane));
        iss_m(27); iss_m(28); iss_m(29); iss_m(30);
        {
            const int pa = (8 * an - 14) >> 7;
            SPIN_UNTIL(flags_ready(ctl + CTL_QF + ti * 512, 64, ctl + CTL_QF + (pa < 0 ? ti : pa) * 512, 64, tag, lane));
        }
        BAR();
#pragma unroll 1
        for (int g = 28; g <= 55; ++g) { PW(16, 3); iss_m(g + 3); }
        PSTEP(2, 3); attn_issue(rQ, lds, an, p, lane, 0);
        PSTEP(1, 3); attn_issue(rQ, lds, an, p, lane, 1);
        PSTEP(1, 3);
        BAR();
        ST(1);
        WAIT_V(0);
        attn_compute(lds, rO, an, p, lane);
        ST(2);
        if (!fin) publish(ctl, CTL_OF + (an * 4 + p) * 8, tag, lane);
        else {
            {
                const int p0 = (rb * 80) >> 7, p1 = min(rb * 80 + 79, S_ - 1) >> 7;
                WAIT_V(0);
                if (lane == 0) __hip_atomic_store((GAS u64*)(ctl + CTL_OF + (an * 4 + p) * 8), tag, __ATOMIC_RELAXED, __HIP_MEMORY_SCOPE_AGENT);
#pragma unroll
                for (int s4 = 0; s4 < 4; ++s4) iss_ew(G_G + s4, mk_rsrc(a.wout, FH * D_ * 2), (9 * s4 + 2 * p) * TILE_B + wcol, (9 * s4 + 2 * p + 1) * TILE_B + wcol);
                SPIN_UNTIL(flags_ready(ctl + CTL_HF + p0 * 512, 64, ctl + CTL_HF + p1 * 512, 64, tag, lane));
                SPIN_UNTIL(flags_ready(ctl + CTL_OF + rb * 320, rb == 25 ? 24 : 40, ctl, 0, tag, lane));
            }
            ST(3);
            {
                const rsrc_t rh = mk_rsrc(a.ws + WS_HF, S_ * FH * 2);
                iss_eb(G_G, rh, eH, 0); iss_eb(G_G + 1, rh, eH, 4096); iss_eb(G_G + 2, rh, eH, 8192); iss_eb(G_G + 3, rh, eH, 12288);
            }
            PSTEP(2, 5);
            PW(10, 5); iss_g(4);
            PW(14, 5); iss_g(5);
            {
                const rsrc_t rw = mk_rsrc(a.wout, FH * D_ * 2), rh = mk_rsrc(a.ws + WS_HF, S_ * FH * 2);
                unsigned w0 = (54 + 2 * p) * TILE_B + wcol, bo = 6 * 4096;
#pragma unroll 1
                for (int g = 62; g <= 71; ++g) {
                    PW(18, 5);
                    iss_e(g + 3, rw, rh, eH, w0, w0 + TILE_B, bo);
                    w0 += 9 * TILE_B; bo += 4096;
                }
            }
            PW(18, 5); iss_g(16);
            PW(18, 5); iss_g(17);
            PW(18, 4); iss_s(0);
            PW(18, 4); iss_s(1);
            PW(18, 4); iss_s(2);
            PW(18, 4); iss_s(3);
            PW(18, 4); iss_s(4);
            PSTEP(2, 4); PSTEP(1, 4); PSTEP(0, 4);
        }
        ST_END(1);
        return;
        }
}

extern "C" void kernel_launch(void* const* d_in, const int* in_sizes, int n_in, void* d_out, int out_size, void* d_ws, size_t ws_size, hipStream_t stream) {
    Args a;
    a.x = (const u16*)d_in[0]; a.wqkv = (const u16*)d_in[1]; a.wo = (const u16*)d_in[2]; a.win = (const u16*)d_in[4]; a.wsp = (const u16*)d_in[5];
    a.bsp = (const u16*)d_in[6]; a.wout = (const u16*)d_in[7]; a.out = (u16*)d_out; a.ws = (char*)d_ws;
    (void)hipFuncSetAttribute((const void*)fused_kernel, hipFuncAttributeMaxDynamicSharedMemorySize, LDS_BYTES);
    fused_kernel<<<256, 512, LDS_BYTES, stream>>>(a);
}
```

```cpp
#include <hip/hip_runtime.h>
#include <hip/hip_bf16.h>
#include <stdint.h>

typedef unsigned short u16;
typedef unsigned long long u64;
typedef short bf16x8 __attribute__((ext_vector_type(8)));
typedef short s16x4 __attribute__((ext_vector_type(4)));
typedef float f32x4 __attribute__((ext_vector_type(4)));
typedef float f32x2 __attribute__((ext_vector_type(2)));
typedef unsigned u32x4 __attribute__((ext_vector_type(4)));
typedef unsigned u32x2 __attribute__((ext_vector_type(2)));
typedef __bf16 bf16x2_t __attribute__((ext_vector_type(2)));
#define LAS __attribute__((address_space(3)))
#define GAS __attribute__((address_space(1)))
typedef LAS void* lds_vp;
typedef __amdgpu_buffer_rsrc_t rsrc_t;

constexpr int S_ = 2048, D_ = 576, F_ = 4608, FH = 2304, QW = 1728;
constexpr int SLOT = 36864, SPARE = 4 * SLOT, LDS_BYTES = 163840;
constexpr int SP_CS = SPARE, SP_PF = SPARE + 1024, SP_EP = SPARE + 3072, SP_AO = SPARE + 4096;
constexpr size_t WS_Z2T = 0;
constexpr size_t WS_QKV = WS_Z2T + (size_t)S_ * FH * 2;
constexpr size_t WS_HF = WS_QKV + (size_t)S_ * QW * 2;
constexpr size_t WS_OF = WS_HF + (size_t)S_ * FH * 2;
constexpr size_t WS_ST = WS_OF + (size_t)S_ * D_ * 2;
constexpr size_t WS_CTL = WS_ST + (size_t)S_ * 16 * 16;
constexpr size_t CTL_EP = 0, CTL_QF = 1024, CTL_ZF = CTL_QF + 8192, CTL_OF = CTL_ZF + 8192, CTL_HF = CTL_OF + 8192, CTL_END = CTL_HF + 8192;
constexpr int G_QKV = 0, G_Z2 = 9, G_Z1 = 18, G_MAIN = 27, G_G = 59, G_S = 77;
constexpr unsigned SPIN_CAP = 400000u;

__device__ __forceinline__ float bflo(unsigned v) { return __uint_as_float(v << 16); }
__device__ __forceinline__ float bfhi(unsigned v) { return __uint_as_float(v & 0xffff0000u); }
__device__ __forceinline__ float bf2f(u16 v) { return __uint_as_float(((unsigned)v) << 16); }
__device__ __forceinline__ unsigned pack_bf16(float lo, float hi) {
    f32x2 v = {lo, hi};
    return __builtin_bit_cast(unsigned, __builtin_convertvector(v, bf16x2_t));
}
#define BAR() do { asm volatile("" ::: "memory"); __builtin_amdgcn_s_barrier(); asm volatile("" ::: "memory"); } while (0)
#define WAIT_V(n) asm volatile("s_waitcnt vmcnt(%0)" ::"n"(n) : "memory")
#define WAIT_L0() asm volatile("s_waitcnt lgkmcnt(0)" ::: "memory")
#define SB0() __builtin_amdgcn_sched_barrier(0)
#define ST_DECL
#define ST(k)
#define ST_END(who)

__device__ __forceinline__ rsrc_t mk_rsrc(const void* p, unsigned bytes) { return __builtin_amdgcn_make_buffer_rsrc((void*)p, 0, bytes, 0x00020000); }
__device__ __forceinline__ void dma16(rsrc_t r, LAS char* lds, unsigned voff, unsigned soff) {
    __builtin_amdgcn_raw_ptr_buffer_load_lds(r, (lds_vp)lds, 16, voff, soff, 0, 0);
}
__device__ __forceinline__ void dma16_dev(rsrc_t r, LAS char* lds, unsigned voff, unsigned soff) {
    __builtin_amdgcn_raw_ptr_buffer_load_lds(r, (lds_vp)lds, 16, voff, soff, 0, 16);
}
#define ATT_PRIO 1
#define STAGE_ST 0
#define Z1A 4
#define ST_AUX 16
__device__ __forceinline__ void touch_lines(rsrc_t r, LAS char* scratch, unsigned first, unsigned cnt, int lane) {
    __builtin_amdgcn_raw_ptr_buffer_load_lds(r, (lds_vp)scratch, 4, (unsigned)lane < cnt ? (first + lane) * 128u : 0x7ffffff0u, 0, 0, 0);
}
__device__ __forceinline__ void st128_dev(rsrc_t r, unsigned off, u32x4 v) { __builtin_amdgcn_raw_buffer_store_b128(v, r, off, 0, ST_AUX); }
__device__ __forceinline__ void st64_dev(rsrc_t r, unsigned off, u32x2 v) { __builtin_amdgcn_raw_buffer_store_b64(v, r, off, 0, ST_AUX); }
__device__ __forceinline__ bf16x8 lds_b128(LAS char* p) { return *(LAS bf16x8*)p; }
__device__ __forceinline__ bf16x8 lds_tr(LAS char* p, int off1) {
    s16x4 a = __builtin_amdgcn_ds_read_tr16_b64_v4i16((LAS s16x4*)p);
    s16x4 b = __builtin_amdgcn_ds_read_tr16_b64_v4i16((LAS s16x4*)(p + off1));
    return __builtin_shufflevector(a, b, 0, 1, 2, 3, 4, 5, 6, 7);
}
#define MFMA(a, b, c) __builtin_amdgcn_mfma_f32_16x16x32_bf16(a, b, c, 0, 0, 0)
template <int CTRL> __device__ __forceinline__ float dpp_add(float x) {
    return x + __builtin_bit_cast(float, __builtin_amdgcn_update_dpp(0, __builtin_bit_cast(int, x), CTRL, 0xf, 0xf, false));
}
__device__ __forceinline__ float row16_sum(float x) { x = dpp_add<0x128>(x); x = dpp_add<0x124>(x); x = dpp_add<0x122>(x); x = dpp_add<0x121>(x); return x; }
__device__ __forceinline__ float gelu_f(float x) {
    const float t = x * (-2.3022081498f - 0.1029432397f * x * x);
    return x * __builtin_amdgcn_rcpf(1.0f + __builtin_amdgcn_exp2f(t));
}
__device__ __forceinline__ f32x2 gelu2(f32x2 x) {
    const f32x2 t = x * (x * x * -0.1029432397f + -2.3022081498f);
    f32x2 e; e.x = __builtin_amdgcn_exp2f(t.x); e.y = __builtin_amdgcn_exp2f(t.y);
    const f32x2 d = e + 1.0f;
    f32x2 r; r.x = __builtin_amdgcn_rcpf(d.x); r.y = __builtin_amdgcn_rcpf(d.y);
    return x * r;
}
__device__ __forceinline__ f32x2 lo2(f32x4 v) { return (f32x2){v[0], v[1]}; }
__device__ __forceinline__ f32x2 hi2(f32x4 v) { return (f32x2){v[2], v[3]}; }
__device__ __forceinline__ unsigned pack2(f32x2 v) { return __builtin_bit_cast(unsigned, __builtin_convertvector(v, bf16x2_t)); }
__device__ __forceinline__ f32x2 unpack2(unsigned v) { return (f32x2){__uint_as_float(v << 16), __uint_as_float(v & 0xffff0000u)}; }
__device__ __forceinline__ int swap23(int r) { return (r & 0x13) | ((r & 4) << 1) | ((r & 8) >> 1); }
__device__ __forceinline__ float xchg32(float x) {
    auto r = __builtin_amdgcn_permlane32_swap(__float_as_uint(x), __float_as_uint(x), false, false);
    return __uint_as_float((threadIdx.x & 32) ? r[0] : r[1]);
}
__device__ __forceinline__ u32x4 gather8(unsigned x0, unsigned x1, unsigned y0, unsigned y1) {
    auto s0 = __builtin_amdgcn_permlane32_swap(x0, y0, false, false);
    auto s1 = __builtin_amdgcn_permlane32_swap(x1, y1, false, false);
    auto t0 = __builtin_amdgcn_permlane16_swap(s0[0], s0[1], false, false);
    auto t1 = __builtin_amdgcn_permlane16_swap(s1[0], s1[1], false, false);
    return (u32x4){t0[0], t1[0], t0[1], t1[1]};
}
__device__ __forceinline__ void publish(char* ctl, size_t word, u64 tag, int lane) {
    asm volatile("s_waitcnt vmcnt(0)" ::: "memory");
    if (lane == 0) __hip_atomic_store((GAS u64*)(ctl + word), tag, __ATOMIC_RELAXED, __HIP_MEMORY_SCOPE_AGENT);
}
__device__ __forceinline__ bool flags_ready(const char* f0, int n0, const char* f1, int n1, u64 tag, int lane) {
    u64 a = tag, b = tag;
    if (lane < n0) a = __hip_atomic_load((const GAS u64*)(f0 + lane * 8), __ATOMIC_RELAXED, __HIP_MEMORY_SCOPE_AGENT);
    if (lane < n1) b = __hip_atomic_load((const GAS u64*)(f1 + lane * 8), __ATOMIC_RELAXED, __HIP_MEMORY_SCOPE_AGENT);
    return __all(a == tag && b == tag);
}

struct Args { const u16* x; const u16* wqkv; const u16* wo; const u16* win; const u16* wsp; const u16* bsp; const u16* wout; u16* out; char* ws; };

struct RowsImg {
    unsigned v0, v1;
    __device__ __forceinline__ void init(int lane, unsigned rs) {
        const int r8 = lane >> 3, ch = lane & 7;
        v0 = r8 * rs + ((ch ^ ((lane >> 4) & 7)) << 4); v1 = r8 * rs + ((ch ^ ((4 + (lane >> 4)) & 7)) << 4);
    }
    __device__ __forceinline__ void issue(rsrc_t r, LAS char* dst, unsigned soff, unsigned rs) const {
#pragma unroll
        for (int q = 0; q < 4; ++q) dma16(r, dst + q * 1024, (q & 1) ? v1 : v0, soff + q * 8 * rs);
    }
    __device__ __forceinline__ void issue35(rsrc_t r, LAS char* dst, unsigned soff, unsigned rs, int p) const {
        const int q0 = p < 2 ? 3 * p : 6 + 5 * (p - 2);
        const unsigned va = (q0 & 1) ? v1 : v0, vb = (q0 & 1) ? v0 : v1;
#pragma unroll
        for (int q = 0; q < 3; ++q) dma16(r, dst + (q0 + q) * 1024, (q & 1) ? vb : va, soff + (q0 + q) * 8 * rs);
        if (p >= 2) {
#pragma unroll
            for (int q = 3; q < 5; ++q) dma16(r, dst + (q0 + q) * 1024, (q & 1) ? vb : va, soff + (q0 + q) * 8 * rs);
        }
    }
};
template <int NC> struct WImg {
    static constexpr int CPR = 2 * NC, BIG = (NC == 9) ? 5 : 4;
    unsigned v[BIG];
    static __device__ __forceinline__ int first(int p) { return NC == 9 ? (p < 2 ? 5 * p : 10 + 4 * (p - 2)) : (p < 2 ? 4 * p : 8 + 3 * (p - 2)); }
    __device__ __forceinline__ void init(int lane, int p, unsigned ldb) {
        const int I0 = first(p);
#pragma unroll
        for (int q = 0; q < BIG; ++q) {
            const int n = (I0 + q) * 64 + lane, pos = n / CPR, cc = n - pos * CPR;
            v[q] = ((pos & 32) + swap23(pos & 31)) * ldb + cc * 16;
        }
    }
    __device__ __forceinline__ void issue(rsrc_t r, LAS char* dst, unsigned soff, int p) const {
        const int I0 = first(p);
#pragma unroll
        for (int q = 0; q < BIG - 1; ++q) dma16(r, dst + (I0 + q) * 1024, v[q], soff);
        if (p < 2) dma16(r, dst + (I0 + BIG - 1) * 1024, v[BIG - 1], soff);
    }
};

#define MMA(acc, a, b) asm volatile("v_mfma_f32_16x16x32_bf16 %0, %1, %2, %0" : "+v"(acc) : "v"(a), "v"(b))
#define MMA_DRAIN() do { SB0(); asm volatile("s_nop 7\n\ts_nop 3"); SB0(); } while (0)
#define CST(idx)
struct NoHook { __device__ __forceinline__ void operator()(int) const {} };
__device__ __forceinline__ s16x4 lds_tr1(LAS char* p) { return __builtin_amdgcn_ds_read_tr16_b64_v4i16((LAS s16x4*)p); }
__device__ __forceinline__ bf16x8 cat8(s16x4 a, s16x4 b) { return __builtin_shufflevector(a, b, 0, 1, 2, 3, 4, 5, 6, 7); }

template <int NC, bool XST> struct XWAcc { f32x4 a[2][NC]; f32x4 cs[3]; f32x4 sx[2], sq[2]; };
template <int NC> struct XF { bf16x8 x[2]; s16x4 w[NC][2]; };
struct XAddr { unsigned x, w, r[3]; };
template <int NC> __device__ __forceinline__ void xa_read1(XF<NC>& f, int i, const XAddr& a) {
    if (i < 2) f.x[i] = lds_b128((LAS char*)(size_t)a.x + i * 2048);
    else {
        const int c = (i - 2) >> 1;
        const unsigned q = c >= NC - 3 ? a.r[c >= NC - 3 ? c - (NC - 3) : 0] : a.w;
        f.w[c][i & 1] = lds_tr1((LAS char*)(size_t)q + c * 32 + (i & 1) * (8 * NC * 32));
    }
}
#define VADD(v, d) asm volatile("v_add_u32 %0, %1, %0" : "+v"(v) : "s"(d))
template <int NC, bool TR, bool XST, int DBL, bool LOAD, bool UPD>
__device__ __forceinline__ void xa_step(XWAcc<NC, XST>& A, const XF<NC>& f, XF<NC>& n, XAddr& ra, XAddr& ua, int d, const bf16x8& ones) {
    constexpr int R = 2 + 2 * NC, G = 2 * NC + (NC + 3) / 4 + (XST ? 4 : 0);
    int rd = 0, gap = 0;
#define XA_UPD(k) do { if ((k) == 0) VADD(ua.x, d); if ((k) == 1) VADD(ua.w, d); if ((k) == 2) VADD(ua.r[0], d); if ((k) == 3) VADD(ua.r[1], d); if ((k) == 4) VADD(ua.r[2], d); \
                       if ((k) == 5) VADD(ra.x, d); if ((k) == 6) VADD(ra.w, d); if ((k) == 7) VADD(ra.r[0], d); if ((k) == 8) VADD(ra.r[1], d); if ((k) == 9) VADD(ra.r[2], d); } while (0)
#define XA_GAP() do { if (LOAD) { if (rd < R) { xa_read1<NC>(n, rd, ra); ++rd; } if (gap < DBL && rd < R) { xa_read1<NC>(n, rd, ra); ++rd; } } \
                      if (UPD && gap >= G - 6 && gap < G - 1) { XA_UPD(2 * (gap - (G - 6))); XA_UPD(2 * (gap - (G - 6)) + 1); } ++gap; SB0(); } while (0)
#pragma unroll
    for (int c = 0; c < NC; ++c) {
        const bf16x8 w = cat8(f.w[c][0], f.w[c][1]);
#pragma unroll
        for (int t = 0; t < 2; ++t) {
            if (TR) MMA(A.a[t][c], w, f.x[t]); else MMA(A.a[t][c], f.x[t], w);
            XA_GAP();
        }
        if ((c & 3) == 0) {
            if (TR) MMA(A.cs[c >> 2], w, ones); else MMA(A.cs[c >> 2], ones, w);
            XA_GAP();
        }
    }
    if (XST) {
#pragma unroll
        for (int t = 0; t < 2; ++t) { MMA(A.sx[t], ones, f.x[t]); XA_GAP(); MMA(A.sq[t], f.x[t], f.x[t]); XA_GAP(); }
    }
    SB0();
#undef XA_GAP
#undef XA_UPD
}
template <int NC, bool XST> __device__ __forceinline__ void xw_zero(XWAcc<NC, XST>& A) {
    const f32x4 z = {0.f, 0.f, 0.f, 0.f};
#pragma unroll
    for (int t = 0; t < 2; ++t) {
#pragma unroll
        for (int c = 0; c < NC; ++c) { A.a[t][c] = z; asm volatile("" : "+v"(A.a[t][c])); }
        if (XST) { A.sx[t] = z; A.sq[t] = z; asm volatile("" : "+v"(A.sx[t]), "+v"(A.sq[t])); }
    }
    A.cs[0] = z; A.cs[1] = z; asm volatile("" : "+v"(A.cs[0]), "+v"(A.cs[1]));
    if (NC > 8) { A.cs[2] = z; asm volatile("" : "+v"(A.cs[2])); }
}
template <int NC, bool TR, bool XST, int NST, class Hook>
__device__ __forceinline__ void xw_gemm(XWAcc<NC, XST>& A, LAS char* lds, int g0, int m, int lane, Hook hook) {
    asm volatile("" : "+v"(lane));
    const int fr = lane & 15, g = lane >> 4, row = 32 * m + fr;
    constexpr int G = 2 * NC + (NC + 3) / 4 + (XST ? 4 : 0), R = 2 + 2 * NC;
    constexpr int DA = R > G - 7 ? R - (G - 7) : 0, DB = R > G ? R - G : 0;
    static_assert(R <= G - 7 + DA && R <= G + DB, "all reads of a k-step are issued inside it");
    const unsigned lb = (unsigned)(size_t)lds + (g0 & 3) * SLOT;
    XAddr a0, a1;
    a0.x = lb + row * 128 + ((g ^ ((row >> 1) & 7)) << 4); a1.x = lb + ((row * 128 + ((g ^ ((row >> 1) & 7)) << 4)) ^ 64);
    a0.w = lb + 16384 + (16 * (g >> 1) + 4 * (g & 1) + (fr >> 2)) * (NC * 32) + (lane & 3) * 8 + m * 32; a1.w = a0.w + 32 * NC * 32;
#pragma unroll
    for (int k = 0; k < 3; ++k) { const int o = (NC - 3 + k + m >= NC) ? -NC * 32 : 0; a0.r[k] = a0.w + o; a1.r[k] = a1.w + o; }
    asm volatile("" : "+v"(a0.x), "+v"(a0.w), "+v"(a0.r[0]), "+v"(a0.r[1]), "+v"(a0.r[2]), "+v"(a1.x), "+v"(a1.w), "+v"(a1.r[0]), "+v"(a1.r[1]), "+v"(a1.r[2]));
    XF<NC> f0, f1;
    bf16x8 ones = {0x3F80, 0x3F80, 0x3F80, 0x3F80, 0x3F80, 0x3F80, 0x3F80, 0x3F80};
    asm volatile("" : "+v"(ones));
#pragma unroll
    for (int i = 0; i < R; ++i) xa_read1<NC>(f0, i, a0);
    int d = ((g0 & 3) == 3) ? -3 * SLOT : SLOT, pos = g0 & 3;
    asm volatile("s_nop 1"); SB0();
#pragma unroll 1
    for (int s = 0; s < NST - 1; ++s) {
        xa_step<NC, TR, XST, DA, true, true>(A, f0, f1, a1, a0, d, ones);
        pos = (pos + 1) & 3; d = pos == 3 ? -3 * SLOT : SLOT;
        WAIT_L0(); BAR(); SB0();
        hook(s);
        xa_step<NC, TR, XST, DB, true, false>(A, f1, f0, a0, a0, 0, ones);
    }
    xa_step<NC, TR, XST, DA, true, false>(A, f0, f1, a1, a1, 0, ones);
    WAIT_L0(); BAR(); SB0();
    xa_step<NC, TR, XST, 0, false, false>(A, f1, f0, a0, a0, 0, ones);
    MMA_DRAIN();
}
template <int NC, bool TR, bool XST> __device__ __forceinline__ void share_colsum(const XWAcc<NC, XST>& A, LAS char* lds, int m, int lane) {
    LAS float* tab = (LAS float*)(lds + SP_CS);
    const int fr = lane & 15, g = lane >> 4;
#pragma unroll
    for (int k = 0; k < (NC + 3) / 4; ++k) {
        int c = m + 4 * k; c = c >= NC ? c - NC : c;
        if (TR) { if (fr == 0) *(LAS f32x4*)(tab + c * 16 + 4 * g) = A.cs[k]; }
        else { if (g == 0) tab[c * 16 + fr] = A.cs[k][0]; }
    }
    WAIT_L0(); BAR();
}
template <int NC> __device__ __forceinline__ void unrotate(unsigned (&v)[NC][2], int m) {
    unsigned u[NC][2];
#define UNROT(M) _Pragma("unroll") for (int c = 0; c < NC; ++c) { u[(c + M) % NC][0] = v[c][0]; u[(c + M) % NC][1] = v[c][1]; }
    if (m == 0) { UNROT(0) } else if (m == 1) { UNROT(1) } else if (m == 2) { UNROT(2) } else { UNROT(3) }
#undef UNROT
#pragma unroll
    for (int c = 0; c < NC; ++c) { v[c][0] = u[c][0]; v[c][1] = u[c][1]; asm volatile("" : "+v"(v[c][0]), "+v"(v[c][1])); }
}

struct MFrag { bf16x8 w[2]; bf16x8 z[9]; };
template <unsigned MASK, int G0, int G1> __device__ __forceinline__ void m_part(f32x4 (&acc)[2][9], const MFrag& f, MFrag& n, LAS char* pW, LAS char* pZ) {
    int r = 0;
#pragma unroll
    for (int i = 0; i < G0; ++i) r += (MASK >> i) & 1;
#pragma unroll
    for (int i = G0; i < G1; ++i) {
        MMA(acc[i & 1][i >> 1], f.z[i >> 1], f.w[i & 1]);
        if ((MASK >> i) & 1) { if (r < 2) n.w[r] = lds_b128(pW + r * 2048); else n.z[r - 2] = lds_b128(pZ + (r - 2) * 1024); ++r; }
        SB0();
    }
}
#ifndef M_MASK_A
#define M_MASK_A 0x07FF
#define M_MASK_B 0x07FF
#define M_BPOS 0
#endif
__device__ __forceinline__ void main_gemm(f32x4 (&acc)[2][9], LAS char* lds, int g0, int m, int lane) {
    asm volatile("" : "+v"(lane));
    const int fr = lane & 15, g = lane >> 4, row = 32 * m + fr;
    const unsigned oW = row * 128 + ((g ^ ((row >> 1) & 7)) << 4);
    LAS char* pW0[4]; LAS char* pW1[4]; LAS char* pZ[4];
#pragma unroll
    for (int k = 0; k < 4; ++k) {
        const unsigned sb = ((g0 + k) & 3) * SLOT;
        pW0[k] = lds + sb + oW; pW1[k] = lds + sb + (oW ^ 64); pZ[k] = lds + sb + 16384 + lane * 16;
        asm volatile("" : "+v"(pW0[k]), "+v"(pW1[k]), "+v"(pZ[k]));
    }
#pragma unroll
    for (int t = 0; t < 2; ++t)
#pragma unroll
        for (int c = 0; c < 9; ++c) { acc[t][c] = (f32x4){0.f, 0.f, 0.f, 0.f}; asm volatile("" : "+v"(acc[t][c])); }
    MFrag f0, f1;
#pragma unroll
    for (int i = 0; i < 2; ++i) f0.w[i] = lds_b128(pW0[0] + i * 2048);
#pragma unroll
    for (int c = 0; c < 9; ++c) f0.z[c] = lds_b128(pZ[0] + c * 1024);
    asm volatile("s_nop 1"); SB0();
#define M_STAGE(K, KN) do { m_part<M_MASK_A, 0, 18>(acc, f0, f1, pW1[K], pZ[K] + 9216); m_part<0, 0, M_BPOS>(acc, f1, f0, pW0[KN], pZ[KN]); WAIT_L0(); BAR(); SB0(); m_part<M_MASK_B, M_BPOS, 18>(acc, f1, f0, pW0[KN], pZ[KN]); } while (0)
#pragma unroll 1
    for (int s = 0; s < 7; ++s) { M_STAGE(0, 1); M_STAGE(1, 2); M_STAGE(2, 3); M_STAGE(3, 0); }
    M_STAGE(0, 1); M_STAGE(1, 2); M_STAGE(2, 3);
#undef M_STAGE
    m_part<M_MASK_A, 0, 18>(acc, f0, f1, pW1[3], pZ[3] + 9216);
    WAIT_L0(); BAR(); SB0();
    m_part<0, 0, 18>(acc, f1, f0, pW0[0], pZ[0]);
    MMA_DRAIN();
}

struct EFrag { s16x4 w[2]; bf16x8 b[5]; };
template <bool LOAD, bool MM> __device__ __forceinline__ void e_step(f32x4 (&acc)[5], const EFrag& f, EFrag& n, LAS char* pW, LAS char* pB) {
    const bf16x8 w = cat8(f.w[0], f.w[1]);
#pragma unroll
    for (int t = 0; t < 5; ++t) {
        if (MM) MMA(acc[t], w, f.b[t]);
        if (LOAD) {
            if (t == 0) { n.w[0] = lds_tr1(pW); n.w[1] = lds_tr1(pW + 2048); }
            else if (t == 1) { n.b[0] = lds_b128(pB); n.b[1] = lds_b128(pB + 1024); }
            else n.b[t] = lds_b128(pB + t * 1024);
        }
        SB0();
    }
}
template <int NST, int NKS, bool MORE, class Hook> __device__ __forceinline__ void e_gemm(f32x4 (&acc)[5], LAS char* lds, int g0, int w, int lane, Hook hook) {
    asm volatile("" : "+v"(lane));
    const int g = lane >> 4, q = (lane & 15) >> 2;
    LAS char* pW = lds + (4 * g + q) * 128 + ((w ^ ((2 * g + (q >> 1)) & 3)) << 5) + (lane & 3) * 8;
    LAS char* pB = lds + 16384 + lane * 16;
#pragma unroll
    for (int t = 0; t < 5; ++t) { acc[t] = (f32x4){0.f, 0.f, 0.f, 0.f}; asm volatile("" : "+v"(acc[t])); }
    EFrag f0, f1;
    e_step<true, false>(acc, f1, f0, pW + (g0 & 3) * SLOT, pB + (g0 & 3) * SLOT);
    asm volatile("s_nop 1"); SB0();
#pragma unroll 1
    for (int s = 0; s < NST - 1; ++s) {
        const unsigned sb = ((g0 + s) & 3) * SLOT, sn = ((g0 + s + 1) & 3) * SLOT;
        e_step<true, true>(acc, f0, f1, pW + sb + 4096, pB + sb + 5120);
        e_step<true, true>(acc, f1, f0, pW + sb + 8192, pB + sb + 10240);
        e_step<true, true>(acc, f0, f1, pW + sb + 12288, pB + sb + 15360);
        WAIT_L0(); BAR(); SB0();
        hook(s);
        e_step<true, true>(acc, f1, f0, pW + sn, pB + sn);
    }
    const unsigned sb = ((g0 + NST - 1) & 3) * SLOT;
    constexpr int LAST = NKS - 4 * (NST - 1);
    e_step<true, true>(acc, f0, f1, pW + sb + 4096, pB + sb + 5120);
    if (LAST == 4) {
        e_step<true, true>(acc, f1, f0, pW + sb + 8192, pB + sb + 10240);
        e_step<true, true>(acc, f0, f1, pW + sb + 12288, pB + sb + 15360);
    }
    if (MORE) { WAIT_L0(); BAR(); SB0(); }
    e_step<false, true>(acc, f1, f0, pW, pB);
    MMA_DRAIN();
}

__device__ __forceinline__ float dot2(unsigned a, unsigned b, float c) { return __builtin_amdgcn_fdot2_f32_bf16(__builtin_bit_cast(bf16x2_t, a), __builtin_bit_cast(bf16x2_t, b), c, false); }
__device__ __forceinline__ unsigned at_region(int r, int p) { return (unsigned)(((r >> 1) ? 0 : 3 * SLOT) + ((r & 1) ? 16384 + p * 5120 : p * 4096)); }
__device__ __forceinline__ unsigned at_row(int rho, int p) { const int r = rho / 14; return at_region(r, p) + (unsigned)(rho - r * 14) * 288u; }
__device__ __forceinline__ void attn_issue(rsrc_t rQ, LAS char* lds, int n, int p, int lane, int half) {
#pragma unroll
    for (int rr = 0; rr < 2; ++rr)
#pragma unroll
        for (int q = 0; q < 4; ++q) {
            const int r = 2 * half + rr, c = q * 64 + lane, lr = c / 18, cc = c - lr * 18, rho = r * 14 + lr;
            int t, seg;
            if (rho < 22) { t = 8 * n - 14 + rho; seg = 1; } else if (rho < 44) { t = 8 * n - 14 + (rho - 22); seg = 2; } else { t = 8 * n + (rho - 44); seg = 0; }
            const unsigned voff = (t < 0 || rho >= 52 || lr >= 14) ? 0x7ffffff0u : (unsigned)(t * (QW * 2) + (seg * D_ + 144 * p) * 2 + cc * 16);
            dma16_dev(rQ, lds + at_region(r, p) + q * 1024, voff, 0);
        }
}
__device__ __forceinline__ void attn_compute(LAS char* lds, rsrc_t rO, int n, int p, int lane) {
    const int i = lane >> 3, hh = lane & 7, t = 8 * n + i;
    const unsigned hb = hh * 36, r0 = at_region(0, p) + hb, r1 = at_region(1, p) + hb - 14 * 288, r2 = at_region(2, p) + hb - 28 * 288, r3 = at_region(3, p) + hb - 42 * 288;
    unsigned qv[9];
    {
        LAS unsigned* q = (LAS unsigned*)(lds + r3 + (44 + i) * 288);
#pragma unroll
        for (int d = 0; d < 9; ++d) qv[d] = q[d];
    }
    float lg[8];
#pragma unroll
    for (int w = 0; w < 8; ++w) {
        const int rho = 14 + i - 2 * w;
        LAS unsigned* kp = (LAS unsigned*)(lds + (rho < 14 ? r0 : r1) + rho * 288);
        float s = 0.f;
#pragma unroll
        for (int d = 0; d < 9; ++d) s = dot2(qv[d], kp[d], s);
        lg[w] = (t - 2 * w >= 0) ? s * (0.23570226039551584f * 1.4426950408889634f) : -1e9f;
    }
    float mx = fmaxf(fmaxf(fmaxf(lg[0], lg[1]), fmaxf(lg[2], lg[3])), fmaxf(fmaxf(lg[4], lg[5]), fmaxf(lg[6], lg[7])));
    float sum = 0.f;
#pragma unroll
    for (int w = 0; w < 8; ++w) { lg[w] = __builtin_amdgcn_exp2f(lg[w] - mx); sum += lg[w]; }
    const float inv = __builtin_amdgcn_rcpf(sum);
    unsigned pp[4];
#pragma unroll
    for (int w = 0; w < 4; ++w) pp[w] = pack_bf16(lg[2 * w] * inv, lg[2 * w + 1] * inv);
    float acc[18];
#pragma unroll
    for (int d = 0; d < 18; ++d) acc[d] = 0.f;
#pragma unroll
    for (int w = 0; w < 4; ++w) {
        const int ra = 22 + 14 + i - 4 * w, rb = ra - 2;
        LAS unsigned* va = (LAS unsigned*)(lds + (ra < 28 ? r1 : ra < 42 ? r2 : r3) + ra * 288);
        LAS unsigned* vb = (LAS unsigned*)(lds + (rb < 28 ? r1 : rb < 42 ? r2 : r3) + rb * 288);
#pragma unroll
        for (int d = 0; d < 9; ++d) {
            const unsigned x = va[d], y = vb[d];
            acc[2 * d] = dot2(__builtin_amdgcn_perm(y, x, 0x05040100u), pp[w], acc[2 * d]);
            acc[2 * d + 1] = dot2(__builtin_amdgcn_perm(y, x, 0x07060302u), pp[w], acc[2 * d + 1]);
        }
    }
    LAS char* ob = lds + SP_AO + p * 2304;
#pragma unroll
    for (int d = 0; d < 9; ++d) *(LAS unsigned*)(ob + i * 288 + hh * 36 + d * 4) = pack_bf16(acc[2 * d], acc[2 * d + 1]);
    WAIT_L0();
    {
        const int r = lane & 7, g = (lane >> 3) & 3, tt = n >> 1, rl = (8 * n + r) & 15;
#pragma unroll
        for (int q = 0; q < 2; ++q) {
            const int a = (lane >> 5) + 2 * q;
            const u32x2 lo = *(LAS u32x2*)(ob + r * 288 + (32 * a + 4 * g) * 2), hi = *(LAS u32x2*)(ob + r * 288 + (32 * a + 16 + 4 * g) * 2);
            st128_dev(rO, ((tt * 18 + 4 * p + a) * 64 + rl + 16 * g) * 16, (u32x4){lo.x, lo.y, hi.x, hi.y});
        }
        const u32x2 v = *(LAS u32x2*)(ob + r * 288 + (128 + 4 * g) * 2);
        if (lane < 32) st64_dev(rO, ((tt * 18 + 16 + (p >> 1)) * 64 + rl + 16 * g) * 16 + (p & 1) * 8, v);
    }
}

__global__ __launch_bounds__(512, 2) void fused_kernel(Args a) {
    asm volatile("fusedk_pf_begin:");
    extern __shared__ __attribute__((aligned(16))) char smem[];
    LAS char* lds = (LAS char*)smem;
    const int wid = __builtin_amdgcn_readfirstlane(threadIdx.x >> 6), lane = threadIdx.x & 63;
    const int xcd = blockIdx.x & 7, loc = blockIdx.x >> 3;
    unsigned pf_dummy = 0;
    if (wid == 0) {
        u64 base; unsigned sz, back;
        asm volatile("s_getpc_b64 %0\nfusedk_pf_here:\n\ts_mov_b32 %1, fusedk_pf_end-fusedk_pf_begin\n\ts_mov_b32 %2, fusedk_pf_here-fusedk_pf_begin" : "=s"(base), "=s"(sz), "=s"(back));
        base -= back;
        const unsigned off = (unsigned)(loc * 13 + lane) * 128u;
        if (lane < 13 && (int)off < (int)sz) asm volatile("global_load_dword %0, %1, off" : "=v"(pf_dummy) : "v"(base + off));
    }
    const int ti = (xcd & 1) * 8 + (loc & 7), tj = (xcd >> 1) * 4 + (loc >> 3);
    const int nt = xcd * 30 + loc;
    const bool fin = loc < 30 && nt < 234;
    const int rb = nt / 9, cb = nt - rb * 9;
    {
        RowsImg xi0; xi0.init(lane, D_ * 2);
        const rsrc_t rx = mk_rsrc(a.x, S_ * D_ * 2);
        const unsigned so = (unsigned)(ti * 128 + 16 * wid) * (D_ * 2);
        dma16(rx, lds + wid * 2048, xi0.v0, so); dma16(rx, lds + wid * 2048 + 1024, xi0.v1, so + 8 * (D_ * 2));
        dma16(rx, lds + SLOT + wid * 2048, xi0.v0, so + 128); dma16(rx, lds + SLOT + wid * 2048 + 1024, xi0.v1, so + 8 * (D_ * 2) + 128);
    }
    char* ctl = a.ws + WS_CTL;
    if (lane < 2) __builtin_amdgcn_raw_ptr_buffer_load_lds(mk_rsrc(ctl, 4096), (lds_vp)(lds + SP_EP + wid * 64), 4, lane * 4 + (unsigned)CTL_EP, 0, 0, 16);
#define TAG_FROM_EPOCH() \
    u64 ep_; asm volatile("ds_read_b64 %0, %1\n\ts_waitcnt lgkmcnt(0)" : "=&v"(ep_) : "v"((unsigned)(size_t)(lds + SP_EP + wid * 64)) : "memory"); \
    const u64 tg_ = ep_ * 0x9E3779B97F4A7C15ull + 0xD1B54A32D192ED03ull; \
    const u64 tag = ((u64)(unsigned)__builtin_amdgcn_readfirstlane((unsigned)(tg_ >> 32)) << 32) | (unsigned)__builtin_amdgcn_readfirstlane((unsigned)tg_)
    const int an = ti * 16 + tj;

    if (__builtin_expect(wid < 4, 0)) {
    const int m = wid, fr = lane & 15, g4 = lane >> 4;
    ST_DECL; ST(0);
    const rsrc_t rQs = mk_rsrc(a.ws + WS_QKV, S_ * QW * 2), rZs = mk_rsrc(a.ws + WS_Z2T, S_ * FH * 2), rHs = mk_rsrc(a.ws + WS_HF, S_ * FH * 2), rSt = mk_rsrc(a.ws + WS_ST, S_ * 16 * 16);
    LAS float* cst = (LAS float*)(lds + SP_CS);
    float xm[2], xr[2];
    unsigned pf1_dummy = 0;
    {
        const unsigned W = blockIdx.x * 4 + m;
        const bool isx = lane < 18;
        const unsigned ln = isx ? W * 18 + lane : W * 16 + (lane - 18);
        const char* src = isx ? (const char*)a.x : (const char*)a.wqkv;
        const unsigned lines = isx ? S_ * D_ * 2 / 128 : D_ * QW * 2 / 128;
        if (lane < 34 && ln < lines) asm volatile("global_load_dword %0, %1, off" : "=v"(pf1_dummy) : "v"(src + (size_t)ln * 128));
    }
    asm volatile("s_waitcnt vmcnt(1)" : "+v"(pf_dummy) :: "memory");
    TAG_FROM_EPOCH();
    BAR();
    ST(1);
    {
        XWAcc<7, true> A; xw_zero(A);
        xw_gemm<7, true, true, 9>(A, lds, G_QKV, m, lane, NoHook());
        ST(2);
        asm volatile("s_waitcnt vmcnt(0)" : "+v"(pf1_dummy) :: "memory");
        share_colsum<7, true, true>(A, lds, m, lane);
#pragma unroll
        for (int t = 0; t < 2; ++t) {
            const int r3 = fr & 3;
            const float dq = r3 == 0 ? A.sq[t][0] : r3 == 1 ? A.sq[t][1] : r3 == 2 ? A.sq[t][2] : A.sq[t][3];
            const float sq = __shfl(dq, (fr >> 2) * 16 + fr);
            xm[t] = A.sx[t][0] * (1.0f / D_);
            xr[t] = __builtin_amdgcn_rsqf(fmaxf(sq * (1.0f / D_) - xm[t] * xm[t], 0.f) + 1e-5f);
        }
        const int c0 = (tj * 27) >> 2, dup = tj > 0 && c0 < (((tj - 1) * 27) >> 2) + 7;
#pragma unroll
        for (int t = 0; t < 2; ++t) {
            unsigned pk[7][2];
#pragma unroll
            for (int c = 0; c < 7; ++c) {
                const int tile = c + m >= 7 ? c + m - 7 : c + m;
                const f32x4 cs = *(LAS f32x4*)(cst + tile * 16 + 4 * g4);
                pk[c][0] = pack2((lo2(A.a[t][c]) - lo2(cs) * xm[t]) * xr[t]); pk[c][1] = pack2((hi2(A.a[t][c]) - hi2(cs) * xm[t]) * xr[t]);
            }
            unrotate<7>(pk, m);
            const unsigned ro = (unsigned)(ti * 128 + 32 * m + 16 * t + fr) * (QW * 2) + c0 * 32;
#pragma unroll
            for (int pp = 0; pp < 3; ++pp) st128_dev(rQs, ro + (1 + 2 * pp) * 32 + g4 * 16, gather8(pk[1 + 2 * pp][0], pk[1 + 2 * pp][1], pk[2 + 2 * pp][0], pk[2 + 2 * pp][1]));
            if (!dup) st64_dev(rQs, ro + g4 * 8, (u32x2){pk[0][0], pk[0][1]});
        }
    }
    ST(3);
    unsigned zp[2][9][2];
    {
        XWAcc<9, false> A; xw_zero(A);
        xw_gemm<9, false, false, 9>(A, lds, G_Z2, m, lane, [&](int s) { if (s == 4) publish(ctl, CTL_QF + ((ti * 16 + tj) * 4 + m) * 8, tag, lane); });
        ST(4);
        share_colsum<9, false, false>(A, lds, m, lane);
        f32x2 rm[2][2], rr[2][2];
#pragma unroll
        for (int t = 0; t < 2; ++t)
#pragma unroll
            for (int h = 0; h < 2; ++h) {
                rm[t][h] = (f32x2){__shfl(xm[t], 4 * g4 + 2 * h), __shfl(xm[t], 4 * g4 + 2 * h + 1)};
                rr[t][h] = (f32x2){__shfl(xr[t], 4 * g4 + 2 * h), __shfl(xr[t], 4 * g4 + 2 * h + 1)};
            }
        f32x2 s1[2][2], s2[2][2];
#pragma unroll
        for (int t = 0; t < 2; ++t)
#pragma unroll
            for (int h = 0; h < 2; ++h) { s1[t][h] = (f32x2){0.f, 0.f}; s2[t][h] = (f32x2){0.f, 0.f}; }
#pragma unroll
        for (int c = 0; c < 9; ++c) {
            const float cs = cst[(c + m >= 9 ? c + m - 9 : c + m) * 16 + fr];
#pragma unroll
            for (int t = 0; t < 2; ++t)
#pragma unroll
                for (int h = 0; h < 2; ++h) {
                    const f32x2 v = gelu2(((h ? hi2(A.a[t][c]) : lo2(A.a[t][c])) - rm[t][h] * cs) * rr[t][h]);
                    s1[t][h] += v; s2[t][h] += v * v;
                    zp[t][c][h] = pack2(v);
                    asm volatile("" : "+v"(zp[t][c][h]));
                }
        }
        float mm = 0.f, qq = 0.f;
        {
            const int idx = lane & 7;
            float a1[8], a2[8];
#pragma unroll
            for (int k = 0; k < 8; ++k) { a1[k] = s1[k >> 2][(k >> 1) & 1][k & 1]; a2[k] = s2[k >> 2][(k >> 1) & 1][k & 1]; }
#pragma unroll
            for (int k = 0; k < 8; ++k) { a1[k] = dpp_add<0x128>(a1[k]); a2[k] = dpp_add<0x128>(a2[k]); }
#pragma unroll
            for (int k = 0; k < 8; ++k) { a1[k] = dpp_add<0x124>(a1[k]); a2[k] = dpp_add<0x124>(a2[k]); }
#pragma unroll
            for (int k = 0; k < 8; ++k) { a1[k] = dpp_add<0x122>(a1[k]); a2[k] = dpp_add<0x122>(a2[k]); }
#pragma unroll
            for (int k = 0; k < 8; ++k) { a1[k] = dpp_add<0x121>(a1[k]); a2[k] = dpp_add<0x121>(a2[k]); }
            float u1 = a1[0], u2 = a2[0];
#pragma unroll
            for (int k = 1; k < 8; ++k) { u1 = idx == k ? a1[k] : u1; u2 = idx == k ? a2[k] : u2; }
            mm = u1 * (1.0f / 144.0f);
            qq = fmaxf(u2 - u1 * mm, 0.f);
            if (fr < 8) st128_dev(rSt, ((ti * 16 + tj) * 128 + 32 * m + 16 * (idx >> 2) + 4 * g4 + (idx & 3)) * 16,
                                  (u32x4){__float_as_uint(mm), (unsigned)tag, __float_as_uint(qq), (unsigned)(tag >> 32)});
        }
    }
    ST(5);
    unsigned z1p[2][9][2];
    {
        XWAcc<9, false> A; xw_zero(A);
        xw_gemm<9, true, false, Z1A>(A, lds, G_Z1, m, lane, NoHook());
        ST(6);
        float mu, rs;
        {
            const unsigned off = ((ti * 16 + (lane >> 5) * 8) * 128 + 32 * m + (lane & 31)) * 16;
            u32x4 v[8]; unsigned spins = 0;
            for (;;) {
#pragma unroll
                for (int k = 0; k < 8; ++k) v[k] = __builtin_amdgcn_raw_buffer_load_b128(rSt, off + k * 2048, 0, 16);
                unsigned bad = 0;
#pragma unroll
                for (int k = 0; k < 8; ++k) bad |= (v[k].y ^ (unsigned)tag) | (v[k].w ^ (unsigned)(tag >> 32));
                if (__all(bad == 0) || ++spins > SPIN_CAP) break;
                __builtin_amdgcn_s_sleep(2);
            }
            float s = 0.f;
#pragma unroll
            for (int k = 0; k < 8; ++k) s += __uint_as_float(v[k].x);
            s += xchg32(s);
            mu = s * (1.0f / 16.0f);
            float q = 0.f;
#pragma unroll
            for (int k = 0; k < 8; ++k) { const float d = __uint_as_float(v[k].x) - mu; q += __uint_as_float(v[k].z) + 144.0f * d * d; }
            q += xchg32(q);
            rs = __builtin_amdgcn_rsqf(q * (1.0f / FH) + 1e-5f);
        }
        ST(7);
        f32x2 mr[2][2], rr[2][2];
#pragma unroll
        for (int t = 0; t < 2; ++t)
#pragma unroll
            for (int h = 0; h < 2; ++h) {
                const int src = 16 * t + 4 * g4 + 2 * h;
                mr[t][h] = (f32x2){__shfl(mu, src), __shfl(mu, src + 1)}; rr[t][h] = (f32x2){__shfl(rs, src), __shfl(rs, src + 1)};
            }
        const unsigned zo = (unsigned)((tj * 64 + ti * 4 + m) * 9) * 1024 + lane * 16;
#pragma unroll
        for (int c = 0; c < 9; ++c) {
            const unsigned x0 = pack2((unpack2(zp[0][c][0]) - mr[0][0]) * rr[0][0]), x1 = pack2((unpack2(zp[0][c][1]) - mr[0][1]) * rr[0][1]);
            const unsigned y0 = pack2((unpack2(zp[1][c][0]) - mr[1][0]) * rr[1][0]), y1 = pack2((unpack2(zp[1][c][1]) - mr[1][1]) * rr[1][1]);
            st128_dev(rZs, zo + (c + m >= 9 ? c + m - 9 : c + m) * 1024, gather8(x0, x1, y0, y1));
        }
        ST(8);
        xw_gemm<9, true, false, 9 - Z1A>(A, lds, G_Z1 + Z1A, m, lane, [&](int s) { if (s == 1) publish(ctl, CTL_ZF + ((tj * 16 + ti) * 4 + m) * 8, tag, lane); });
        ST(9);
        share_colsum<9, true, false>(A, lds, m, lane);
#pragma unroll
        for (int c = 0; c < 9; ++c) {
            const f32x4 cs = *(LAS f32x4*)(cst + (c + m >= 9 ? c + m - 9 : c + m) * 16 + 4 * g4);
#pragma unroll
            for (int t = 0; t < 2; ++t) {
                z1p[t][c][0] = pack2(gelu2((lo2(A.a[t][c]) - lo2(cs) * xm[t]) * xr[t])); z1p[t][c][1] = pack2(gelu2((hi2(A.a[t][c]) - hi2(cs) * xm[t]) * xr[t]));
                asm volatile("" : "+v"(z1p[t][c][0]), "+v"(z1p[t][c][1]));
            }
        }
        unrotate<9>(z1p[0], m); unrotate<9>(z1p[1], m);
    }
    ST(10);
    BAR();
    {
        f32x4 acc[2][9];
        float bt[2];
#pragma unroll
        for (int t = 0; t < 2; ++t) bt[t] = bf2f(a.bsp[ti * 128 + 32 * m + 16 * t + fr]);
        main_gemm(acc, lds, G_MAIN, m, lane);
        ST(11);
#pragma unroll
        for (int t = 0; t < 2; ++t) {
            const unsigned hp = (unsigned)(ti * 8 + 2 * m + t) * 72 * 1024 + lane * 16;
#pragma unroll
            for (int pp = 0; pp < 4; ++pp) {
                u32x4 o;
                o.x = pack2((lo2(acc[t][2 * pp]) + bt[t]) * unpack2(z1p[t][2 * pp][0])); o.y = pack2((hi2(acc[t][2 * pp]) + bt[t]) * unpack2(z1p[t][2 * pp][1]));
                o.z = pack2((lo2(acc[t][2 * pp + 1]) + bt[t]) * unpack2(z1p[t][2 * pp + 1][0])); o.w = pack2((hi2(acc[t][2 * pp + 1]) + bt[t]) * unpack2(z1p[t][2 * pp + 1][1]));
                st128_dev(rHs, hp + (tj * 4 + pp) * 1024, o);
            }
            u32x2 o;
            o.x = pack2((lo2(acc[t][8]) + bt[t]) * unpack2(z1p[t][8][0])); o.y = pack2((hi2(acc[t][8]) + bt[t]) * unpack2(z1p[t][8][1]));
            st64_dev(rHs, hp + (64 + (tj >> 1)) * 1024 + (tj & 1) * 8, o);
        }
    }
    ST(12);
    publish(ctl, CTL_HF + ((ti * 16 + tj) * 4 + m) * 8, tag, lane);
    ST(13);
    if (!fin) { ST_END(0); return; }
    {
        f32x4 sa[5], ga[5];
        u32x2 xv[5];
        const int fr = lane & 15, g4 = lane >> 4;
#pragma unroll
        for (int t = 0; t < 5; ++t) xv[t] = *(const u32x2*)(a.x + (size_t)(min(rb * 5 + t, 127) * 16 + fr) * D_ + cb * 64 + m * 16 + g4 * 4);
        BAR();
        ST(14);
        e_gemm<18, 72, true>(ga, lds, G_G, m, lane, NoHook());
        ST(15);
        e_gemm<5, 18, false>(sa, lds, G_S, m, lane, NoHook());
        ST(16);
#pragma unroll
        for (int t = 0; t < 5; ++t) {
            if (rb * 5 + t < 128) {
                float o[4];
#pragma unroll
                for (int r = 0; r < 4; ++r) {
                    const float gv = ga[t][r], sg = gv * __builtin_amdgcn_rcpf(1.0f + __builtin_amdgcn_exp2f(gv * -1.4426950408889634f));
                    const float xx = (r & 1) ? bfhi(xv[t][r >> 1]) : bflo(xv[t][r >> 1]);
                    o[r] = xx + sa[t][r] * sg;
                }
                u32x2 ov; ov.x = pack_bf16(o[0], o[1]); ov.y = pack_bf16(o[2], o[3]);
                *(u32x2*)(a.out + (size_t)((rb * 5 + t) * 16 + fr) * D_ + cb * 64 + m * 16 + g4 * 4) = ov;
            }
        }
    }
    if (blockIdx.x == 0 && threadIdx.x == 0) __hip_atomic_store((GAS u64*)(ctl + CTL_EP), tag, __ATOMIC_RELAXED, __HIP_MEMORY_SCOPE_AGENT);
    ST(17);
    ST_END(0);
    asm volatile("fusedk_pf_end:");
        return;
    }
    {

        const int p = wid - 4;
        ST_DECL; ST(0);
#define rX mk_rsrc(a.x, S_ * D_ * 2)
#define rQW mk_rsrc(a.wqkv, D_ * QW * 2)
#define rW mk_rsrc(a.win, D_ * F_ * 2)
#define rS mk_rsrc(a.wsp, S_ * S_ * 2)
#define rZ mk_rsrc(a.ws + WS_Z2T, S_ * FH * 2)
#define rQ mk_rsrc(a.ws + WS_QKV, S_ * QW * 2)
#define rO mk_rsrc(a.ws + WS_OF, S_ * D_ * 2)
        WImg<7> wq; wq.init(lane, p, QW * 2);
        const unsigned sQ = (unsigned)((tj * 27) >> 2) * 32;
        wq.issue(rQW, lds + 16384, sQ, p); wq.issue(rQW, lds + SLOT + 16384, sQ + 64 * (QW * 2), p);
        SB0();
        RowsImg xi; xi.init(lane, D_ * 2);
        RowsImg si; si.init(lane, S_ * 2);
        WImg<9> wz; wz.init(lane, p, F_ * 2);
        const unsigned sX0 = (unsigned)(ti * 128) * (D_ * 2), sZ1 = (unsigned)(tj * 144) * 2, sZ2 = sZ1 + FH * 2;
        const unsigned sS = (unsigned)(ti * 128 + 32 * p) * (S_ * 2), sZ = (unsigned)tj * (64u * 9216u);
        const unsigned vl = lane * 16;
        const unsigned vE = (lane >> 3) * (D_ * 2) + ((((lane & 7) >> 1) ^ ((lane >> 4) & 3)) << 5) + (lane & 1) * 16;
        const int zI0 = WImg<9>::first(p);
        auto iss_q = [&](int g) {
            LAS char* slot = lds + (g & 3) * SLOT;
            xi.issue35(rX, slot, sX0 + g * 128, D_ * 2, p); wq.issue(rQW, slot + 16384, sQ + g * 64 * (QW * 2), p);
        };
        auto iss_z = [&](int g, int s, unsigned col) {
            LAS char* slot = lds + (g & 3) * SLOT;
            xi.issue35(rX, slot, sX0 + s * 128, D_ * 2, p);
            wz.issue(rW, slot + 16384, col + s * 64 * (F_ * 2), p);
        };
        auto iss_m = [&](int g) {
            LAS char* slot = lds + (g & 3) * SLOT; const int s = g - G_MAIN;
            si.issue(rS, slot + p * 4096, sS + s * 128, S_ * 2);
#pragma unroll
            for (int q = 0; q < 4; ++q) dma16_dev(rZ, slot + 16384 + (zI0 + q) * 1024, vl, sZ + s * 18432 + (zI0 + q) * 1024);
            if (p < 2) dma16_dev(rZ, slot + 16384 + (zI0 + 4) * 1024, vl, sZ + s * 18432 + (zI0 + 4) * 1024);
        };
        unsigned eH[5], eO[5];
#pragma unroll
        for (int t = 0; t < 5; ++t) { const unsigned tt = (unsigned)__builtin_amdgcn_readfirstlane(min(rb * 5 + t, 127)); eH[t] = tt * 73728u + p * 1024u; eO[t] = tt * 18432u + p * 1024u; }
        auto iss_ew = [&](int g, rsrc_t rw, unsigned w0, unsigned w1) {
            LAS char* slot = lds + (g & 3) * SLOT;
            dma16(rw, slot + p * 4096, vE, w0); dma16(rw, slot + p * 4096 + 1024, vE, w0 + 8 * (D_ * 2));
            dma16(rw, slot + p * 4096 + 2048, vE, w1); dma16(rw, slot + p * 4096 + 3072, vE, w1 + 8 * (D_ * 2));
        };
        auto iss_eb = [&](int g, rsrc_t rb_, const unsigned (&eb)[5], unsigned bo) {
            LAS char* slot = lds + (g & 3) * SLOT;
#pragma unroll
            for (int t = 0; t < 5; ++t) dma16_dev(rb_, slot + 16384 + p * 5120 + t * 1024, vl, eb[t] + bo);
        };
        auto iss_e = [&](int g, rsrc_t rw, rsrc_t rb_, const unsigned (&eb)[5], unsigned w0, unsigned w1, unsigned bo) { iss_ew(g, rw, w0, w1); iss_eb(g, rb_, eb, bo); };
        constexpr unsigned TILE_B = 16 * D_ * 2;
        const unsigned wcol = cb * 128;
        auto iss_g = [&](int s) {
            const unsigned c0 = s < 16 ? 9 * s + 2 * p : 18 * (4 * (s - 16) + p) + 8, c1 = s < 16 ? c0 + 1 : c0 + 9;
            iss_e(G_G + s, mk_rsrc(a.wout, FH * D_ * 2), mk_rsrc(a.ws + WS_HF, S_ * FH * 2), eH, c0 * TILE_B + wcol, c1 * TILE_B + wcol, s * 4096);
        };
        auto iss_s = [&](int s) {
            const int pp = s == 4 && p > 1 ? 1 : p;
            const unsigned c0 = s < 4 ? 9 * s + 2 * p : 18 * pp + 8, c1 = s < 4 ? c0 + 1 : c0 + 9;
            iss_e(G_S + s, mk_rsrc(a.wo, D_ * D_ * 2), rO, eO, c0 * TILE_B + wcol, c1 * TILE_B + wcol, s * 4096 + (pp - p) * 1024);
        };
#define PSTEP(K, PH) do { WAIT_V(7 * (K)); BAR(); } while (0)
#define PW(N, PH) do { WAIT_V(N); BAR(); } while (0)
#define SPIN_UNTIL(cond) do { unsigned spins_ = 0; while (!(cond) && ++spins_ < SPIN_CAP) __builtin_amdgcn_s_sleep(4); asm volatile("" ::: "memory"); } while (0)
        iss_q(2); iss_q(3);
        touch_lines(rW, lds + SP_PF + wid * 256, (blockIdx.x * 4 + p) * 41, 41, lane);
        WAIT_V(20);
        TAG_FROM_EPOCH();
        WAIT_V(14); BAR();
#pragma unroll 1
        for (int g = 1; g <= 5; ++g) { PW(14, 0); iss_q(g + 3); }
#pragma unroll 1
        for (int g = 6; g <= 7; ++g) { PSTEP(2, 1); iss_z(g + 3, g + 3 - G_Z2, sZ2); }
#pragma unroll 1
        for (int g = 8; g <= 14; ++g) { PW(16, 1); if (g == G_Z2) BAR(); iss_z(g + 3, g + 3 - G_Z2, sZ2); }
        PW(16, 1); iss_z(18, 0, sZ1);
        PW(16, 1); iss_z(19, 1, sZ1);
        PW(16, 1); iss_z(20, 2, sZ1);
        PW(16, 2);
        BAR();
        iss_z(21, 3, sZ1);
        {
            const unsigned W = blockIdx.x * 4 + p;
            touch_lines(rS, lds + SP_PF + wid * 256, W * 64, 64, lane);
            touch_lines(mk_rsrc(a.wout, FH * D_ * 2), lds + SP_PF + wid * 256, W * 21, 21, lane);
            touch_lines(mk_rsrc(a.wo, D_ * D_ * 2), lds + SP_PF + wid * 256, W * 6, 6, lane);
        }
#pragma unroll 1
        for (int g = 19; g <= 23; ++g) { PW(16, 2); iss_z(g + 3, g + 3 - G_Z1, sZ1); }
        PSTEP(2, 2); PSTEP(1, 2); PSTEP(0, 2);
        BAR(); BAR();
        SPIN_UNTIL(flags_ready(ctl + CTL_ZF + tj * 512, 64, ctl, 0, tag, lane));
        iss_m(27); iss_m(28); iss_m(29); iss_m(30);
        {
            const int pa = (8 * an - 14) >> 7;
            SPIN_UNTIL(flags_ready(ctl + CTL_QF + ti * 512, 64, ctl + CTL_QF + (pa < 0 ? ti : pa) * 512, 64, tag, lane));
        }
        BAR();
#pragma unroll 1
        for (int g = 28; g <= 55; ++g) { PW(16, 3); iss_m(g + 3); }
        PSTEP(2, 3); attn_issue(rQ, lds, an, p, lane, 0);
        PSTEP(1, 3); attn_issue(rQ, lds, an, p, lane, 1);
        PSTEP(1, 3);
        BAR();
        ST(1);
        WAIT_V(0);
        attn_compute(lds, rO, an, p, lane);
        ST(2);
        if (!fin) publish(ctl, CTL_OF + (an * 4 + p) * 8, tag, lane);
        else {
            {
                const int p0 = (rb * 80) >> 7, p1 = min(rb * 80 + 79, S_ - 1) >> 7;
                WAIT_V(0);
                if (lane == 0) __hip_atomic_store((GAS u64*)(ctl + CTL_OF + (an * 4 + p) * 8), tag, __ATOMIC_RELAXED, __HIP_MEMORY_SCOPE_AGENT);
#pragma unroll
                for (int s4 = 0; s4 < 4; ++s4) iss_ew(G_G + s4, mk_rsrc(a.wout, FH * D_ * 2), (9 * s4 + 2 * p) * TILE_B + wcol, (9 * s4 + 2 * p + 1) * TILE_B + wcol);
                SPIN_UNTIL(flags_ready(ctl + CTL_HF + p0 * 512, 64, ctl + CTL_HF + p1 * 512, 64, tag, lane));
                SPIN_UNTIL(flags_ready(ctl + CTL_OF + rb * 320, rb == 25 ? 24 : 40, ctl, 0, tag, lane));
            }
            ST(3);
            {
                const rsrc_t rh = mk_rsrc(a.ws + WS_HF, S_ * FH * 2);
                iss_eb(G_G, rh, eH, 0); iss_eb(G_G + 1, rh, eH, 4096); iss_eb(G_G + 2, rh, eH, 8192); iss_eb(G_G + 3, rh, eH, 12288);
            }
            PSTEP(2, 5);
            PW(10, 5); iss_g(4);
            PW(14, 5); iss_g(5);
            {
                const rsrc_t rw = mk_rsrc(a.wout, FH * D_ * 2), rh = mk_rsrc(a.ws + WS_HF, S_ * FH * 2);
                unsigned w0 = (54 + 2 * p) * TILE_B + wcol, bo = 6 * 4096;
#pragma unroll 1
                for (int g = 62; g <= 71; ++g) {
                    PW(18, 5);
                    iss_e(g + 3, rw, rh, eH, w0, w0 + TILE_B, bo);
                    w0 += 9 * TILE_B; bo += 4096;
                }
            }
            PW(18, 5); iss_g(16);
            PW(18, 5); iss_g(17);
            PW(18, 4); iss_s(0);
            PW(18, 4); iss_s(1);
            PW(18, 4); iss_s(2);
            PW(18, 4); iss_s(3);
            PW(18, 4); iss_s(4);
            PSTEP(2, 4); PSTEP(1, 4); PSTEP(0, 4);
        }
        ST_END(1);
        return;
        }
}

extern "C" void kernel_launch(void* const* d_in, const int* in_sizes, int n_in, void* d_out, int out_size, void* d_ws, size_t ws_size, hipStream_t stream) {
    Args a;
    a.x = (const u16*)d_in[0]; a.wqkv = (const u16*)d_in[1]; a.wo = (const u16*)d_in[2]; a.win = (const u16*)d_in[4]; a.wsp = (const u16*)d_in[5];
    a.bsp = (const u16*)d_in[6]; a.wout = (const u16*)d_in[7]; a.out = (u16*)d_out; a.ws = (char*)d_ws;
    (void)hipFuncSetAttribute((const void*)fused_kernel, hipFuncAttributeMaxDynamicSharedMemorySize, LDS_BYTES);
    fused_kernel<<<256, 512, LDS_BYTES, stream>>>(a);
}
```

```cpp
#include <hip/hip_runtime.h>
#include <hip/hip_bf16.h>
#include <stdint.h>

typedef unsigned short u16;
typedef unsigned long long u64;
typedef short bf16x8 __attribute__((ext_vector_type(8)));
typedef short s16x4 __attribute__((ext_vector_type(4)));
typedef float f32x4 __attribute__((ext_vector_type(4)));
typedef float f32x2 __attribute__((ext_vector_type(2)));
typedef unsigned u32x4 __attribute__((ext_vector_type(4)));
typedef unsigned u32x2 __attribute__((ext_vector_type(2)));
typedef __bf16 bf16x2_t __attribute__((ext_vector_type(2)));
#define LAS __attribute__((address_space(3)))
#define GAS __attribute__((address_space(1)))
typedef LAS void* lds_vp;
typedef __amdgpu_buffer_rsrc_t rsrc_t;

constexpr int S_ = 2048, D_ = 576, F_ = 4608, FH = 2304, QW = 1728;
constexpr int SLOT = 36864, SPARE = 4 * SLOT, LDS_BYTES = 163840;
constexpr int SP_CS = SPARE, SP_PF = SPARE + 1024, SP_EP = SPARE + 3072, SP_AO = SPARE + 4096;
constexpr size_t WS_Z2T = 0;
constexpr size_t WS_QKV = WS_Z2T + (size_t)S_ * FH * 2;
constexpr size_t WS_HF = WS_QKV + (size_t)S_ * QW * 2;
constexpr size_t WS_OF = WS_HF + (size_t)S_ * FH * 2;
constexpr size_t WS_ST = WS_OF + (size_t)S_ * D_ * 2;
constexpr size_t WS_CTL = WS_ST + (size_t)S_ * 16 * 16;
constexpr size_t CTL_EP = 0, CTL_QF = 1024, CTL_ZF = CTL_QF + 8192, CTL_OF = CTL_ZF + 8192, CTL_HF = CTL_OF + 8192, CTL_END = CTL_HF + 8192;
constexpr int G_QKV = 0, G_Z2 = 9, G_Z1 = 18, G_MAIN = 27, G_G = 59, G_S = 77;
constexpr int Z1A = 4;
constexpr unsigned SPIN_CAP = 400000u;

__device__ __forceinline__ float bflo(unsigned v) { return __uint_as_float(v << 16); }
__device__ __forceinline__ float bfhi(unsigned v) { return __uint_as_float(v & 0xffff0000u); }
__device__ __forceinline__ float bf2f(u16 v) { return __uint_as_float(((unsigned)v) << 16); }
__device__ __forceinline__ unsigned pack_bf16(float lo, float hi) {
    f32x2 v = {lo, hi};
    return __builtin_bit_cast(unsigned, __builtin_convertvector(v, bf16x2_t));
}
#define BAR() do { asm volatile("" ::: "memory"); __builtin_amdgcn_s_barrier(); asm volatile("" ::: "memory"); } while (0)
#define WAIT_V(n) asm volatile("s_waitcnt vmcnt(%0)" ::"n"(n) : "memory")
#define WAIT_L0() asm volatile("s_waitcnt lgkmcnt(0)" ::: "memory")
#define SB0() __builtin_amdgcn_sched_barrier(0)

__device__ __forceinline__ rsrc_t mk_rsrc(const void* p, unsigned bytes) { return __builtin_amdgcn_make_buffer_rsrc((void*)p, 0, bytes, 0x00020000); }
__device__ __forceinline__ void dma16(rsrc_t r, LAS char* lds, unsigned voff, unsigned soff) {
    __builtin_amdgcn_raw_ptr_buffer_load_lds(r, (lds_vp)lds, 16, voff, soff, 0, 0);
}
__device__ __forceinline__ void dma16_dev(rsrc_t r, LAS char* lds, unsigned voff, unsigned soff) {
    __builtin_amdgcn_raw_ptr_buffer_load_lds(r, (lds_vp)lds, 16, voff, soff, 0, 16);
}
__device__ __forceinline__ void touch_lines(rsrc_t r, LAS char* scratch, unsigned first, unsigned cnt, int lane) {
    __builtin_amdgcn_raw_ptr_buffer_load_lds(r, (lds_vp)scratch, 4, (unsigned)lane < cnt ? (first + lane) * 128u : 0x7ffffff0u, 0, 0, 0);
}
__device__ __forceinline__ void st128_dev(rsrc_t r, unsigned off, u32x4 v) { __builtin_amdgcn_raw_buffer_store_b128(v, r, off, 0, 16); }
__device__ __forceinline__ void st64_dev(rsrc_t r, unsigned off, u32x2 v) { __builtin_amdgcn_raw_buffer_store_b64(v, r, off, 0, 16); }
__device__ __forceinline__ bf16x8 lds_b128(LAS char* p) { return *(LAS bf16x8*)p; }
template <int CTRL> __device__ __forceinline__ float dpp_add(float x) {
    return x + __builtin_bit_cast(float, __builtin_amdgcn_update_dpp(0, __builtin_bit_cast(int, x), CTRL, 0xf, 0xf, false));
}
__device__ __forceinline__ f32x2 gelu2(f32x2 x) {
    const f32x2 t = x * (x * x * -0.1029432397f + -2.3022081498f);
    f32x2 e; e.x = __builtin_amdgcn_exp2f(t.x); e.y = __builtin_amdgcn_exp2f(t.y);
    const f32x2 d = e + 1.0f;
    f32x2 r; r.x = __builtin_amdgcn_rcpf(d.x); r.y = __builtin_amdgcn_rcpf(d.y);
    return x * r;
}
__device__ __forceinline__ f32x2 lo2(f32x4 v) { return (f32x2){v[0], v[1]}; }
__device__ __forceinline__ f32x2 hi2(f32x4 v) { return (f32x2){v[2], v[3]}; }
__device__ __forceinline__ unsigned pack2(f32x2 v) { return __builtin_bit_cast(unsigned, __builtin_convertvector(v, bf16x2_t)); }
__device__ __forceinline__ f32x2 unpack2(unsigned v) { return (f32x2){__uint_as_float(v << 16), __uint_as_float(v & 0xffff0000u)}; }
__device__ __forceinline__ int swap23(int r) { return (r & 0x13) | ((r & 4) << 1) | ((r & 8) >> 1); }
__device__ __forceinline__ float xchg32(float x) {
    auto r = __builtin_amdgcn_permlane32_swap(__float_as_uint(x), __float_as_uint(x), false, false);
    return __uint_as_float((threadIdx.x & 32) ? r[0] : r[1]);
}
__device__ __forceinline__ u32x4 gather8(unsigned x0, unsigned x1, unsigned y0, unsigned y1) {
    auto s0 = __builtin_amdgcn_permlane32_swap(x0, y0, false, false);
    auto s1 = __builtin_amdgcn_permlane32_swap(x1, y1, false, false);
    auto t0 = __builtin_amdgcn_permlane16_swap(s0[0], s0[1], false, false);
    auto t1 = __builtin_amdgcn_permlane16_swap(s1[0], s1[1], false, false);
    return (u32x4){t0[0], t1[0], t0[1], t1[1]};
}
__device__ __forceinline__ void publish(char* ctl, size_t word, u64 tag, int lane) {
    asm volatile("s_waitcnt vmcnt(0)" ::: "memory");
    if (lane == 0) __hip_atomic_store((GAS u64*)(ctl + word), tag, __ATOMIC_RELAXED, __HIP_MEMORY_SCOPE_AGENT);
}
__device__ __forceinline__ bool flags_ready(const char* f0, int n0, const char* f1, int n1, u64 tag, int lane) {
    u64 a = tag, b = tag;
    if (lane < n0) a = __hip_atomic_load((const GAS u64*)(f0 + lane * 8), __ATOMIC_RELAXED, __HIP_MEMORY_SCOPE_AGENT);
    if (lane < n1) b = __hip_atomic_load((const GAS u64*)(f1 + lane * 8), __ATOMIC_RELAXED, __HIP_MEMORY_SCOPE_AGENT);
    return __all(a == tag && b == tag);
}

struct Args { const u16* x; const u16* wqkv; const u16* wo; const u16* win; const u16* wsp; const u16* bsp; const u16* wout; u16* out; char* ws; };

struct RowsImg {
    unsigned v0, v1;
    __device__ __forceinline__ void init(int lane, unsigned rs) {
        const int r8 = lane >> 3, ch = lane & 7;
        v0 = r8 * rs + ((ch ^ ((lane >> 4) & 7)) << 4); v1 = r8 * rs + ((ch ^ ((4 + (lane >> 4)) & 7)) << 4);
    }
    __device__ __forceinline__ void issue(rsrc_t r, LAS char* dst, unsigned soff, unsigned rs) const {
#pragma unroll
        for (int q = 0; q < 4; ++q) dma16(r, dst + q * 1024, (q & 1) ? v1 : v0, soff + q * 8 * rs);
    }
    __device__ __forceinline__ void issue35(rsrc_t r, LAS char* dst, unsigned soff, unsigned rs, int p) const {
        const int q0 = p < 2 ? 3 * p : 6 + 5 * (p - 2);
        const unsigned va = (q0 & 1) ? v1 : v0, vb = (q0 & 1) ? v0 : v1;
#pragma unroll
        for (int q = 0; q < 3; ++q) dma16(r, dst + (q0 + q) * 1024, (q & 1) ? vb : va, soff + (q0 + q) * 8 * rs);
        if (p >= 2) {
#pragma unroll
            for (int q = 3; q < 5; ++q) dma16(r, dst + (q0 + q) * 1024, (q & 1) ? vb : va, soff + (q0 + q) * 8 * rs);
        }
    }
};
template <int NC> struct WImg {
    static constexpr int CPR = 2 * NC, BIG = (NC == 9) ? 5 : 4;
    unsigned v[BIG];
    static __device__ __forceinline__ int first(int p) { return NC == 9 ? (p < 2 ? 5 * p : 10 + 4 * (p - 2)) : (p < 2 ? 4 * p : 8 + 3 * (p - 2)); }
    __device__ __forceinline__ void init(int lane, int p, unsigned ldb) {
        const int I0 = first(p);
#pragma unroll
        for (int q = 0; q < BIG; ++q) {
            const int n = (I0 + q) * 64 + lane, pos = n / CPR, cc = n - pos * CPR;
            v[q] = ((pos & 32) + swap23(pos & 31)) * ldb + cc * 16;
        }
    }
    __device__ __forceinline__ void issue(rsrc_t r, LAS char* dst, unsigned soff, int p) const {
        const int I0 = first(p);
#pragma unroll
        for (int q = 0; q < BIG - 1; ++q) dma16(r, dst + (I0 + q) * 1024, v[q], soff);
        if (p < 2) dma16(r, dst + (I0 + BIG - 1) * 1024, v[BIG - 1], soff);
    }
};

#define MMA(acc, a, b) asm volatile("v_mfma_f32_16x16x32_bf16 %0, %1, %2, %0" : "+v"(acc) : "v"(a), "v"(b))
#define MMA_DRAIN() do { SB0(); asm volatile("s_nop 7\n\ts_nop 3"); SB0(); } while (0)
struct NoHook { __device__ __forceinline__ void operator()(int) const {} };
__device__ __forceinline__ s16x4 lds_tr1(LAS char* p) { return __builtin_amdgcn_ds_read_tr16_b64_v4i16((LAS s16x4*)p); }
__device__ __forceinline__ bf16x8 cat8(s16x4 a, s16x4 b) { return __builtin_shufflevector(a, b, 0, 1, 2, 3, 4, 5, 6, 7); }

template <int NC, bool XST> struct XWAcc { f32x4 a[2][NC]; f32x4 cs[3]; f32x4 sx[2], sq[2]; };
template <int NC> struct XF { bf16x8 x[2]; s16x4 w[NC][2]; };
struct XAddr { unsigned x, w, r[3]; };
template <int NC> __device__ __forceinline__ void xa_read1(XF<NC>& f, int i, const XAddr& a) {
    if (i < 2) f.x[i] = lds_b128((LAS char*)(size_t)a.x + i * 2048);
    else {
        const int c = (i - 2) >> 1;
        const unsigned q = c >= NC - 3 ? a.r[c >= NC - 3 ? c - (NC - 3) : 0] : a.w;
        f.w[c][i & 1] = lds_tr1((LAS char*)(size_t)q + c * 32 + (i & 1) * (8 * NC * 32));
    }
}
#define VADD(v, d) asm volatile("v_add_u32 %0, %1, %0" : "+v"(v) : "s"(d))
template <int NC, bool TR, bool XST, int DBL, bool LOAD, bool UPD>
__device__ __forceinline__ void xa_step(XWAcc<NC, XST>& A, const XF<NC>& f, XF<NC>& n, XAddr& ra, XAddr& ua, int d, const bf16x8& ones) {
    constexpr int R = 2 + 2 * NC, G = 2 * NC + (NC + 3) / 4 + (XST ? 4 : 0);
    int rd = 0, gap = 0;
#define XA_UPD(k) do { if ((k) == 0) VADD(ua.x, d); if ((k) == 1) VADD(ua.w, d); if ((k) == 2) VADD(ua.r[0], d); if ((k) == 3) VADD(ua.r[1], d); if ((k) == 4) VADD(ua.r[2], d); \
                       if ((k) == 5) VADD(ra.x, d); if ((k) == 6) VADD(ra.w, d); if ((k) == 7) VADD(ra.r[0], d); if ((k) == 8) VADD(ra.r[1], d); if ((k) == 9) VADD(ra.r[2], d); } while (0)
#define XA_GAP() do { if (LOAD) { if (rd < R) { xa_read1<NC>(n, rd, ra); ++rd; } if (gap < DBL && rd < R) { xa_read1<NC>(n, rd, ra); ++rd; } } \
                      if (UPD && gap >= G - 6 && gap < G - 1) { XA_UPD(2 * (gap - (G - 6))); XA_UPD(2 * (gap - (G - 6)) + 1); } ++gap; SB0(); } while (0)
#pragma unroll
    for (int c = 0; c < NC; ++c) {
        const bf16x8 w = cat8(f.w[c][0], f.w[c][1]);
#pragma unroll
        for (int t = 0; t < 2; ++t) {
            if (TR) MMA(A.a[t][c], w, f.x[t]); else MMA(A.a[t][c], f.x[t], w);
            XA_GAP();
        }
        if ((c & 3) == 0) {
            if (TR) MMA(A.cs[c >> 2], w, ones); else MMA(A.cs[c >> 2], ones, w);
            XA_GAP();
        }
    }
    if (XST) {
#pragma unroll
        for (int t = 0; t < 2; ++t) { MMA(A.sx[t], ones, f.x[t]); XA_GAP(); MMA(A.sq[t], f.x[t], f.x[t]); XA_GAP(); }
    }
    SB0();
#undef XA_GAP
#undef XA_UPD
}
template <int NC, bool XST> __device__ __forceinline__ void xw_zero(XWAcc<NC, XST>& A) {
    const f32x4 z = {0.f, 0.f, 0.f, 0.f};
#pragma unroll
    for (int t = 0; t < 2; ++t) {
#pragma unroll
        for (int c = 0; c < NC; ++c) { A.a[t][c] = z; asm volatile("" : "+v"(A.a[t][c])); }
        if (XST) { A.sx[t] = z; A.sq[t] = z; asm volatile("" : "+v"(A.sx[t]), "+v"(A.sq[t])); }
    }
    A.cs[0] = z; A.cs[1] = z; asm volatile("" : "+v"(A.cs[0]), "+v"(A.cs[1]));
    if (NC > 8) { A.cs[2] = z; asm volatile("" : "+v"(A.cs[2])); }
}
template <int NC, bool TR, bool XST, int NST, class Hook>
__device__ __forceinline__ void xw_gemm(XWAcc<NC, XST>& A, LAS char* lds, int g0, int m, int lane, Hook hook) {
    asm volatile("" : "+v"(lane));
    const int fr = lane & 15, g = lane >> 4, row = 32 * m + fr;
    constexpr int G = 2 * NC + (NC + 3) / 4 + (XST ? 4 : 0), R = 2 + 2 * NC;
    constexpr int DA = R > G - 7 ? R - (G - 7) : 0, DB = R > G ? R - G : 0;
    const unsigned lb = (unsigned)(size_t)lds + (g0 & 3) * SLOT;
    XAddr a0, a1;
    a0.x = lb + row * 128 + ((g ^ ((row >> 1) & 7)) << 4); a1.x = lb + ((row * 128 + ((g ^ ((row >> 1) & 7)) << 4)) ^ 64);
    a0.w = lb + 16384 + (16 * (g >> 1) + 4 * (g & 1) + (fr >> 2)) * (NC * 32) + (lane & 3) * 8 + m * 32; a1.w = a0.w + 32 * NC * 32;
#pragma unroll
    for (int k = 0; k < 3; ++k) { const int o = (NC - 3 + k + m >= NC) ? -NC * 32 : 0; a0.r[k] = a0.w + o; a1.r[k] = a1.w + o; }
    asm volatile("" : "+v"(a0.x), "+v"(a0.w), "+v"(a0.r[0]), "+v"(a0.r[1]), "+v"(a0.r[2]), "+v"(a1.x), "+v"(a1.w), "+v"(a1.r[0]), "+v"(a1.r[1]), "+v"(a1.r[2]));
    XF<NC> f0, f1;
    bf16x8 ones = {0x3F80, 0x3F80, 0x3F80, 0x3F80, 0x3F80, 0x3F80, 0x3F80, 0x3F80};
    asm volatile("" : "+v"(ones));
#pragma unroll
    for (int i = 0; i < R; ++i) xa_read1<NC>(f0, i, a0);
    int d = ((g0 & 3) == 3) ? -3 * SLOT : SLOT, pos = g0 & 3;
    asm volatile("s_nop 1"); SB0();
#pragma unroll 1
    for (int s = 0; s < NST - 1; ++s) {
        xa_step<NC, TR, XST, DA, true, true>(A, f0, f1, a1, a0, d, ones);
        pos = (pos + 1) & 3; d = pos == 3 ? -3 * SLOT : SLOT;
        WAIT_L0(); BAR(); SB0();
        hook(s);
        xa_step<NC, TR, XST, DB, true, false>(A, f1, f0, a0, a0, 0, ones);
    }
    xa_step<NC, TR, XST, DA, true, false>(A, f0, f1, a1, a1, 0, ones);
    WAIT_L0(); BAR(); SB0();
    xa_step<NC, TR, XST, 0, false, false>(A, f1, f0, a0, a0, 0, ones);
    MMA_DRAIN();
}
template <int NC, bool TR, bool XST> __device__ __forceinline__ void share_colsum(const XWAcc<NC, XST>& A, LAS char* lds, int m, int lane) {
    LAS float* tab = (LAS float*)(lds + SP_CS);
    const int fr = lane & 15, g = lane >> 4;
#pragma unroll
    for (int k = 0; k < (NC + 3) / 4; ++k) {
        int c = m + 4 * k; c = c >= NC ? c - NC : c;
        if (TR) { if (fr == 0) *(LAS f32x4*)(tab + c * 16 + 4 * g) = A.cs[k]; }
        else { if (g == 0) tab[c * 16 + fr] = A.cs[k][0]; }
    }
    WAIT_L0(); BAR();
}
template <int NC> __device__ __forceinline__ void unrotate(unsigned (&v)[NC][2], int m) {
    unsigned u[NC][2];
#define UNROT(M) _Pragma("unroll") for (int c = 0; c < NC; ++c) { u[(c + M) % NC][0] = v[c][0]; u[(c + M) % NC][1] = v[c][1]; }
    if (m == 0) { UNROT(0) } else if (m == 1) { UNROT(1) } else if (m == 2) { UNROT(2) } else { UNROT(3) }
#undef UNROT
#pragma unroll
    for (int c = 0; c < NC; ++c) { v[c][0] = u[c][0]; v[c][1] = u[c][1]; asm volatile("" : "+v"(v[c][0]), "+v"(v[c][1])); }
}

struct MFrag { bf16x8 w[2]; bf16x8 z[9]; };
template <bool LOAD> __device__ __forceinline__ void m_step(f32x4 (&acc)[2][9], const MFrag& f, MFrag& n, LAS char* pW, LAS char* pZ) {
#pragma unroll
    for (int i = 0; i < 18; ++i) {
        MMA(acc[i & 1][i >> 1], f.z[i >> 1], f.w[i & 1]);
        if (LOAD) { if (i < 2) n.w[i] = lds_b128(pW + i * 2048); else if (i < 11) n.z[i - 2] = lds_b128(pZ + (i - 2) * 1024); }
        SB0();
    }
}
__device__ __forceinline__ void main_gemm(f32x4 (&acc)[2][9], LAS char* lds, int g0, int m, int lane) {
    asm volatile("" : "+v"(lane));
    const int fr = lane & 15, g = lane >> 4, row = 32 * m + fr;
    const unsigned oW = row * 128 + ((g ^ ((row >> 1) & 7)) << 4);
    LAS char* pW0[4]; LAS char* pW1[4]; LAS char* pZ[4];
#pragma unroll
    for (int k = 0; k < 4; ++k) {
        const unsigned sb = ((g0 + k) & 3) * SLOT;
        pW0[k] = lds + sb + oW; pW1[k] = lds + sb + (oW ^ 64); pZ[k] = lds + sb + 16384 + lane * 16;
        asm volatile("" : "+v"(pW0[k]), "+v"(pW1[k]), "+v"(pZ[k]));
    }
#pragma unroll
    for (int t = 0; t < 2; ++t)
#pragma unroll
        for (int c = 0; c < 9; ++c) { acc[t][c] = (f32x4){0.f, 0.f, 0.f, 0.f}; asm volatile("" : "+v"(acc[t][c])); }
    MFrag f0, f1;
#pragma unroll
    for (int i = 0; i < 2; ++i) f0.w[i] = lds_b128(pW0[0] + i * 2048);
#pragma unroll
    for (int c = 0; c < 9; ++c) f0.z[c] = lds_b128(pZ[0] + c * 1024);
    asm volatile("s_nop 1"); SB0();
#define M_STAGE(K, KN) do { m_step<true>(acc, f0, f1, pW1[K], pZ[K] + 9216); WAIT_L0(); BAR(); SB0(); m_step<true>(acc, f1, f0, pW0[KN], pZ[KN]); } while (0)
#pragma unroll 1
    for (int s = 0; s < 7; ++s) { M_STAGE(0, 1); M_STAGE(1, 2); M_STAGE(2, 3); M_STAGE(3, 0); }
    M_STAGE(0, 1); M_STAGE(1, 2); M_STAGE(2, 3);
#undef M_STAGE
    m_step<true>(acc, f0, f1, pW1[3], pZ[3] + 9216);
    WAIT_L0(); BAR(); SB0();
    m_step<false>(acc, f1, f0, pW0[0], pZ[0]);
    MMA_DRAIN();
}

struct EFrag { s16x4 w[2]; bf16x8 b[5]; };
template <bool LOAD, bool MM> __device__ __forceinline__ void e_step(f32x4 (&acc)[5], const EFrag& f, EFrag& n, LAS char* pW, LAS char* pB) {
    const bf16x8 w = cat8(f.w[0], f.w[1]);
#pragma unroll
    for (int t = 0; t < 5; ++t) {
        if (MM) MMA(acc[t], w, f.b[t]);
        if (LOAD) {
            if (t == 0) { n.w[0] = lds_tr1(pW); n.w[1] = lds_tr1(pW + 2048); }
            else if (t == 1) { n.b[0] = lds_b128(pB); n.b[1] = lds_b128(pB + 1024); }
            else n.b[t] = lds_b128(pB + t * 1024);
        }
        SB0();
    }
}
template <int NST, int NKS, bool MORE> __device__ __forceinline__ void e_gemm(f32x4 (&acc)[5], LAS char* lds, int g0, int w, int lane) {
    asm volatile("" : "+v"(lane));
    const int g = lane >> 4, q = (lane & 15) >> 2;
    LAS char* pW = lds + (4 * g + q) * 128 + ((w ^ ((2 * g + (q >> 1)) & 3)) << 5) + (lane & 3) * 8;
    LAS char* pB = lds + 16384 + lane * 16;
#pragma unroll
    for (int t = 0; t < 5; ++t) { acc[t] = (f32x4){0.f, 0.f, 0.f, 0.f}; asm volatile("" : "+v"(acc[t])); }
    EFrag f0, f1;
    e_step<true, false>(acc, f1, f0, pW + (g0 & 3) * SLOT, pB + (g0 & 3) * SLOT);
    asm volatile("s_nop 1"); SB0();
#pragma unroll 1
    for (int s = 0; s < NST - 1; ++s) {
        const unsigned sb = ((g0 + s) & 3) * SLOT, sn = ((g0 + s + 1) & 3) * SLOT;
        e_step<true, true>(acc, f0, f1, pW + sb + 4096, pB + sb + 5120);
        e_step<true, true>(acc, f1, f0, pW + sb + 8192, pB + sb + 10240);
        e_step<true, true>(acc, f0, f1, pW + sb + 12288, pB + sb + 15360);
        WAIT_L0(); BAR(); SB0();
        e_step<true, true>(acc, f1, f0, pW + sn, pB + sn);
    }
    const unsigned sb = ((g0 + NST - 1) & 3) * SLOT;
    constexpr int LAST = NKS - 4 * (NST - 1);
    e_step<true, true>(acc, f0, f1, pW + sb + 4096, pB + sb + 5120);
    if (LAST == 4) {
        e_step<true, true>(acc, f1, f0, pW + sb + 8192, pB + sb + 10240);
        e_step<true, true>(acc, f0, f1, pW + sb + 12288, pB + sb + 15360);
    }
    if (MORE) { WAIT_L0(); BAR(); SB0(); }
    e_step<false, true>(acc, f1, f0, pW, pB);
    MMA_DRAIN();
}

__device__ __forceinline__ float dot2(unsigned a, unsigned b, float c) { return __builtin_amdgcn_fdot2_f32_bf16(__builtin_bit_cast(bf16x2_t, a), __builtin_bit_cast(bf16x2_t, b), c, false); }
__device__ __forceinline__ unsigned at_region(int r, int p) { return (unsigned)(((r >> 1) ? 0 : 3 * SLOT) + ((r & 1) ? 16384 + p * 5120 : p * 4096)); }
__device__ __forceinline__ void attn_issue(rsrc_t rQ, LAS char* lds, int n, int p, int lane, int half) {
#pragma unroll
    for (int rr = 0; rr < 2; ++rr)
#pragma unroll
        for (int q = 0; q < 4; ++q) {
            const int r = 2 * half + rr, c = q * 64 + lane, lr = c / 18, cc = c - lr * 18, rho = r * 14 + lr;
            int t, seg;
            if (rho < 22) { t = 8 * n - 14 + rho; seg = 1; } else if (rho < 44) { t = 8 * n - 14 + (rho - 22); seg = 2; } else { t = 8 * n + (rho - 44); seg = 0; }
            const unsigned voff = (t < 0 || rho >= 52 || lr >= 14) ? 0x7ffffff0u : (unsigned)(t * (QW * 2) + (seg * D_ + 144 * p) * 2 + cc * 16);
            dma16_dev(rQ, lds + at_region(r, p) + q * 1024, voff, 0);
        }
}
__device__ __forceinline__ void attn_compute(LAS char* lds, rsrc_t rO, int n, int p, int lane) {
    const int i = lane >> 3, hh = lane & 7, t = 8 * n + i;
    const unsigned hb = hh * 36, r0 = at_region(0, p) + hb, r1 = at_region(1, p) + hb - 14 * 288, r2 = at_region(2, p) + hb - 28 * 288, r3 = at_region(3, p) + hb - 42 * 288;
    unsigned qv[9];
    {
        LAS unsigned* q = (LAS unsigned*)(lds + r3 + (44 + i) * 288);
#pragma unroll
        for (int d = 0; d < 9; ++d) qv[d] = q[d];
    }
    float lg[8];
#pragma unroll
    for (int w = 0; w < 8; ++w) {
        const int rho = 14 + i - 2 * w;
        LAS unsigned* kp = (LAS unsigned*)(lds + (rho < 14 ? r0 : r1) + rho * 288);
        float s = 0.f;
#pragma unroll
        for (int d = 0; d < 9; ++d) s = dot2(qv[d], kp[d], s);
        lg[w] = (t - 2 * w >= 0) ? s * (0.23570226039551584f * 1.4426950408889634f) : -1e9f;
    }
    float mx = fmaxf(fmaxf(fmaxf(lg[0], lg[1]), fmaxf(lg[2], lg[3])), fmaxf(fmaxf(lg[4], lg[5]), fmaxf(lg[6], lg[7])));
    float sum = 0.f;
#pragma unroll
    for (int w = 0; w < 8; ++w) { lg[w] = __builtin_amdgcn_exp2f(lg[w] - mx); sum += lg[w]; }
    const float inv = __builtin_amdgcn_rcpf(sum);
    unsigned pp[4];
#pragma unroll
    for (int w = 0; w < 4; ++w) pp[w] = pack_bf16(lg[2 * w] * inv, lg[2 * w + 1] * inv);
    float acc[18];
#pragma unroll
    for (int d = 0; d < 18; ++d) acc[d] = 0.f;
#pragma unroll
    for (int w = 0; w < 4; ++w) {
        const int ra = 22 + 14 + i - 4 * w, rb = ra - 2;
        LAS unsigned* va = (LAS unsigned*)(lds + (ra < 28 ? r1 : ra < 42 ? r2 : r3) + ra * 288);
        LAS unsigned* vb = (LAS unsigned*)(lds + (rb < 28 ? r1 : rb < 42 ? r2 : r3) + rb * 288);
#pragma unroll
        for (int d = 0; d < 9; ++d) {
            const unsigned x = va[d], y = vb[d];
            acc[2 * d] = dot2(__builtin_amdgcn_perm(y, x, 0x05040100u), pp[w], acc[2 * d]);
            acc[2 * d + 1] = dot2(__builtin_amdgcn_perm(y, x, 0x07060302u), pp[w], acc[2 * d + 1]);
        }
    }
    LAS char* ob = lds + SP_AO + p * 2304;
#pragma unroll
    for (int d = 0; d < 9; ++d) *(LAS unsigned*)(ob + i * 288 + hh * 36 + d * 4) = pack_bf16(acc[2 * d], acc[2 * d + 1]);
    WAIT_L0();
    {
        const int r = lane & 7, g = (lane >> 3) & 3, tt = n >> 1, rl = (8 * n + r) & 15;
#pragma unroll
        for (int q = 0; q < 2; ++q) {
            const int a = (lane >> 5) + 2 * q;
            const u32x2 lo = *(LAS u32x2*)(ob + r * 288 + (32 * a + 4 * g) * 2), hi = *(LAS u32x2*)(ob + r * 288 + (32 * a + 16 + 4 * g) * 2);
            st128_dev(rO, ((tt * 18 + 4 * p + a) * 64 + rl + 16 * g) * 16, (u32x4){lo.x, lo.y, hi.x, hi.y});
        }
        const u32x2 v = *(LAS u32x2*)(ob + r * 288 + (128 + 4 * g) * 2);
        if (lane < 32) st64_dev(rO, ((tt * 18 + 16 + (p >> 1)) * 64 + rl + 16 * g) * 16 + (p & 1) * 8, v);
    }
}

__global__ __launch_bounds__(512, 2) void fused_kernel(Args a) {
    asm volatile("fusedk_pf_begin:");
    extern __shared__ __attribute__((aligned(16))) char smem[];
    LAS char* lds = (LAS char*)smem;
    const int wid = __builtin_amdgcn_readfirstlane(threadIdx.x >> 6), lane = threadIdx.x & 63;
    const int xcd = blockIdx.x & 7, loc = blockIdx.x >> 3;
    unsigned pf_dummy = 0;
    if (wid == 0) {
        u64 base; unsigned sz, back;
        asm volatile("s_getpc_b64 %0\nfusedk_pf_here:\n\ts_mov_b32 %1, fusedk_pf_end-fusedk_pf_begin\n\ts_mov_b32 %2, fusedk_pf_here-fusedk_pf_begin" : "=s"(base), "=s"(sz), "=s"(back));
        base -= back;
        const unsigned off = (unsigned)(loc * 13 + lane) * 128u;
        if (lane < 13 && (int)off < (int)sz) asm volatile("global_load_dword %0, %1, off" : "=v"(pf_dummy) : "v"(base + off));
    }
    const int ti = (xcd & 1) * 8 + (loc & 7), tj = (xcd >> 1) * 4 + (loc >> 3);
    const int nt = xcd * 30 + loc;
    const bool fin = loc < 30 && nt < 234;
    const int rb = nt / 9, cb = nt - rb * 9;
    {
        RowsImg xi0; xi0.init(lane, D_ * 2);
        const rsrc_t rx = mk_rsrc(a.x, S_ * D_ * 2);
        const unsigned so = (unsigned)(ti * 128 + 16 * wid) * (D_ * 2);
        dma16(rx, lds + wid * 2048, xi0.v0, so); dma16(rx, lds + wid * 2048 + 1024, xi0.v1, so + 8 * (D_ * 2));
        dma16(rx, lds + SLOT + wid * 2048, xi0.v0, so + 128); dma16(rx, lds + SLOT + wid * 2048 + 1024, xi0.v1, so + 8 * (D_ * 2) + 128);
    }
    char* ctl = a.ws + WS_CTL;
    if (lane < 2) __builtin_amdgcn_raw_ptr_buffer_load_lds(mk_rsrc(ctl, 4096), (lds_vp)(lds + SP_EP + wid * 64), 4, lane * 4 + (unsigned)CTL_EP, 0, 0, 16);
#define TAG_FROM_EPOCH() \
    u64 ep_; asm volatile("ds_read_b64 %0, %1\n\ts_waitcnt lgkmcnt(0)" : "=&v"(ep_) : "v"((unsigned)(size_t)(lds + SP_EP + wid * 64)) : "memory"); \
    const u64 tg_ = ep_ * 0x9E3779B97F4A7C15ull + 0xD1B54A32D192ED03ull; \
    const u64 tag = ((u64)(unsigned)__builtin_amdgcn_readfirstlane((unsigned)(tg_ >> 32)) << 32) | (unsigned)__builtin_amdgcn_readfirstlane((unsigned)tg_)
    const int an = ti * 16 + tj;

    if (__builtin_expect(wid < 4, 0)) {
    const int m = wid, fr = lane & 15, g4 = lane >> 4;
    const rsrc_t rQs = mk_rsrc(a.ws + WS_QKV, S_ * QW * 2), rZs = mk_rsrc(a.ws + WS_Z2T, S_ * FH * 2), rHs = mk_rsrc(a.ws + WS_HF, S_ * FH * 2), rSt = mk_rsrc(a.ws + WS_ST, S_ * 16 * 16);
    LAS float* cst = (LAS float*)(lds + SP_CS);
    float xm[2], xr[2];
    unsigned pf1_dummy = 0;
    {
        const unsigned W = blockIdx.x * 4 + m;
        const bool isx = lane < 18;
        const unsigned ln = isx ? W * 18 + lane : W * 16 + (lane - 18);
        const char* src = isx ? (const char*)a.x : (const char*)a.wqkv;
        const unsigned lines = isx ? S_ * D_ * 2 / 128 : D_ * QW * 2 / 128;
        if (lane < 34 && ln < lines) asm volatile("global_load_dword %0, %1, off" : "=v"(pf1_dummy) : "v"(src + (size_t)ln * 128));
    }
    asm volatile("s_waitcnt vmcnt(1)" : "+v"(pf_dummy) :: "memory");
    TAG_FROM_EPOCH();
    BAR();
    {
        XWAcc<7, true> A; xw_zero(A);
        xw_gemm<7, true, true, 9>(A, lds, G_QKV, m, lane, NoHook());
        asm volatile("s_waitcnt vmcnt(0)" : "+v"(pf1_dummy) :: "memory");
        share_colsum<7, true, true>(A, lds, m, lane);
#pragma unroll
        for (int t = 0; t < 2; ++t) {
            const int r3 = fr & 3;
            const float dq = r3 == 0 ? A.sq[t][0] : r3 == 1 ? A.sq[t][1] : r3 == 2 ? A.sq[t][2] : A.sq[t][3];
            const float sq = __shfl(dq, (fr >> 2) * 16 + fr);
            xm[t] = A.sx[t][0] * (1.0f / D_);
            xr[t] = __builtin_amdgcn_rsqf(fmaxf(sq * (1.0f / D_) - xm[t] * xm[t], 0.f) + 1e-5f);
        }
        const int c0 = (tj * 27) >> 2, dup = tj > 0 && c0 < (((tj - 1) * 27) >> 2) + 7;
#pragma unroll
        for (int t = 0; t < 2; ++t) {
            unsigned pk[7][2];
#pragma unroll
            for (int c = 0; c < 7; ++c) {
                const int tile = c + m >= 7 ? c + m - 7 : c + m;
                const f32x4 cs = *(LAS f32x4*)(cst + tile * 16 + 4 * g4);
                pk[c][0] = pack2((lo2(A.a[t][c]) - lo2(cs) * xm[t]) * xr[t]); pk[c][1] = pack2((hi2(A.a[t][c]) - hi2(cs) * xm[t]) * xr[t]);
            }
            unrotate<7>(pk, m);
            const unsigned ro = (unsigned)(ti * 128 + 32 * m + 16 * t + fr) * (QW * 2) + c0 * 32;
#pragma unroll
            for (int pp = 0; pp < 3; ++pp) st128_dev(rQs, ro + (1 + 2 * pp) * 32 + g4 * 16, gather8(pk[1 + 2 * pp][0], pk[1 + 2 * pp][1], pk[2 + 2 * pp][0], pk[2 + 2 * pp][1]));
            if (!dup) st64_dev(rQs, ro + g4 * 8, (u32x2){pk[0][0], pk[0][1]});
        }
    }
    unsigned zp[2][9][2];
    {
        XWAcc<9, false> A; xw_zero(A);
        xw_gemm<9, false, false, 9>(A, lds, G_Z2, m, lane, [&](int s) { if (s == 4) publish(ctl, CTL_QF + ((ti * 16 + tj) * 4 + m) * 8, tag, lane); });
        share_colsum<9, false, false>(A, lds, m, lane);
        f32x2 rm[2][2], rr[2][2];
#pragma unroll
        for (int t = 0; t < 2; ++t)
#pragma unroll
            for (int h = 0; h < 2; ++h) {
                rm[t][h] = (f32x2){__shfl(xm[t], 4 * g4 + 2 * h), __shfl(xm[t], 4 * g4 + 2 * h + 1)};
                rr[t][h] = (f32x2){__shfl(xr[t], 4 * g4 + 2 * h), __shfl(xr[t], 4 * g4 + 2 * h + 1)};
            }
        f32x2 s1[2][2], s2[2][2];
#pragma unroll
        for (int t = 0; t < 2; ++t)
#pragma unroll
            for (int h = 0; h < 2; ++h) { s1[t][h] = (f32x2){0.f, 0.f}; s2[t][h] = (f32x2){0.f, 0.f}; }
#pragma unroll
        for (int c = 0; c < 9; ++c) {
            const float cs = cst[(c + m >= 9 ? c + m - 9 : c + m) * 16 + fr];
#pragma unroll
            for (int t = 0; t < 2; ++t)
#pragma unroll
                for (int h = 0; h < 2; ++h) {
                    const f32x2 v = gelu2(((h ? hi2(A.a[t][c]) : lo2(A.a[t][c])) - rm[t][h] * cs) * rr[t][h]);
                    s1[t][h] += v; s2[t][h] += v * v;
                    zp[t][c][h] = pack2(v);
                    asm volatile("" : "+v"(zp[t][c][h]));
                }
        }
        float mm = 0.f, qq = 0.f;
        {
            const int idx = lane & 7;
            float a1[8], a2[8];
#pragma unroll
            for (int k = 0; k < 8; ++k) { a1[k] = s1[k >> 2][(k >> 1) & 1][k & 1]; a2[k] = s2[k >> 2][(k >> 1) & 1][k & 1]; }
#pragma unroll
            for (int k = 0; k < 8; ++k) { a1[k] = dpp_add<0x128>(a1[k]); a2[k] = dpp_add<0x128>(a2[k]); }
#pragma unroll
            for (int k = 0; k < 8; ++k) { a1[k] = dpp_add<0x124>(a1[k]); a2[k] = dpp_add<0x124>(a2[k]); }
#pragma unroll
            for (int k = 0; k < 8; ++k) { a1[k] = dpp_add<0x122>(a1[k]); a2[k] = dpp_add<0x122>(a2[k]); }
#pragma unroll
            for (int k = 0; k < 8; ++k) { a1[k] = dpp_add<0x121>(a1[k]); a2[k] = dpp_add<0x121>(a2[k]); }
            float u1 = a1[0], u2 = a2[0];
#pragma unroll
            for (int k = 1; k < 8; ++k) { u1 = idx == k ? a1[k] : u1; u2 = idx == k ? a2[k] : u2; }
            mm = u1 * (1.0f / 144.0f);
            qq = fmaxf(u2 - u1 * mm, 0.f);
            if (fr < 8) st128_dev(rSt, ((ti * 16 + tj) * 128 + 32 * m + 16 * (idx >> 2) + 4 * g4 + (idx & 3)) * 16,
                                  (u32x4){__float_as_uint(mm), (unsigned)tag, __float_as_uint(qq), (unsigned)(tag >> 32)});
        }
    }
    unsigned z1p[2][9][2];
    {
        XWAcc<9, false> A; xw_zero(A);
        xw_gemm<9, true, false, Z1A>(A, lds, G_Z1, m, lane, NoHook());
        float mu, rs;
        {
            const unsigned off = ((ti * 16 + (lane >> 5) * 8) * 128 + 32 * m + (lane & 31)) * 16;
            u32x4 v[8]; unsigned spins = 0;
            for (;;) {
#pragma unroll
                for (int k = 0; k < 8; ++k) v[k] = __builtin_amdgcn_raw_buffer_load_b128(rSt, off + k * 2048, 0, 16);
                unsigned bad = 0;
#pragma unroll
                for (int k = 0; k < 8; ++k) bad |= (v[k].y ^ (unsigned)tag) | (v[k].w ^ (unsigned)(tag >> 32));
                if (__all(bad == 0) || ++spins > SPIN_CAP) break;
                __builtin_amdgcn_s_sleep(2);
            }
            float s = 0.f;
#pragma unroll
            for (int k = 0; k < 8; ++k) s += __uint_as_float(v[k].x);
            s += xchg32(s);
            mu = s * (1.0f / 16.0f);
            float q = 0.f;
#pragma unroll
            for (int k = 0; k < 8; ++k) { const float d = __uint_as_float(v[k].x) - mu; q += __uint_as_float(v[k].z) + 144.0f * d * d; }
            q += xchg32(q);
            rs = __builtin_amdgcn_rsqf(q * (1.0f / FH) + 1e-5f);
        }
        f32x2 mr[2][2], rr[2][2];
#pragma unroll
        for (int t = 0; t < 2; ++t)
#pragma unroll
            for (int h = 0; h < 2; ++h) {
                const int src = 16 * t + 4 * g4 + 2 * h;
                mr[t][h] = (f32x2){__shfl(mu, src), __shfl(mu, src + 1)}; rr[t][h] = (f32x2){__shfl(rs, src), __shfl(rs, src + 1)};
            }
        const unsigned zo = (unsigned)((tj * 64 + ti * 4 + m) * 9) * 1024 + lane * 16;
#pragma unroll
        for (int c = 0; c < 9; ++c) {
            const unsigned x0 = pack2((unpack2(zp[0][c][0]) - mr[0][0]) * rr[0][0]), x1 = pack2((unpack2(zp[0][c][1]) - mr[0][1]) * rr[0][1]);
            const unsigned y0 = pack2((unpack2(zp[1][c][0]) - mr[1][0]) * rr[1][0]), y1 = pack2((unpack2(zp[1][c][1]) - mr[1][1]) * rr[1][1]);
            st128_dev(rZs, zo + (c + m >= 9 ? c + m - 9 : c + m) * 1024, gather8(x0, x1, y0, y1));
        }
        xw_gemm<9, true, false, 9 - Z1A>(A, lds, G_Z1 + Z1A, m, lane, [&](int s) { if (s == 1) publish(ctl, CTL_ZF + ((tj * 16 + ti) * 4 + m) * 8, tag, lane); });
        share_colsum<9, true, false>(A, lds, m, lane);
#pragma unroll
        for (int c = 0; c < 9; ++c) {
            const f32x4 cs = *(LAS f32x4*)(cst + (c + m >= 9 ? c + m - 9 : c + m) * 16 + 4 * g4);
#pragma unroll
            for (int t = 0; t < 2; ++t) {
                z1p[t][c][0] = pack2(gelu2((lo2(A.a[t][c]) - lo2(cs) * xm[t]) * xr[t])); z1p[t][c][1] = pack2(gelu2((hi2(A.a[t][c]) - hi2(cs) * xm[t]) * xr[t]));
                asm volatile("" : "+v"(z1p[t][c][0]), "+v"(z1p[t][c][1]));
            }
        }
        unrotate<9>(z1p[0], m); unrotate<9>(z1p[1], m);
    }
    BAR();
    {
        f32x4 acc[2][9];
        float bt[2];
#pragma unroll
        for (int t = 0; t < 2; ++t) bt[t] = bf2f(a.bsp[ti * 128 + 32 * m + 16 * t + fr]);
        main_gemm(acc, lds, G_MAIN, m, lane);
#pragma unroll
        for (int t = 0; t < 2; ++t) {
            const unsigned hp = (unsigned)(ti * 8 + 2 * m + t) * 72 * 1024 + lane * 16;
#pragma unroll
            for (int pp = 0; pp < 4; ++pp) {
                u32x4 o;
                o.x = pack2((lo2(acc[t][2 * pp]) + bt[t]) * unpack2(z1p[t][2 * pp][0])); o.y = pack2((hi2(acc[t][2 * pp]) + bt[t]) * unpack2(z1p[t][2 * pp][1]));
                o.z = pack2((lo2(acc[t][2 * pp + 1]) + bt[t]) * unpack2(z1p[t][2 * pp + 1][0])); o.w = pack2((hi2(acc[t][2 * pp + 1]) + bt[t]) * unpack2(z1p[t][2 * pp + 1][1]));
                st128_dev(rHs, hp + (tj * 4 + pp) * 1024, o);
            }
            u32x2 o;
            o.x = pack2((lo2(acc[t][8]) + bt[t]) * unpack2(z1p[t][8][0])); o.y = pack2((hi2(acc[t][8]) + bt[t]) * unpack2(z1p[t][8][1]));
            st64_dev(rHs, hp + (64 + (tj >> 1)) * 1024 + (tj & 1) * 8, o);
        }
    }
    publish(ctl, CTL_HF + ((ti * 16 + tj) * 4 + m) * 8, tag, lane);
    if (!fin) return;
    {
        f32x4 sa[5], ga[5];
        u32x2 xv[5];
        const int fr = lane & 15, g4 = lane >> 4;
#pragma unroll
        for (int t = 0; t < 5; ++t) xv[t] = *(const u32x2*)(a.x + (size_t)(min(rb * 5 + t, 127) * 16 + fr) * D_ + cb * 64 + m * 16 + g4 * 4);
        BAR();
        e_gemm<18, 72, true>(ga, lds, G_G, m, lane);
        e_gemm<5, 18, false>(sa, lds, G_S, m, lane);
#pragma unroll
        for (int t = 0; t < 5; ++t) {
            if (rb * 5 + t < 128) {
                float o[4];
#pragma unroll
                for (int r = 0; r < 4; ++r) {
                    const float gv = ga[t][r], sg = gv * __builtin_amdgcn_rcpf(1.0f + __builtin_amdgcn_exp2f(gv * -1.4426950408889634f));
                    const float xx = (r & 1) ? bfhi(xv[t][r >> 1]) : bflo(xv[t][r >> 1]);
                    o[r] = xx + sa[t][r] * sg;
                }
                u32x2 ov; ov.x = pack_bf16(o[0], o[1]); ov.y = pack_bf16(o[2], o[3]);
                *(u32x2*)(a.out + (size_t)((rb * 5 + t) * 16 + fr) * D_ + cb * 64 + m * 16 + g4 * 4) = ov;
            }
        }
    }
    if (blockIdx.x == 0 && threadIdx.x == 0) __hip_atomic_store((GAS u64*)(ctl + CTL_EP), tag, __ATOMIC_RELAXED, __HIP_MEMORY_SCOPE_AGENT);
    asm volatile("fusedk_pf_end:");
        return;
    }
    {

        const int p = wid - 4;
    #define rX mk_rsrc(a.x, S_ * D_ * 2)
#define rQW mk_rsrc(a.wqkv, D_ * QW * 2)
#define rW mk_rsrc(a.win, D_ * F_ * 2)
#define rS mk_rsrc(a.wsp, S_ * S_ * 2)
#define rZ mk_rsrc(a.ws + WS_Z2T, S_ * FH * 2)
#define rQ mk_rsrc(a.ws + WS_QKV, S_ * QW * 2)
#define rO mk_rsrc(a.ws + WS_OF, S_ * D_ * 2)
        WImg<7> wq; wq.init(lane, p, QW * 2);
        const unsigned sQ = (unsigned)((tj * 27) >> 2) * 32;
        wq.issue(rQW, lds + 16384, sQ, p); wq.issue(rQW, lds + SLOT + 16384, sQ + 64 * (QW * 2), p);
        SB0();
        RowsImg xi; xi.init(lane, D_ * 2);
        RowsImg si; si.init(lane, S_ * 2);
        WImg<9> wz; wz.init(lane, p, F_ * 2);
        const unsigned sX0 = (unsigned)(ti * 128) * (D_ * 2), sZ1 = (unsigned)(tj * 144) * 2, sZ2 = sZ1 + FH * 2;
        const unsigned sS = (unsigned)(ti * 128 + 32 * p) * (S_ * 2), sZ = (unsigned)tj * (64u * 9216u);
        const unsigned vl = lane * 16;
        const unsigned vE = (lane >> 3) * (D_ * 2) + ((((lane & 7) >> 1) ^ ((lane >> 4) & 3)) << 5) + (lane & 1) * 16;
        const int zI0 = WImg<9>::first(p);
        auto iss_q = [&](int g) {
            LAS char* slot = lds + (g & 3) * SLOT;
            xi.issue35(rX, slot, sX0 + g * 128, D_ * 2, p); wq.issue(rQW, slot + 16384, sQ + g * 64 * (QW * 2), p);
        };
        auto iss_z = [&](int g, int s, unsigned col) {
            LAS char* slot = lds + (g & 3) * SLOT;
            xi.issue35(rX, slot, sX0 + s * 128, D_ * 2, p);
            wz.issue(rW, slot + 16384, col + s * 64 * (F_ * 2), p);
        };
        auto iss_m = [&](int g) {
            LAS char* slot = lds + (g & 3) * SLOT; const int s = g - G_MAIN;
            si.issue(rS, slot + p * 4096, sS + s * 128, S_ * 2);
#pragma unroll
            for (int q = 0; q < 4; ++q) dma16_dev(rZ, slot + 16384 + (zI0 + q) * 1024, vl, sZ + s * 18432 + (zI0 + q) * 1024);
            if (p < 2) dma16_dev(rZ, slot + 16384 + (zI0 + 4) * 1024, vl, sZ + s * 18432 + (zI0 + 4) * 1024);
        };
        unsigned eH[5], eO[5];
#pragma unroll
        for (int t = 0; t < 5; ++t) { const unsigned tt = (unsigned)__builtin_amdgcn_readfirstlane(min(rb * 5 + t, 127)); eH[t] = tt * 73728u + p * 1024u; eO[t] = tt * 18432u + p * 1024u; }
        auto iss_ew = [&](int g, rsrc_t rw, unsigned w0, unsigned w1) {
            LAS char* slot = lds + (g & 3) * SLOT;
            dma16(rw, slot + p * 4096, vE, w0); dma16(rw, slot + p * 4096 + 1024, vE, w0 + 8 * (D_ * 2));
            dma16(rw, slot + p * 4096 + 2048, vE, w1); dma16(rw, slot + p * 4096 + 3072, vE, w1 + 8 * (D_ * 2));
        };
        auto iss_eb = [&](int g, rsrc_t rb_, const unsigned (&eb)[5], unsigned bo) {
            LAS char* slot = lds + (g & 3) * SLOT;
#pragma unroll
            for (int t = 0; t < 5; ++t) dma16_dev(rb_, slot + 16384 + p * 5120 + t * 1024, vl, eb[t] + bo);
        };
        auto iss_e = [&](int g, rsrc_t rw, rsrc_t rb_, const unsigned (&eb)[5], unsigned w0, unsigned w1, unsigned bo) { iss_ew(g, rw, w0, w1); iss_eb(g, rb_, eb, bo); };
        constexpr unsigned TILE_B = 16 * D_ * 2;
        const unsigned wcol = cb * 128;
        auto iss_g = [&](int s) {
            const unsigned c0 = s < 16 ? 9 * s + 2 * p : 18 * (4 * (s - 16) + p) + 8, c1 = s < 16 ? c0 + 1 : c0 + 9;
            iss_e(G_G + s, mk_rsrc(a.wout, FH * D_ * 2), mk_rsrc(a.ws + WS_HF, S_ * FH * 2), eH, c0 * TILE_B + wcol, c1 * TILE_B + wcol, s * 4096);
        };
        auto iss_s = [&](int s) {
            const int pp = s == 4 && p > 1 ? 1 : p;
            const unsigned c0 = s < 4 ? 9 * s + 2 * p : 18 * pp + 8, c1 = s < 4 ? c0 + 1 : c0 + 9;
            iss_e(G_S + s, mk_rsrc(a.wo, D_ * D_ * 2), rO, eO, c0 * TILE_B + wcol, c1 * TILE_B + wcol, s * 4096 + (pp - p) * 1024);
        };
#define PSTEP(K) do { WAIT_V(7 * (K)); BAR(); } while (0)
#define PW(N) do { WAIT_V(N); BAR(); } while (0)
#define SPIN_UNTIL(cond) do { unsigned spins_ = 0; while (!(cond) && ++spins_ < SPIN_CAP) __builtin_amdgcn_s_sleep(4); asm volatile("" ::: "memory"); } while (0)
        iss_q(2); iss_q(3);
        touch_lines(rW, lds + SP_PF + wid * 256, (blockIdx.x * 4 + p) * 41, 41, lane);
        WAIT_V(20);
        TAG_FROM_EPOCH();
        WAIT_V(14); BAR();
#pragma unroll 1
        for (int g = 1; g <= 5; ++g) { PW(14); iss_q(g + 3); }
#pragma unroll 1
        for (int g = 6; g <= 7; ++g) { PSTEP(2); iss_z(g + 3, g + 3 - G_Z2, sZ2); }
#pragma unroll 1
        for (int g = 8; g <= 14; ++g) { PW(16); if (g == G_Z2) BAR(); iss_z(g + 3, g + 3 - G_Z2, sZ2); }
        PW(16); iss_z(18, 0, sZ1);
        PW(16); iss_z(19, 1, sZ1);
        PW(16); iss_z(20, 2, sZ1);
        PW(16);
        BAR();
        iss_z(21, 3, sZ1);
        {
            const unsigned W = blockIdx.x * 4 + p;
            touch_lines(rS, lds + SP_PF + wid * 256, W * 64, 64, lane);
            touch_lines(mk_rsrc(a.wout, FH * D_ * 2), lds + SP_PF + wid * 256, W * 21, 21, lane);
            touch_lines(mk_rsrc(a.wo, D_ * D_ * 2), lds + SP_PF + wid * 256, W * 6, 6, lane);
        }
#pragma unroll 1
        for (int g = 19; g <= 23; ++g) { PW(16); iss_z(g + 3, g + 3 - G_Z1, sZ1); }
        PSTEP(2); PSTEP(1); PSTEP(0);
        BAR(); BAR();
        SPIN_UNTIL(flags_ready(ctl + CTL_ZF + tj * 512, 64, ctl, 0, tag, lane));
        iss_m(27); iss_m(28); iss_m(29); iss_m(30);
        {
            const int pa = (8 * an - 14) >> 7;
            SPIN_UNTIL(flags_ready(ctl + CTL_QF + ti * 512, 64, ctl + CTL_QF + (pa < 0 ? ti : pa) * 512, 64, tag, lane));
        }
        BAR();
#pragma unroll 1
        for (int g = 28; g <= 55; ++g) { PW(16); iss_m(g + 3); }
        PSTEP(2); attn_issue(rQ, lds, an, p, lane, 0);
        PSTEP(1); attn_issue(rQ, lds, an, p, lane, 1);
        PSTEP(1);
        BAR();
        WAIT_V(0);
        attn_compute(lds, rO, an, p, lane);
        if (!fin) publish(ctl, CTL_OF + (an * 4 + p) * 8, tag, lane);
        else {
            {
                const int p0 = (rb * 80) >> 7, p1 = min(rb * 80 + 79, S_ - 1) >> 7;
                WAIT_V(0);
                if (lane == 0) __hip_atomic_store((GAS u64*)(ctl + CTL_OF + (an * 4 + p) * 8), tag, __ATOMIC_RELAXED, __HIP_MEMORY_SCOPE_AGENT);
#pragma unroll
                for (int s4 = 0; s4 < 4; ++s4) iss_ew(G_G + s4, mk_rsrc(a.wout, FH * D_ * 2), (9 * s4 + 2 * p) * TILE_B + wcol, (9 * s4 + 2 * p + 1) * TILE_B + wcol);
                SPIN_UNTIL(flags_ready(ctl + CTL_HF + p0 * 512, 64, ctl + CTL_HF + p1 * 512, 64, tag, lane));
                SPIN_UNTIL(flags_ready(ctl + CTL_OF + rb * 320, rb == 25 ? 24 : 40, ctl, 0, tag, lane));
            }
            {
                const rsrc_t rh = mk_rsrc(a.ws + WS_HF, S_ * FH * 2);
                iss_eb(G_G, rh, eH, 0); iss_eb(G_G + 1, rh, eH, 4096); iss_eb(G_G + 2, rh, eH, 8192); iss_eb(G_G + 3, rh, eH, 12288);
            }
            PSTEP(2);
            PW(10); iss_g(4);
            PW(14); iss_g(5);
            {
                const rsrc_t rw = mk_rsrc(a.wout, FH * D_ * 2), rh = mk_rsrc(a.ws + WS_HF, S_ * FH * 2);
                unsigned w0 = (54 + 2 * p) * TILE_B + wcol, bo = 6 * 4096;
#pragma unroll 1
                for (int g = 62; g <= 71; ++g) {
                    PW(18);
                    iss_e(g + 3, rw, rh, eH, w0, w0 + TILE_B, bo);
                    w0 += 9 * TILE_B; bo += 4096;
                }
            }
            PW(18); iss_g(16);
            PW(18); iss_g(17);
            PW(18); iss_s(0);
            PW(18); iss_s(1);
            PW(18); iss_s(2);
            PW(18); iss_s(3);
            PW(18); iss_s(4);
            PSTEP(2); PSTEP(1); PSTEP(0);
        }
        return;
        }
}

extern "C" void kernel_launch(void* const* d_in, const int* in_sizes, int n_in, void* d_out, int out_size, void* d_ws, size_t ws_size, hipStream_t stream) {
    Args a;
    a.x = (const u16*)d_in[0]; a.wqkv = (const u16*)d_in[1]; a.wo = (const u16*)d_in[2]; a.win = (const u16*)d_in[4]; a.wsp = (const u16*)d_in[5];
    a.bsp = (const u16*)d_in[6]; a.wout = (const u16*)d_in[7]; a.out = (u16*)d_out; a.ws = (char*)d_ws;
    (void)hipFuncSetAttribute((const void*)fused_kernel, hipFuncAttributeMaxDynamicSharedMemorySize, LDS_BYTES);
    fused_kernel<<<256, 512, LDS_BYTES, stream>>>(a);
}
```
